```python
import torch

def _softmax(x, axis=-1):
    return torch.softmax(x, dim=axis)
def _gelu(x):
    return torch.nn.functional.gelu(x, approximate="tanh")
def _silu(x):
    return torch.nn.functional.silu(x)
def _sigmoid(x):
    return torch.sigmoid(x)
def _layernorm(x, axis=-1, eps=1e-5):
    m = x.mean(dim=axis, keepdim=True)
    v = x.var(dim=axis, keepdim=True, unbiased=False)
    return (x - m) / torch.sqrt(v + eps)
def _scatter_add(tgt, idx, src, axis=0):
    return tgt.clone().index_add_(axis, idx, src)
def _segment_sum(src, seg, n):
    out = torch.zeros((n,) + src.shape[1:], dtype=src.dtype)
    return out.index_add_(0, seg, src)
def _pad(x, pw):
    flat = tuple(v for pair in reversed(pw) for v in pair)
    return torch.nn.functional.pad(x, flat)

kernel_name = 'recurrent_scan__gqa_block_cached__dense_activation_squared_relu__decode'

def setup_inputs():

    _w = (lambda a: (lambda t: t.to(torch.bfloat16) if t.is_floating_point() else t)(torch.as_tensor(a)))
    def _A_setup():
        B, Sq, D = 2, 1, 640
        Hq, Hkv, Sk, Dh = 32, 32, 8192, 192
        assert Hq % Hkv == 0, (Hq, Hkv)
        return {
            "x": _w((lambda *s: torch.randn(*s, dtype=torch.bfloat16) * 0.05)(B, Sq, D)),
            "k_cache": _w((lambda *s: torch.randn(*s, dtype=torch.bfloat16) * 1.6)(B, Hkv, Sk, Dh)),
            "v_cache": _w((lambda *s: torch.randn(*s, dtype=torch.bfloat16) * 0.2)(B, Hkv, Sk, Dh)),
            "wq": _w((lambda *s: torch.randn(*s, dtype=torch.bfloat16) * 1.6)(D, Hq * Dh) * (D ** -0.5)),
            "wo": _w((lambda *s: torch.randn(*s, dtype=torch.bfloat16) * 0.2)(Hq * Dh, D) * ((Hq * Dh) ** -0.5)),
        }
    _A_in = _A_setup()

    def _B_setup():
        B, S, D, F = 2, 1, 640, 1536
        return {
            "x": _w((lambda *s: torch.randn(*s, dtype=torch.bfloat16) * 0.05)(B, S, D)),
            "w1": _w((lambda *s: torch.randn(*s, dtype=torch.bfloat16) * 0.1)(D, F)),
            "b1": _w((lambda *s: torch.randn(*s, dtype=torch.bfloat16) * 0.1)(F)),
            "w2": _w((lambda *s: torch.randn(*s, dtype=torch.bfloat16) * 0.1)(F, D)),
            "b2": _w((lambda *s: torch.randn(*s, dtype=torch.bfloat16) * 0.1)(D)),
        }
    _B_in = _B_setup()

    return {
        "A_x": _A_in["x"],
        "A_k_cache": _A_in["k_cache"],
        "A_v_cache": _A_in["v_cache"],
        "A_wq": _A_in["wq"],
        "A_wo": _A_in["wo"],
        "B_x": _B_in["x"],
        "B_w1": _B_in["w1"],
        "B_b1": _B_in["b1"],
        "B_w2": _B_in["w2"],
        "B_b2": _B_in["b2"],
    }

def reference(A_x, A_k_cache, A_v_cache, A_wq, A_wo, B_x, B_w1, B_b1, B_w2, B_b2):
    def _A_ref(x, k_cache, v_cache, wq, wo):
        B, Sq, D = x.shape
        Hq, Dh = 32, 192
        Hkv = k_cache.shape[1]
        rep = Hq // Hkv
        q = (lambda x, *d: x.permute(*d))((x @ wq).reshape(B, Sq, Hq, Dh), 0, 2, 1, 3)
        k = (lambda x, n, axis: torch.repeat_interleave(x, n, dim=axis))(k_cache, rep, axis=1)
        v = (lambda x, n, axis: torch.repeat_interleave(x, n, dim=axis))(v_cache, rep, axis=1)
        scores = torch.einsum("bhqd,bhkd->bhqk", q, k) * (Dh ** -0.5)
        p = _softmax(scores, axis=-1)
        o = torch.einsum("bhqk,bhkd->bhqd", p, v)
        return (lambda x, *d: x.permute(*d))(o, 0, 2, 1, 3).reshape(B, Sq, Hq * Dh) @ wo

    def _B_ref(x, w1, b1, w2, b2):
        h = torch.einsum("bsd,df->bsf", x, w1) + b1
        zero = torch.zeros_like(h)
        r = torch.where(h > zero, h, zero)
        h = r * r
        out = torch.einsum("bsf,fd->bsd", h, w2) + b2
        return out

    x = A_x
    xn = _layernorm(x, axis=-1)
    s = _A_ref(xn, A_k_cache, A_v_cache, A_wq, A_wo)
    g = _silu(_B_ref(xn, B_w1, B_b1, B_w2, B_b2))
    return x + s * g
```

```cpp
#include <hip/hip_runtime.h>
#include <hip/hip_bf16.h>
#include <stdint.h>

#define GAS __attribute__((address_space(1)))
typedef unsigned v4u __attribute__((ext_vector_type(4)));
typedef __bf16 bf16x2 __attribute__((ext_vector_type(2)));
typedef unsigned short u16;

constexpr int B = 2, D = 640, H = 32, SK = 8192, DH = 192, F = 1536;
constexpr int NSPLIT = 4, KEYS_PER_WG = SK / NSPLIT;
constexpr int NW = 8;
constexpr int KEYS_PER_WAVE = KEYS_PER_WG / NW;

__device__ __forceinline__ float bf2f(u16 b) { return __builtin_bit_cast(float, (unsigned)b << 16); }
__device__ __forceinline__ u16 f2bf(float f) { return __builtin_bit_cast(u16, (__bf16)f); }
__device__ __forceinline__ float rbf(float f) { return bf2f(f2bf(f)); }
__device__ __forceinline__ float dot2(unsigned k, unsigned q, float acc) {
  return __builtin_amdgcn_fdot2_f32_bf16(__builtin_bit_cast(bf16x2, k), __builtin_bit_cast(bf16x2, q), acc, false);
}
template <int CTRL> __device__ __forceinline__ float dpp(float x) {
  return __builtin_bit_cast(float, __builtin_amdgcn_mov_dpp(__builtin_bit_cast(int, x), CTRL, 0xf, 0xf, true));
}
constexpr int XOR1 = 0xB1, XOR2 = 0x4E, XOR7 = 0x141, XOR8 = 0x128;
__device__ __forceinline__ float lo16(unsigned v) { return __builtin_bit_cast(float, v << 16); }
__device__ __forceinline__ float hi16(unsigned v) { return __builtin_bit_cast(float, v & 0xffff0000u); }

__device__ __forceinline__ void layernorm(const u16* x, float* xn  , float* red) {
  const int tid = threadIdx.x, lane = tid & 63, w = tid >> 6;
  if (w < 2) {
    float v[10]; float s = 0.f;
#pragma unroll
    for (int i = 0; i < 10; ++i) { v[i] = bf2f(x[w * D + lane + 64 * i]); s += v[i]; }
#pragma unroll
    for (int m = 32; m >= 1; m >>= 1) s += __shfl_xor(s, m);
    const float mean = s * (1.f / D);
    float q = 0.f;
#pragma unroll
    for (int i = 0; i < 10; ++i) { const float d = v[i] - mean; q += d * d; }
#pragma unroll
    for (int m = 32; m >= 1; m >>= 1) q += __shfl_xor(q, m);
    const float mb = rbf(mean), vb = rbf(q * (1.f / D));
    const float sd = rbf(sqrtf(rbf(vb + 1e-5f)));
#pragma unroll
    for (int i = 0; i < 10; ++i) xn[w * D + lane + 64 * i] = rbf(rbf(v[i] - mb) / sd);
  }
  __syncthreads();
}

__global__ void __launch_bounds__(256) k_prep(const u16* x, const u16* wq, const u16* w1, const u16* b1, u16* q, float* hbuf) {
  __shared__ float xn[2 * D];
  __shared__ float red[32 * 2 * 64];
  layernorm(x, xn, red);
  const int g = blockIdx.x, tid = threadIdx.x, c = tid & 7, r = tid >> 3;
  const bool isq = g < 96;
  const u16* W = isq ? wq + g * 64 : w1 + (g - 96) * 64;
  const int ld = isq ? H * DH : F;
  float a0[8], a1[8];
#pragma unroll
  for (int k = 0; k < 8; ++k) { a0[k] = 0.f; a1[k] = 0.f; }
  v4u wv[20];
#pragma unroll
  for (int i = 0; i < 20; ++i) wv[i] = *(const v4u*)(W + (size_t)(r + 32 * i) * ld + c * 8);
#pragma unroll
  for (int i = 0; i < 20; ++i) {
    const float x0 = xn[r + 32 * i], x1 = xn[D + r + 32 * i];
#pragma unroll
    for (int k = 0; k < 4; ++k) {
      a0[2 * k] = fmaf(x0, lo16(wv[i][k]), a0[2 * k]); a0[2 * k + 1] = fmaf(x0, hi16(wv[i][k]), a0[2 * k + 1]);
      a1[2 * k] = fmaf(x1, lo16(wv[i][k]), a1[2 * k]); a1[2 * k + 1] = fmaf(x1, hi16(wv[i][k]), a1[2 * k + 1]);
    }
  }
#pragma unroll
  for (int k = 0; k < 8; ++k) { red[(r * 2 + 0) * 64 + c * 8 + k] = a0[k]; red[(r * 2 + 1) * 64 + c * 8 + k] = a1[k]; }
  __syncthreads();
  if (tid < 128) {
    const int b = tid >> 6, n = tid & 63; float s = 0.f;
    for (int rr = 0; rr < 32; ++rr) s += red[(rr * 2 + b) * 64 + n];
    if (isq) q[b * H * DH + g * 64 + n] = f2bf(s);
    else { const int f = (g - 96) * 64 + n; float hv = rbf(rbf(s) + bf2f(b1[f])); hv = hv > 0.f ? hv : 0.f; hbuf[b * F + f] = rbf(hv * hv); }
  }
}

__device__ __forceinline__ v4u ldnt(const u16* p) { return __builtin_nontemporal_load((const GAS v4u*)p); }

__global__ void __launch_bounds__(NW * 64, 2) k_attn(const u16* kc, const u16* vc, const u16* q, float* part  ) {
  __shared__ float mrg[NW][200];
  const int bid = blockIdx.x;
  const int bh = bid / NSPLIT, sp = bid % NSPLIT;
  const int tid = threadIdx.x, lane = tid & 63, w = __builtin_amdgcn_readfirstlane(tid >> 6), r = lane >> 3, c = lane & 7;
  constexpr float C2 = 0.07216878364870322f * 1.4426950408889634f;
  v4u qv[3];
#pragma unroll
  for (int j = 0; j < 3; ++j) qv[j] = *(const v4u*)(q + bh * DH + j * 64 + c * 8);
  const int sbase = (bh * SK + sp * KEYS_PER_WG + w * 32) * (DH * 2);
  const int voff = r * (DH * 2) + c * 16;
  const __amdgpu_buffer_rsrc_t kp = __builtin_amdgcn_make_buffer_rsrc((void*)kc, 0, B * H * SK * DH * 2, 0x00020000);
  const __amdgpu_buffer_rsrc_t vp = __builtin_amdgcn_make_buffer_rsrc((void*)vc, 0, B * H * SK * DH * 2, 0x00020000);
  constexpr int TSTR = NW * 32 * DH * 2;
  constexpr int NT = KEYS_PER_WAVE / 32;
  v4u KA[12], VA[12], KB[12], VB[12];
#define LOADT(dst, p, t) _Pragma("unroll") for (int u = 0; u < 4; ++u) _Pragma("unroll") for (int j = 0; j < 3; ++j) dst[u * 3 + j] = __builtin_amdgcn_raw_buffer_load_b128(p, voff + j * 128, sbase + (t) * TSTR + u * 8 * DH * 2, 2);
  float m = -INFINITY, l = 0.f, acc[24];
#pragma unroll
  for (int k = 0; k < 24; ++k) acc[k] = 0.f;
  LOADT(KA, kp, 0) LOADT(VA, vp, 0) LOADT(KB, kp, 1) LOADT(VB, vp, 1)
  __builtin_amdgcn_sched_barrier(0);
#define STEP(Kb, Vb, tn, more) { \
    float s[4]; \
    _Pragma("unroll") for (int u = 0; u < 4; ++u) { float d0 = 0.f, d1 = 0.f; \
      _Pragma("unroll") for (int j = 0; j < 3; ++j) { d0 = dot2(Kb[u * 3 + j][0], qv[j][0], d0); d1 = dot2(Kb[u * 3 + j][1], qv[j][1], d1); d0 = dot2(Kb[u * 3 + j][2], qv[j][2], d0); d1 = dot2(Kb[u * 3 + j][3], qv[j][3], d1); } \
      s[u] = d0 + d1; } \
    __builtin_amdgcn_sched_barrier(0); \
    if (more) { LOADT(Kb, kp, tn) } \
    __builtin_amdgcn_sched_barrier(0); \
    _Pragma("unroll") for (int u = 0; u < 4; ++u) { s[u] += dpp<XOR1>(s[u]); s[u] += dpp<XOR2>(s[u]); s[u] += dpp<XOR7>(s[u]); s[u] *= C2; } \
    const float mn = fmaxf(fmaxf(m, fmaxf(s[0], s[1])), fmaxf(s[2], s[3])); \
    const float al = __builtin_amdgcn_exp2f(m - mn); m = mn; \
    float p[4]; _Pragma("unroll") for (int u = 0; u < 4; ++u) p[u] = __builtin_amdgcn_exp2f(s[u] - mn); \
    l = fmaf(l, al, (p[0] + p[1]) + (p[2] + p[3])); \
    _Pragma("unroll") for (int k = 0; k < 24; ++k) acc[k] *= al; \
    _Pragma("unroll") for (int u = 0; u < 4; ++u) _Pragma("unroll") for (int j = 0; j < 3; ++j) { __builtin_amdgcn_sched_barrier(0); _Pragma("unroll") for (int k = 0; k < 4; ++k) { \
        acc[j * 8 + 2 * k] = fmaf(p[u], lo16(Vb[u * 3 + j][k]), acc[j * 8 + 2 * k]); acc[j * 8 + 2 * k + 1] = fmaf(p[u], hi16(Vb[u * 3 + j][k]), acc[j * 8 + 2 * k + 1]); } \
        asm volatile("" : "+v"(acc[j * 8]), "+v"(acc[j * 8 + 1]), "+v"(acc[j * 8 + 2]), "+v"(acc[j * 8 + 3]), "+v"(acc[j * 8 + 4]), "+v"(acc[j * 8 + 5]), "+v"(acc[j * 8 + 6]), "+v"(acc[j * 8 + 7])); } \
    __builtin_amdgcn_sched_barrier(0); \
    if (more) { LOADT(Vb, vp, tn) } \
    __builtin_amdgcn_sched_barrier(0); \
  }
  for (int t = 0; t < NT - 2; t += 2) { STEP(KA, VA, t + 2, true) STEP(KB, VB, t + 3, true) }
  STEP(KA, VA, 0, false) STEP(KB, VB, 0, false)
  float M = m; M = fmaxf(M, __shfl_xor(M, 8)); M = fmaxf(M, __shfl_xor(M, 16)); M = fmaxf(M, __shfl_xor(M, 32));
  const float sc = __builtin_amdgcn_exp2f(m - M);
  l *= sc; l += __shfl_xor(l, 8); l += __shfl_xor(l, 16); l += __shfl_xor(l, 32);
#pragma unroll
  for (int k = 0; k < 24; ++k) { float a = acc[k] * sc; a += __shfl_xor(a, 8); a += __shfl_xor(a, 16); a += __shfl_xor(a, 32); acc[k] = a; }
  if (r == 0) {
#pragma unroll
    for (int j = 0; j < 3; ++j)
#pragma unroll
      for (int k = 0; k < 8; ++k) mrg[w][j * 64 + c * 8 + k] = acc[j * 8 + k];
    if (c == 0) { mrg[w][192] = M; mrg[w][193] = l; }
  }
  __syncthreads();
  if (tid < 192) {
    float MM = -INFINITY;
#pragma unroll
    for (int ww = 0; ww < NW; ++ww) MM = fmaxf(MM, mrg[ww][192]);
    float L = 0.f, o = 0.f;
#pragma unroll
    for (int ww = 0; ww < NW; ++ww) { const float e = __builtin_amdgcn_exp2f(mrg[ww][192] - MM); L = fmaf(e, mrg[ww][193], L); o = fmaf(e, mrg[ww][tid], o); }
    float* pp = part + (size_t)bid * 194;
    pp[tid] = o; if (tid == 0) { pp[192] = MM; pp[193] = L; }
  }
}

__global__ void __launch_bounds__(256) k_final(const u16* x, const float* part, const float* hbuf, const u16* wo, const u16* w2, const u16* b2, u16* out) {
  __shared__ float o[2 * H * DH];
  __shared__ float hs[2 * F];
  __shared__ float red[256 * 33];
  const int tid = threadIdx.x, n0 = blockIdx.x * 8;
  v4u wv[24], w2v[6];
#pragma unroll
  for (int i = 0; i < 24; ++i) wv[i] = *(const v4u*)(wo + (size_t)(tid + 256 * i) * D + n0);
#pragma unroll
  for (int i = 0; i < 6; ++i) w2v[i] = *(const v4u*)(w2 + (size_t)(tid + 256 * i) * D + n0);
  for (int i = tid; i < 2 * H * DH; i += 256) {
    const int bh = i / DH, d = i % DH; const float* pp = part + (size_t)bh * NSPLIT * 194;
    float M = -INFINITY;
#pragma unroll
    for (int s = 0; s < NSPLIT; ++s) M = fmaxf(M, pp[s * 194 + 192]);
    float L = 0.f, a = 0.f;
#pragma unroll
    for (int s = 0; s < NSPLIT; ++s) { const float e = __builtin_amdgcn_exp2f(pp[s * 194 + 192] - M); L = fmaf(e, pp[s * 194 + 193], L); a = fmaf(e, pp[s * 194 + d], a); }
    o[i] = rbf(a / L);
  }
  for (int i = tid; i < 2 * F; i += 256) hs[i] = hbuf[i];
  __syncthreads();
  float a[32];
#pragma unroll
  for (int k = 0; k < 32; ++k) a[k] = 0.f;
#pragma unroll
  for (int i = 0; i < 24; ++i) { const float x0 = o[tid + 256 * i], x1 = o[H * DH + tid + 256 * i];
#pragma unroll
    for (int k = 0; k < 4; ++k) { a[2 * k] = fmaf(x0, lo16(wv[i][k]), a[2 * k]); a[2 * k + 1] = fmaf(x0, hi16(wv[i][k]), a[2 * k + 1]);
      a[8 + 2 * k] = fmaf(x1, lo16(wv[i][k]), a[8 + 2 * k]); a[8 + 2 * k + 1] = fmaf(x1, hi16(wv[i][k]), a[8 + 2 * k + 1]); } }
#pragma unroll
  for (int i = 0; i < 6; ++i) { const float x0 = hs[tid + 256 * i], x1 = hs[F + tid + 256 * i];
#pragma unroll
    for (int k = 0; k < 4; ++k) { a[16 + 2 * k] = fmaf(x0, lo16(w2v[i][k]), a[16 + 2 * k]); a[16 + 2 * k + 1] = fmaf(x0, hi16(w2v[i][k]), a[16 + 2 * k + 1]);
      a[24 + 2 * k] = fmaf(x1, lo16(w2v[i][k]), a[24 + 2 * k]); a[24 + 2 * k + 1] = fmaf(x1, hi16(w2v[i][k]), a[24 + 2 * k + 1]); } }
#pragma unroll
  for (int k = 0; k < 32; ++k) red[tid * 33 + k] = a[k];
  __syncthreads();
  if (tid < 32) { float s = 0.f; for (int i = 0; i < 256; ++i) s += red[i * 33 + tid]; red[tid] = s; }
  __syncthreads();
  if (tid < 16) { const int b = tid >> 3, k = tid & 7, n = n0 + k;
    const float s = rbf(red[b * 8 + k]); const float t = rbf(rbf(red[16 + b * 8 + k]) + bf2f(b2[n]));
    const float g = rbf(t / (1.f + __expf(-t)));
    out[b * D + n] = f2bf(bf2f(x[b * D + n]) + rbf(s * g)); }
}

extern "C" void kernel_launch(void* const* d_in, const int* in_sizes, int n_in, void* d_out, int out_size, void* d_ws, size_t ws_size, hipStream_t stream) {
  const u16* x = (const u16*)d_in[0]; const u16* kc = (const u16*)d_in[1]; const u16* vc = (const u16*)d_in[2];
  const u16* wq = (const u16*)d_in[3]; const u16* wo = (const u16*)d_in[4];
  const u16* w1 = (const u16*)d_in[6]; const u16* b1 = (const u16*)d_in[7]; const u16* w2 = (const u16*)d_in[8]; const u16* b2 = (const u16*)d_in[9];
  u16* q = (u16*)d_ws; float* hbuf = (float*)((char*)d_ws + 32768); float* part = (float*)((char*)d_ws + 65536);
  k_prep<<<120, 256, 0, stream>>>(x, wq, w1, b1, q, hbuf);
  k_attn<<<B * H * NSPLIT, NW * 64, 0, stream>>>(kc, vc, q, part);
  k_final<<<80, 256, 0, stream>>>(x, part, hbuf, wo, w2, b2, (u16*)d_out);
}
```

```cpp
#include <hip/hip_runtime.h>
#include <hip/hip_bf16.h>
#include <stdint.h>

#define NS 6
#define P0 6
#define PACE 1
#define STAMPS 0

#define GAS __attribute__((address_space(1)))
#define LAS __attribute__((address_space(3)))
#define RLX_AGENT __ATOMIC_RELAXED, __HIP_MEMORY_SCOPE_AGENT
#define RLX_WG __ATOMIC_RELAXED, __HIP_MEMORY_SCOPE_WORKGROUP
typedef unsigned v4u __attribute__((ext_vector_type(4)));
typedef unsigned v2u __attribute__((ext_vector_type(2)));
typedef short v4s __attribute__((ext_vector_type(4)));
typedef short v8s __attribute__((ext_vector_type(8)));
typedef __bf16 v8bf __attribute__((ext_vector_type(8)));
typedef float f32x4 __attribute__((ext_vector_type(4)));
typedef float f32x2 __attribute__((ext_vector_type(2)));
typedef __bf16 bf16x2 __attribute__((ext_vector_type(2)));
typedef unsigned short u16;
typedef unsigned long long u64;
typedef GAS u64 gu64;

constexpr int NB = 2, D = 640, H = 32, SK = 8192, DH = 192, F = 1536;
constexpr int NWAVE = NS + 2, NTHR = NWAVE * 64;
constexpr int NU = 256;
constexpr int KVBYTES = NB * H * SK * DH * 2;
constexpr int AUX_NT = 2;
constexpr int RSRC3 = 0x00020000;
constexpr int WL = (38 + NWAVE - 1) / NWAVE;
constexpr size_t WS_EPOCH = 0;
constexpr size_t WS_QX = 4096;
constexpr size_t WS_PX = WS_QX + 256 * 512 * 8;
constexpr size_t WS_RX = WS_PX + 256 * 256 * 8;
constexpr size_t WS_CT = WS_RX + 8 * 32 * 320 * 8;
constexpr size_t WS_ST = WS_CT + 8 * 544 * 8;
constexpr int L_XN = 0;
constexpr int L_Q = L_XN + 2 * D * 4;
constexpr int L_HA = L_Q + 384;
constexpr int L_OA = L_HA + 192;
constexpr int L_CT = L_OA + 768;
constexpr int L_MG = L_CT + 16;
constexpr int L_WO = L_MG + NS * 800;
constexpr int L_END = L_WO + 240 * 160;
static_assert(L_WO % 16 == 0 && L_OA % 16 == 0 && L_HA % 16 == 0 && L_Q % 16 == 0 && L_CT % 16 == 0, "alignment");
constexpr unsigned SPIN_TICKS = 2000000u;

__device__ __forceinline__ float bf2f(u16 b) { return __builtin_bit_cast(float, (unsigned)b << 16); }
__device__ __forceinline__ u16 f2bf(float f) { return __builtin_bit_cast(u16, (__bf16)f); }
__device__ __forceinline__ float rbf(float f) { return bf2f(f2bf(f)); }
__device__ __forceinline__ float lo16(unsigned v) { return __builtin_bit_cast(float, v << 16); }
__device__ __forceinline__ float hi16(unsigned v) { return __builtin_bit_cast(float, v & 0xffff0000u); }
__device__ __forceinline__ float asf(unsigned v) { return __builtin_bit_cast(float, v); }
__device__ __forceinline__ unsigned asu(float v) { return __builtin_bit_cast(unsigned, v); }
__device__ __forceinline__ float dot2(unsigned k, unsigned q, float acc) {
  return __builtin_amdgcn_fdot2_f32_bf16(__builtin_bit_cast(bf16x2, k), __builtin_bit_cast(bf16x2, q), acc, false);
}
template <int CTRL> __device__ __forceinline__ float dpp(float x) {
  return __builtin_bit_cast(float, __builtin_amdgcn_mov_dpp(__builtin_bit_cast(int, x), CTRL, 0xf, 0xf, true));
}
constexpr int XOR1 = 0xB1, XOR2 = 0x4E, XOR7 = 0x141, ROR8 = 0x128, ROR4 = 0x124, ROR2 = 0x122, ROR1 = 0x121;
__device__ __forceinline__ float tr32(float x, float y) {
  auto s = __builtin_amdgcn_permlane32_swap(asu(x), asu(y), false, false); return asf(s[0]) + asf(s[1]);
}
__device__ __forceinline__ float tr16(float x, float y) {
  auto s = __builtin_amdgcn_permlane16_swap(asu(x), asu(y), false, false); return asf(s[0]) + asf(s[1]);
}
__device__ __forceinline__ float tr8(float x, float y, bool up) {
  const float keep = up ? y : x, send = up ? x : y; return keep + dpp<ROR8>(send);
}
__device__ __forceinline__ float max16(float x) { auto s = __builtin_amdgcn_permlane16_swap(asu(x), asu(x), false, false); return fmaxf(asf(s[0]), asf(s[1])); }
__device__ __forceinline__ float max32(float x) { auto s = __builtin_amdgcn_permlane32_swap(asu(x), asu(x), false, false); return fmaxf(asf(s[0]), asf(s[1])); }
__device__ __forceinline__ unsigned now() { return (unsigned)__builtin_amdgcn_s_memrealtime(); }
__device__ __forceinline__ void barrier() { asm volatile("s_waitcnt lgkmcnt(0)\n\ts_barrier" ::: "memory"); }
__device__ __forceinline__ void put(gu64* g, unsigned tag, float v) { __hip_atomic_store(g, ((u64)tag << 32) | asu(v), RLX_AGENT); }
__device__ __forceinline__ u64 get(const gu64* g) { return __hip_atomic_load(g, RLX_AGENT); }
__device__ __forceinline__ unsigned lds_ld(LAS unsigned* p) { return __builtin_amdgcn_readfirstlane(__hip_atomic_load(p, RLX_WG)); }
#define SCHED() do { asm volatile("" ::: "memory"); __builtin_amdgcn_sched_barrier(0); } while (0)
#define STAMP(i) do {} while (0)

__global__ void __launch_bounds__(NTHR, 2)
fused_decode(const u16* x, const u16* kc, const u16* vc, const u16* wq, const u16* wo,
             const u16* w1, const u16* b1, const u16* w2, const u16* b2, u16* out, unsigned char* ws) {
  __shared__ __attribute__((aligned(16))) unsigned char lds_raw[L_END];
  LAS unsigned char* L = (LAS unsigned char*)lds_raw;
  const int bid = blockIdx.x, h = bid >> 3, j = bid & 7, b = j >> 2, sp = j & 3;
  const int tid = threadIdx.x, lane = tid & 63, w = __builtin_amdgcn_readfirstlane(tid >> 6), r = lane >> 3, c = lane & 7;
  LAS unsigned* ctr = (LAS unsigned*)(L + L_CT);
  LAS u64* pace = (LAS u64*)(L + L_CT + 8);
  const unsigned ts = now();
  STAMP(0);
  unsigned tag;
  constexpr float C2 = 0.07216878364870322f * 1.4426950408889634f;

  v4u wreg[WL];
  auto load_w = [&]() {
#pragma unroll
    for (int i = 0; i < WL; ++i) {
      const int n = w * WL + i; const bool iswo = n < 30;
      const int id = (iswo ? n : n - 30) * 64 + lane, row = id / 10, cc = id - row * 10;
      const __amdgpu_buffer_rsrc_t rs = __builtin_amdgcn_make_buffer_rsrc((void*)(iswo ? wo : w2), 0, iswo ? H * DH * D * 2 : F * D * 2, RSRC3);
      const int vo = (iswo || id < 480) ? row * (D * 2) + cc * 16 : 0x7ffffff0;
      wreg[i] = __builtin_amdgcn_raw_buffer_load_b128(rs, vo, (iswo ? DH : 48) * h * (D * 2) + 160 * j, AUX_NT);
    }
  };

  if (w < NS) {
    if (tid == 0) { *ctr = NS * P0; *pace = 0ull; }
    barrier();
    const int rbase = ((b * H + h) * SK + sp * 8) * (DH * 2);
    constexpr int USTR = 4 * 8 * DH * 2;
    const int voff = r * (DH * 2) + c * 16;
    v4u Kb[P0][3], Vb[P0][3];
    const __amdgpu_buffer_rsrc_t kr = __builtin_amdgcn_make_buffer_rsrc((void*)kc, 0, KVBYTES, RSRC3), vr = __builtin_amdgcn_make_buffer_rsrc((void*)vc, 0, KVBYTES, RSRC3);
    static_assert(NS * P0 + NS <= NU, "the first ticket of every wave is a real unit");
#define LD(dst, rs, t) { const int so_ = rbase + (t) * USTR; \
    _Pragma("unroll") for (int u = 0; u < 3; ++u) dst[u] = __builtin_amdgcn_raw_buffer_load_b128(rs, voff + u * 128, so_, AUX_NT); }
#pragma unroll
    for (int i = 0; i < P0; ++i) { LD(Kb[i], kr, w + i * NS) SCHED(); LD(Vb[i], vr, w + i * NS) SCHED(); }
    SCHED();
    STAMP(1);
    barrier();
    STAMP(2);
    tag = lds_ld(ctr + 1);
    v4u qv[3];
#pragma unroll
    for (int u = 0; u < 3; ++u) qv[u] = *(LAS v4u*)(L + L_Q + u * 128 + c * 16);
    float m = -INFINITY, l = 0.f; f32x2 ac2[12];
#pragma unroll
    for (int k = 0; k < 12; ++k) ac2[k] = f32x2{0.f, 0.f};
#define QK(Kx, s) { float d0 = 0.f, d1 = 0.f; \
      _Pragma("unroll") for (int u = 0; u < 3; ++u) { d0 = dot2(Kx[u][0], qv[u][0], d0); d1 = dot2(Kx[u][1], qv[u][1], d1); d0 = dot2(Kx[u][2], qv[u][2], d0); d1 = dot2(Kx[u][3], qv[u][3], d1); } \
      s = d0 + d1; asm volatile("" : "+v"(s)); }
#define PV(Vx, s) { s += dpp<XOR1>(s); s += dpp<XOR2>(s); s += dpp<XOR7>(s); s *= C2; \
      const float mn = fmaxf(m, s), al = __builtin_amdgcn_exp2f(m - mn), p = __builtin_amdgcn_exp2f(s - mn); m = mn; l = fmaf(l, al, p); \
      _Pragma("unroll") for (int u = 0; u < 3; ++u) { SCHED(); _Pragma("unroll") for (int k = 0; k < 4; ++k) \
          ac2[u * 4 + k] = __builtin_elementwise_fma(f32x2{p, p}, f32x2{lo16(Vx[u][k]), hi16(Vx[u][k])}, ac2[u * 4 + k] * al); \
        asm volatile("" : "+v"(ac2[u * 4]), "+v"(ac2[u * 4 + 1]), "+v"(ac2[u * 4 + 2]), "+v"(ac2[u * 4 + 3])); } }
    const unsigned ctra = L_CT + (unsigned)(uintptr_t)L, one = 1u;
#define TICKET(tk) asm volatile("s_mov_b64 exec, 1\n\tds_add_rtn_u32 %0, %1, %2\n\ts_mov_b64 exec, -1" : "=&v"(tk) : "v"(ctra), "v"(one) : "memory")
#define TAKE(tk) ({ asm volatile("s_waitcnt lgkmcnt(0)" : "+v"(tk) :: "memory"); __builtin_amdgcn_readfirstlane(tk); })
    { int tk; TICKET(tk); SCHED();
      float s; QK(Kb[0], s)
      const int cur = TAKE(tk);
      SCHED(); LD(Kb[0], kr, cur) SCHED();
      PV(Vb[0], s)
      SCHED(); LD(Vb[0], vr, cur) SCHED();
    }
#pragma unroll
    for (int i = 1; i < P0; ++i) { float s; QK(Kb[i], s) PV(Vb[i], s) SCHED(); }
    STAMP(3);
    float s;
    for (int it = 0; it < NU; ++it) {
      int tk; TICKET(tk); SCHED();
      QK(Kb[0], s)
      const int nxt = TAKE(tk);
      if ((unsigned)nxt >= (unsigned)NU) break;
#pragma unroll 1
      for (int spn = 0; spn < 256; ++spn) {
        const u64 pv = __hip_atomic_load(pace, RLX_WG);
        const float t0 = asf(__builtin_amdgcn_readfirstlane((unsigned)pv)), tau = asf(__builtin_amdgcn_readfirstlane((unsigned)(pv >> 32)));
        if (fmaf((float)nxt, tau, t0) <= (float)(int)(now() - ts)) break;
        __builtin_amdgcn_s_sleep(2);
      }
      SCHED(); LD(Kb[0], kr, nxt) SCHED();
      PV(Vb[0], s)
      SCHED(); LD(Vb[0], vr, nxt) SCHED();
    }
    load_w();
    SCHED();
    PV(Vb[0], s)
    STAMP(4);
    float M = fmaxf(m, dpp<ROR8>(m)); M = max16(M); M = max32(M);
    const float sc = __builtin_amdgcn_exp2f(m - M);
    l *= sc; l += dpp<ROR8>(l); l = tr16(l, l); l = tr32(l, l);
    float acc[24];
#pragma unroll
    for (int k = 0; k < 12; ++k) { const f32x2 t = ac2[k] * sc; acc[2 * k] = t[0]; acc[2 * k + 1] = t[1]; }
#pragma unroll
    for (int k = 0; k < 12; ++k) acc[k] = tr32(acc[k], acc[k + 12]);
#pragma unroll
    for (int k = 0; k < 6; ++k) acc[k] = tr16(acc[k], acc[k + 6]);
#pragma unroll
    for (int k = 0; k < 3; ++k) acc[k] = tr8(acc[k], acc[k + 3], r & 1);
    LAS float* mg = (LAS float*)(L + L_MG) + w * 200;
#pragma unroll
    for (int k = 0; k < 3; ++k) mg[k * 64 + lane] = acc[k];
    if (lane == 0) { mg[192] = M; mg[193] = l; }
  } else {
    __builtin_amdgcn_s_setprio(3);
    const int a = w - NS;
    const unsigned epoch = __hip_atomic_load((GAS unsigned*)(ws + WS_EPOCH), RLX_AGENT);
    const int xb = lane >> 5, xl = lane & 31;
    const __amdgpu_buffer_rsrc_t xr = __builtin_amdgcn_make_buffer_rsrc((void*)x, 0, NB * D * 2, RSRC3);
    const int xo = xb * (D * 2) + xl * 40;
    const v4u x0 = __builtin_amdgcn_raw_buffer_load_b128(xr, xo, 0, 0), x1 = __builtin_amdgcn_raw_buffer_load_b128(xr, xo + 16, 0, 0);
    const v2u x2 = __builtin_amdgcn_raw_buffer_load_b64(xr, xo + 32, 0, 0);
    v4u wa[10], wb[10];
    { const __amdgpu_buffer_rsrc_t ra = __builtin_amdgcn_make_buffer_rsrc((void*)wq, 0, D * H * DH * 2, RSRC3);
      const __amdgpu_buffer_rsrc_t rb = __builtin_amdgcn_make_buffer_rsrc((void*)(a ? w1 : wq), 0, a ? D * F * 2 : D * H * DH * 2, RSRC3);
      const int sa = H * DH * 2, sb = a ? F * 2 : H * DH * 2;
      const int va = r * sa + c * 16, vb = (a && c >= 6) ? 0x7ffffff0 : r * sb + c * 16;
      const int oa = 80 * j * sa + h * (DH * 2) + a * 256, ob = 80 * j * sb + (a ? h * 96 : h * (DH * 2) + 128);
#pragma unroll
      for (int u = 0; u < 10; ++u) { wa[u] = __builtin_amdgcn_raw_buffer_load_b128(ra, va, oa + u * 8 * sa, AUX_NT); wb[u] = __builtin_amdgcn_raw_buffer_load_b128(rb, vb, ob + u * 8 * sb, AUX_NT); }
    }
    SCHED();
    barrier();
    STAMP(1);
    tag = (unsigned)__builtin_amdgcn_readfirstlane(epoch) * 0x9E3779B1u + 0x7F4A7C15u;
    if (a == 0 && lane == 0) ctr[1] = tag;
    {
      float v[20];
#pragma unroll
      for (int k = 0; k < 4; ++k) { v[2 * k] = lo16(x0[k]); v[2 * k + 1] = hi16(x0[k]); v[8 + 2 * k] = lo16(x1[k]); v[9 + 2 * k] = hi16(x1[k]); }
      v[16] = lo16(x2[0]); v[17] = hi16(x2[0]); v[18] = lo16(x2[1]); v[19] = hi16(x2[1]);
      float s = 0.f;
#pragma unroll
      for (int k = 0; k < 20; ++k) s += v[k];
      s += dpp<ROR1>(s); s += dpp<ROR2>(s); s += dpp<ROR4>(s); s += dpp<ROR8>(s); s = tr16(s, s);
      const float mean = s * (1.f / D);
      float q = 0.f;
#pragma unroll
      for (int k = 0; k < 20; ++k) { const float d = v[k] - mean; q = fmaf(d, d, q); }
      q += dpp<ROR1>(q); q += dpp<ROR2>(q); q += dpp<ROR4>(q); q += dpp<ROR8>(q); q = tr16(q, q);
      const float mb = rbf(mean), sd = rbf(sqrtf(rbf(rbf(q * (1.f / D)) + 1e-5f)));
      LAS float* xn = (LAS float*)(L + L_XN) + xb * D + xl * 20;
#pragma unroll
      for (int k = 0; k < 20; k += 4) *(LAS f32x4*)(xn + k) = f32x4{rbf(rbf(v[k] - mb) / sd), rbf(rbf(v[k + 1] - mb) / sd), rbf(rbf(v[k + 2] - mb) / sd), rbf(rbf(v[k + 3] - mb) / sd)};
    }
    asm volatile("s_waitcnt lgkmcnt(0)" ::: "memory");
    float pa[32];
#pragma unroll
    for (int k = 0; k < 32; ++k) pa[k] = 0.f;
    { const LAS float* xn = (const LAS float*)(L + L_XN) + 80 * j + r;
#pragma unroll
      for (int u = 0; u < 10; ++u) { const float xa = xn[8 * u], xc = xn[D + 8 * u];
#pragma unroll
        for (int k = 0; k < 4; ++k) {
          const float a0 = lo16(wa[u][k]), a1 = hi16(wa[u][k]), b0 = lo16(wb[u][k]), b1v = hi16(wb[u][k]);
          pa[2 * k] = fmaf(xa, a0, pa[2 * k]); pa[2 * k + 1] = fmaf(xa, a1, pa[2 * k + 1]); pa[8 + 2 * k] = fmaf(xa, b0, pa[8 + 2 * k]); pa[9 + 2 * k] = fmaf(xa, b1v, pa[9 + 2 * k]);
          pa[16 + 2 * k] = fmaf(xc, a0, pa[16 + 2 * k]); pa[17 + 2 * k] = fmaf(xc, a1, pa[17 + 2 * k]); pa[24 + 2 * k] = fmaf(xc, b0, pa[24 + 2 * k]); pa[25 + 2 * k] = fmaf(xc, b1v, pa[25 + 2 * k]);
        } } }
#pragma unroll
    for (int k = 0; k < 16; ++k) pa[k] = tr32(pa[k], pa[k + 16]);
#pragma unroll
    for (int k = 0; k < 8; ++k) pa[k] = tr16(pa[k], pa[k + 8]);
#pragma unroll
    for (int k = 0; k < 4; ++k) pa[k] = tr8(pa[k], pa[k + 4], r & 1);
    STAMP(2);
    { gu64* qx = (gu64*)(ws + WS_QX) + bid * 512 + (lane >> 5) * 256 + (2 * a + ((lane >> 4) & 1)) * 64 + (lane & 15);
#pragma unroll
      for (int t = 0; t < 4; ++t) put(qx + t * 16, tag, pa[t]); }
    const int col = (lane & 7) * 8 + ((lane >> 3) & 1) * 4 + (lane >> 4);
    if (a == 0) {
      const gu64* qx = (const gu64*)(ws + WS_QX) + (h * 8) * 512 + b * 256 + lane;
      float qs[3];
      for (;;) { bool ok = true; u64 g[24];
#pragma unroll
        for (int p = 0; p < 8; ++p)
#pragma unroll
          for (int u = 0; u < 3; ++u) g[p * 3 + u] = get(qx + p * 512 + u * 64);
        qs[0] = qs[1] = qs[2] = 0.f;
#pragma unroll
        for (int p = 0; p < 8; ++p)
#pragma unroll
          for (int u = 0; u < 3; ++u) { ok &= (unsigned)(g[p * 3 + u] >> 32) == tag; qs[u] += asf((unsigned)g[p * 3 + u]); }
        if (__all(ok) || now() - ts > SPIN_TICKS) break;
      }
#pragma unroll
      for (int u = 0; u < 3; ++u) *(LAS u16*)(L + L_Q + (u * 64 + col) * 2) = f2bf(qs[u]);
    }
    STAMP(3);
    barrier();
    __builtin_amdgcn_s_setprio(0);
    if (a == 0) {
      const gu64* qx = (const gu64*)(ws + WS_QX) + (h * 8) * 512 + 192 + lane;
      const bool live = (lane & 7) < 6;
      const float bias = bf2f(b1[48 * h + (live ? col : 0)]);
      float hs[2];
      for (;;) { bool ok = true; u64 g[16];
#pragma unroll
        for (int p = 0; p < 8; ++p) { g[2 * p] = get(qx + p * 512); g[2 * p + 1] = get(qx + p * 512 + 256); }
        hs[0] = hs[1] = 0.f;
#pragma unroll
        for (int p = 0; p < 16; ++p) { ok &= (unsigned)(g[p] >> 32) == tag; hs[p & 1] += asf((unsigned)g[p]); }
        if (__all(ok) || now() - ts > SPIN_TICKS) break;
        __builtin_amdgcn_s_sleep(16);
      }
      if (live) {
#pragma unroll
        for (int u = 0; u < 2; ++u) { float t = rbf(rbf(hs[u]) + bias); t = t > 0.f ? t : 0.f; *(LAS u16*)(L + L_HA + (u * 48 + col) * 2) = f2bf(t * t); } }
    }
    else {
      constexpr int M0 = NS * P0 + 12, DM = 48, RD = 16;
      for (int rd = 0; M0 + rd * DM + RD < NU - 8; ++rd) {
        const unsigned Mi = M0 + rd * DM;
        gu64* cl = (gu64*)(ws + WS_CT) + rd * 544;
        while (lds_ld(ctr) < Mi && now() - ts < SPIN_TICKS) __builtin_amdgcn_s_sleep(8);
        if (lane == 0) __hip_atomic_store(cl + bid, ((u64)tag << 32) | now(), RLX_AGENT);
        while (lds_ld(ctr) < Mi + RD && now() - ts < SPIN_TICKS) __builtin_amdgcn_s_sleep(8);
        float s = 0.f; int n = 0;
#pragma unroll
        for (int k = 0; k < 4; ++k) { const u64 v = get(cl + lane + 64 * k); const bool ok = (unsigned)(v >> 32) == tag;
          s += ok ? (float)(int)((unsigned)v - ts) : 0.f; n += __builtin_popcountll(__ballot(ok)); }
        s += dpp<ROR1>(s); s += dpp<ROR2>(s); s += dpp<ROR4>(s); s += dpp<ROR8>(s); s = tr16(s, s); s = tr32(s, s);
        const float tm = s / (float)n, tau = tm / ((float)Mi + 6.25f);
        if (lane == 0 && n > 0) __hip_atomic_store(pace, ((u64)asu(tau) << 32) | asu(tm - (float)Mi * tau), RLX_WG);
      }
    }
    while (lds_ld(ctr) < NU && now() - ts < SPIN_TICKS) __builtin_amdgcn_s_sleep(4);
    load_w();
    SCHED();
  }
  const int oi = 5 * h + (w < 5 ? w : 0), ob = oi / 80, ocol = 80 * j + oi - ob * 80;
  const float xres = bf2f(x[ob * D + ocol]), b2v = bf2f(b2[ocol]);
  barrier();
  STAMP(5);
  if (w < 3) {
    const LAS float* mg = (const LAS float*)(L + L_MG);
    float MM = -INFINITY;
#pragma unroll
    for (int i = 0; i < NS; ++i) MM = fmaxf(MM, mg[i * 200 + 192]);
    float Ls = 0.f, o = 0.f;
#pragma unroll
    for (int i = 0; i < NS; ++i) { const float e = __builtin_amdgcn_exp2f(mg[i * 200 + 192] - MM); Ls = fmaf(e, mg[i * 200 + 193], Ls); o = fmaf(e, mg[i * 200 + tid], o); }
    gu64* px = (gu64*)(ws + WS_PX) + bid * 256;
    put(px + tid, tag, o / Ls);
    if (tid == 0) put(px + 192, tag, MM + __builtin_amdgcn_logf(Ls));
  }
  STAMP(6);
#pragma unroll
  for (int i = 0; i < WL; ++i) { const int n = w * WL + i; if (n * 64 + lane < 2400) *(LAS v4u*)(L + L_WO + (n * 64 + lane) * 16) = wreg[i]; }
  barrier();
  STAMP(7);
  v4s Bf[15];
  if (w < 5) {
    const LAS unsigned char* base = L + L_WO + (((lane & 15) >> 2) + 8 * (lane >> 4)) * 160 + w * 32 + (lane & 3) * 8;
#pragma unroll
    for (int k = 0; k < 7; ++k) {
      Bf[2 * k] = __builtin_amdgcn_ds_read_tr16_b64_v4i16((LAS v4s*)(base + k * 32 * 160));
      Bf[2 * k + 1] = __builtin_amdgcn_ds_read_tr16_b64_v4i16((LAS v4s*)(base + k * 32 * 160 + 4 * 160));
    }
    Bf[14] = __builtin_amdgcn_ds_read_tr16_b64_v4i16((LAS v4s*)(base + 7 * 32 * 160 - 4 * (lane >> 4) * 160));
  }
  if (w < 6) {
    const int bb = w >= 3, pos = tid - bb * 192;
    const gu64* px = (const gu64*)(ws + WS_PX) + (h * 8 + bb * 4) * 256;
    u64 g[8];
    for (;;) { bool ok = true;
#pragma unroll
      for (int s = 0; s < 4; ++s) { g[s] = get(px + s * 256 + pos); g[4 + s] = get(px + s * 256 + 192); }
#pragma unroll
      for (int s = 0; s < 8; ++s) ok &= (unsigned)(g[s] >> 32) == tag;
      if (__all(ok) || now() - ts > SPIN_TICKS) break;
    }
    const float e0 = asf((unsigned)g[4]), e1 = asf((unsigned)g[5]), e2 = asf((unsigned)g[6]), e3 = asf((unsigned)g[7]);
    const float em = fmaxf(fmaxf(e0, e1), fmaxf(e2, e3));
    const float w0 = __builtin_amdgcn_exp2f(e0 - em), w1v = __builtin_amdgcn_exp2f(e1 - em), w2v = __builtin_amdgcn_exp2f(e2 - em), w3 = __builtin_amdgcn_exp2f(e3 - em);
    const float o = (w0 * asf((unsigned)g[0]) + w1v * asf((unsigned)g[1]) + w2v * asf((unsigned)g[2]) + w3 * asf((unsigned)g[3])) / (w0 + w1v + w2v + w3);
    const int i = (lane >> 5) * 12 + ((lane >> 4) & 1) * 6 + ((lane >> 3) & 1) * 3 + (w - 3 * bb);
    *(LAS u16*)(L + L_OA + (bb * DH + (i >> 3) * 64 + c * 8 + (i & 7)) * 2) = f2bf(o);
  }
  STAMP(8);
  barrier();
  if (w < 5) {
    f32x4 S = {0.f, 0.f, 0.f, 0.f}, G = {0.f, 0.f, 0.f, 0.f};
    const LAS unsigned char* ao = L + L_OA + (lane & 1) * (DH * 2) + (lane >> 4) * 16;
#pragma unroll
    for (int k = 0; k < 6; ++k) {
      const v8s bf = __builtin_shufflevector(Bf[2 * k], Bf[2 * k + 1], 0, 1, 2, 3, 4, 5, 6, 7);
      S = __builtin_amdgcn_mfma_f32_16x16x32_bf16(*(const LAS v8bf*)(ao + k * 64), __builtin_bit_cast(v8bf, bf), S, 0, 0, 0);
    }
    { const LAS unsigned char* ah = L + L_HA + (lane & 1) * 96;
      const v8s bf = __builtin_shufflevector(Bf[12], Bf[13], 0, 1, 2, 3, 4, 5, 6, 7);
      G = __builtin_amdgcn_mfma_f32_16x16x32_bf16(*(const LAS v8bf*)(ah + (lane >> 4) * 16), __builtin_bit_cast(v8bf, bf), G, 0, 0, 0);
      G = __builtin_amdgcn_mfma_f32_16x16x16bf16_1k(*(const LAS v4s*)(ah + 64 + (lane >> 4) * 8), Bf[14], G, 0, 0, 0);
    }
    const int gq = lane >> 4, vb = gq & 1, which = gq >> 1;
    const float val = which ? (vb ? G[1] : G[0]) : (vb ? S[1] : S[0]);
    const int i = vb * 80 + 16 * w + (lane & 15), hp = i / 5;
    put((gu64*)(ws + WS_RX) + ((j * 32 + hp) * 32 + h) * 10 + which * 5 + (i - hp * 5), tag, val);
    STAMP(9);
    const gu64* rx = (const gu64*)(ws + WS_RX) + ((j * 32 + h) * 32 + (lane >> 1)) * 10 + (lane & 1) * 5 + w;
    u64 g;
    for (;;) { g = get(rx); if (__all((unsigned)(g >> 32) == tag) || now() - ts > SPIN_TICKS) break; }
    float s = asf((unsigned)g);
    s += dpp<ROR2>(s); s += dpp<ROR4>(s); s += dpp<ROR8>(s); s = tr16(s, s); s = tr32(s, s);
    const float t = rbf(rbf(dpp<XOR1>(s)) + b2v);
    const float gate = rbf(t / (1.f + __expf(-t)));
    if (lane == 0) out[ob * D + ocol] = f2bf(xres + rbf(rbf(s) * gate));
    if (bid == 0 && tid == 0) __hip_atomic_store((GAS unsigned*)(ws + WS_EPOCH), tag, RLX_AGENT);
    STAMP(10);
  }
}

extern "C" void kernel_launch(void* const* d_in, const int* in_sizes, int n_in, void* d_out, int out_size, void* d_ws, size_t ws_size, hipStream_t stream) {
  fused_decode<<<256, NTHR, 0, stream>>>((const u16*)d_in[0], (const u16*)d_in[1], (const u16*)d_in[2], (const u16*)d_in[3], (const u16*)d_in[4],
                                         (const u16*)d_in[6], (const u16*)d_in[7], (const u16*)d_in[8], (const u16*)d_in[9], (u16*)d_out, (unsigned char*)d_ws);
}
```

```cpp
#include <hip/hip_runtime.h>
#include <hip/hip_bf16.h>
#include <stdint.h>

#define NS 6
#define P0 6
#define PACE 1
#define STAMPS 0

#define GAS __attribute__((address_space(1)))
#define LAS __attribute__((address_space(3)))
#define RLX_AGENT __ATOMIC_RELAXED, __HIP_MEMORY_SCOPE_AGENT
#define RLX_WG __ATOMIC_RELAXED, __HIP_MEMORY_SCOPE_WORKGROUP
typedef unsigned v4u __attribute__((ext_vector_type(4)));
typedef unsigned v2u __attribute__((ext_vector_type(2)));
typedef short v4s __attribute__((ext_vector_type(4)));
typedef short v8s __attribute__((ext_vector_type(8)));
typedef __bf16 v8bf __attribute__((ext_vector_type(8)));
typedef float f32x4 __attribute__((ext_vector_type(4)));
typedef float f32x2 __attribute__((ext_vector_type(2)));
typedef __bf16 bf16x2 __attribute__((ext_vector_type(2)));
typedef unsigned short u16;
typedef unsigned long long u64;
typedef GAS u64 gu64;

constexpr int NB = 2, D = 640, H = 32, SK = 8192, DH = 192, F = 1536;
constexpr int NWAVE = NS + 2, NTHR = NWAVE * 64;
constexpr int NU = 256;
constexpr int KVBYTES = NB * H * SK * DH * 2;
constexpr int AUX_NT = 2;
#define KAUX 2
#define VAUX 2
constexpr int AUX_kr = KAUX, AUX_vr = VAUX;
constexpr int RSRC3 = 0x00020000;
constexpr int WL = (38 + NWAVE - 1) / NWAVE;
constexpr size_t WS_EPOCH = 0;
constexpr size_t WS_QX = 4096;
constexpr size_t WS_PX = WS_QX + 256 * 512 * 8;
constexpr size_t WS_RX = WS_PX + 256 * 256 * 8;
constexpr size_t WS_CT = WS_RX + 8 * 32 * 320 * 8;
constexpr size_t WS_ST = WS_CT + 8 * 544 * 8;
constexpr int L_XN = 0;
constexpr int L_Q = L_XN + 2 * D * 4;
constexpr int L_HA = L_Q + 384;
constexpr int L_OA = L_HA + 192;
constexpr int L_CT = L_OA + 768;
constexpr int L_MG = L_CT + 16;
constexpr int L_WO = L_MG + NS * 800;
constexpr int L_END = L_WO + 240 * 160;
static_assert(L_WO % 16 == 0 && L_OA % 16 == 0 && L_HA % 16 == 0 && L_Q % 16 == 0 && L_CT % 16 == 0, "alignment");
constexpr unsigned SPIN_TICKS = 2000000u;

__device__ __forceinline__ float bf2f(u16 b) { return __builtin_bit_cast(float, (unsigned)b << 16); }
__device__ __forceinline__ u16 f2bf(float f) { return __builtin_bit_cast(u16, (__bf16)f); }
__device__ __forceinline__ float rbf(float f) { return bf2f(f2bf(f)); }
__device__ __forceinline__ float lo16(unsigned v) { return __builtin_bit_cast(float, v << 16); }
__device__ __forceinline__ float hi16(unsigned v) { return __builtin_bit_cast(float, v & 0xffff0000u); }
__device__ __forceinline__ float asf(unsigned v) { return __builtin_bit_cast(float, v); }
__device__ __forceinline__ unsigned asu(float v) { return __builtin_bit_cast(unsigned, v); }
__device__ __forceinline__ float dot2(unsigned k, unsigned q, float acc) {
  return __builtin_amdgcn_fdot2_f32_bf16(__builtin_bit_cast(bf16x2, k), __builtin_bit_cast(bf16x2, q), acc, false);
}
template <int CTRL> __device__ __forceinline__ float dpp(float x) {
  return __builtin_bit_cast(float, __builtin_amdgcn_mov_dpp(__builtin_bit_cast(int, x), CTRL, 0xf, 0xf, true));
}
constexpr int XOR1 = 0xB1, XOR2 = 0x4E, XOR7 = 0x141, ROR8 = 0x128, ROR4 = 0x124, ROR2 = 0x122, ROR1 = 0x121;
__device__ __forceinline__ float tr32(float x, float y) {
  auto s = __builtin_amdgcn_permlane32_swap(asu(x), asu(y), false, false); return asf(s[0]) + asf(s[1]);
}
__device__ __forceinline__ float tr16(float x, float y) {
  auto s = __builtin_amdgcn_permlane16_swap(asu(x), asu(y), false, false); return asf(s[0]) + asf(s[1]);
}
__device__ __forceinline__ float tr8(float x, float y, bool up) {
  const float keep = up ? y : x, send = up ? x : y; return keep + dpp<ROR8>(send);
}
__device__ __forceinline__ float max16(float x) { auto s = __builtin_amdgcn_permlane16_swap(asu(x), asu(x), false, false); return fmaxf(asf(s[0]), asf(s[1])); }
__device__ __forceinline__ float max32(float x) { auto s = __builtin_amdgcn_permlane32_swap(asu(x), asu(x), false, false); return fmaxf(asf(s[0]), asf(s[1])); }
__device__ __forceinline__ unsigned now() { return (unsigned)__builtin_amdgcn_s_memrealtime(); }
__device__ __forceinline__ void barrier() { asm volatile("s_waitcnt lgkmcnt(0)\n\ts_barrier" ::: "memory"); }
__device__ __forceinline__ void put(gu64* g, unsigned tag, float v) { __hip_atomic_store(g, ((u64)tag << 32) | asu(v), RLX_AGENT); }
__device__ __forceinline__ u64 get(const gu64* g) { return __hip_atomic_load(g, RLX_AGENT); }
__device__ __forceinline__ unsigned lds_ld(LAS unsigned* p) { return __builtin_amdgcn_readfirstlane(__hip_atomic_load(p, RLX_WG)); }
#define SCHED() do { asm volatile("" ::: "memory"); __builtin_amdgcn_sched_barrier(0); } while (0)
#define STAMP(i) do {} while (0)

__global__ void __launch_bounds__(NTHR, 2)
fused_decode(const u16* x, const u16* kc, const u16* vc, const u16* wq, const u16* wo,
             const u16* w1, const u16* b1, const u16* w2, const u16* b2, u16* out, unsigned char* ws) {
  __shared__ __attribute__((aligned(16))) unsigned char lds_raw[L_END];
  LAS unsigned char* L = (LAS unsigned char*)lds_raw;
  const int bid = blockIdx.x, h = bid >> 3, j = bid & 7, b = j >> 2, sp = j & 3;
  const int tid = threadIdx.x, lane = tid & 63, w = __builtin_amdgcn_readfirstlane(tid >> 6), r = lane >> 3, c = lane & 7;
  LAS unsigned* ctr = (LAS unsigned*)(L + L_CT);
  LAS u64* pace = (LAS u64*)(L + L_CT + 8);
  const unsigned ts = now();
  STAMP(0);
  unsigned tag;
  constexpr float C2 = 0.07216878364870322f * 1.4426950408889634f;

  v4u wreg[WL];
  auto load_w = [&]() {
#pragma unroll
    for (int i = 0; i < WL; ++i) {
      const int n = w * WL + i; const bool iswo = n < 30;
      const int id = (iswo ? n : n - 30) * 64 + lane, row = id / 10, cc = id - row * 10;
      const __amdgpu_buffer_rsrc_t rs = __builtin_amdgcn_make_buffer_rsrc((void*)(iswo ? wo : w2), 0, iswo ? H * DH * D * 2 : F * D * 2, RSRC3);
      const int vo = (iswo || id < 480) ? row * (D * 2) + cc * 16 : 0x7ffffff0;
      wreg[i] = __builtin_amdgcn_raw_buffer_load_b128(rs, vo, (iswo ? DH : 48) * h * (D * 2) + 160 * j, AUX_NT);
    }
  };

  if (w < NS) {
    if (tid == 0) { *ctr = NS * P0; *pace = 0ull; }
    barrier();
    const int rbase = ((b * H + h) * SK + sp * 8) * (DH * 2);
    constexpr int USTR = 4 * 8 * DH * 2;
    const int voff = r * (DH * 2) + c * 16;
    v4u Kb[P0][3], Vb[P0][3];
    const __amdgpu_buffer_rsrc_t kr = __builtin_amdgcn_make_buffer_rsrc((void*)kc, 0, KVBYTES, RSRC3), vr = __builtin_amdgcn_make_buffer_rsrc((void*)vc, 0, KVBYTES, RSRC3);
    static_assert(NS * P0 + NS <= NU, "the first ticket of every wave is a real unit");
#define LD(dst, rs, t) { const int so_ = rbase + (t) * USTR; \
    _Pragma("unroll") for (int u = 0; u < 3; ++u) dst[u] = __builtin_amdgcn_raw_buffer_load_b128(rs, voff + u * 128, so_, AUX_##rs); }
#pragma unroll
    for (int i = 0; i < P0; ++i) { LD(Kb[i], kr, w + i * NS) SCHED(); LD(Vb[i], vr, w + i * NS) SCHED(); }
    SCHED();
    STAMP(1);
    barrier();
    STAMP(2);
    tag = lds_ld(ctr + 1);
    v4u qv[3];
#pragma unroll
    for (int u = 0; u < 3; ++u) qv[u] = *(LAS v4u*)(L + L_Q + u * 128 + c * 16);
    float m = -INFINITY, l = 0.f; f32x2 ac2[12];
#pragma unroll
    for (int k = 0; k < 12; ++k) ac2[k] = f32x2{0.f, 0.f};
#define QK(Kx, s) { float d0 = 0.f, d1 = 0.f; \
      _Pragma("unroll") for (int u = 0; u < 3; ++u) { d0 = dot2(Kx[u][0], qv[u][0], d0); d1 = dot2(Kx[u][1], qv[u][1], d1); d0 = dot2(Kx[u][2], qv[u][2], d0); d1 = dot2(Kx[u][3], qv[u][3], d1); } \
      s = d0 + d1; asm volatile("" : "+v"(s)); }
#define PV(Vx, s) { s += dpp<XOR1>(s); s += dpp<XOR2>(s); s += dpp<XOR7>(s); s *= C2; \
      const float mn = fmaxf(m, s), al = __builtin_amdgcn_exp2f(m - mn), p = __builtin_amdgcn_exp2f(s - mn); m = mn; l = fmaf(l, al, p); \
      _Pragma("unroll") for (int u = 0; u < 3; ++u) { SCHED(); _Pragma("unroll") for (int k = 0; k < 4; ++k) \
          ac2[u * 4 + k] = __builtin_elementwise_fma(f32x2{p, p}, f32x2{lo16(Vx[u][k]), hi16(Vx[u][k])}, ac2[u * 4 + k] * al); \
        asm volatile("" : "+v"(ac2[u * 4]), "+v"(ac2[u * 4 + 1]), "+v"(ac2[u * 4 + 2]), "+v"(ac2[u * 4 + 3])); } }
    const unsigned ctra = L_CT + (unsigned)(uintptr_t)L, one = 1u;
#define TICKET(tk) asm volatile("s_mov_b64 exec, 1\n\tds_add_rtn_u32 %0, %1, %2\n\ts_mov_b64 exec, -1" : "=&v"(tk) : "v"(ctra), "v"(one) : "memory")
#define TAKE(tk) ({ asm volatile("s_waitcnt lgkmcnt(0)" : "+v"(tk) :: "memory"); __builtin_amdgcn_readfirstlane(tk); })
    { int tk; TICKET(tk); SCHED();
      float s; QK(Kb[0], s)
      const int cur = TAKE(tk);
      SCHED(); LD(Kb[0], kr, cur) SCHED();
      PV(Vb[0], s)
      SCHED(); LD(Vb[0], vr, cur) SCHED();
    }
#pragma unroll
    for (int i = 1; i < P0; ++i) { float s; QK(Kb[i], s) PV(Vb[i], s) SCHED(); }
    STAMP(3);
    float s;
    for (int it = 0; it < NU; ++it) {
      int tk; TICKET(tk); SCHED();
      QK(Kb[0], s)
      const int nxt = TAKE(tk);
      if ((unsigned)nxt >= (unsigned)NU) break;
#pragma unroll 1
      for (int spn = 0; spn < 256; ++spn) {
        const u64 pv = __hip_atomic_load(pace, RLX_WG);
        const float t0 = asf(__builtin_amdgcn_readfirstlane((unsigned)pv)), tau = asf(__builtin_amdgcn_readfirstlane((unsigned)(pv >> 32)));
        if (fmaf((float)nxt, tau, t0) <= (float)(int)(now() - ts)) break;
        __builtin_amdgcn_s_sleep(2);
      }
      SCHED(); LD(Kb[0], kr, nxt) SCHED();
      PV(Vb[0], s)
      SCHED(); LD(Vb[0], vr, nxt) SCHED();
    }
    load_w();
    SCHED();
    PV(Vb[0], s)
    STAMP(4);
    float M = fmaxf(m, dpp<ROR8>(m)); M = max16(M); M = max32(M);
    const float sc = __builtin_amdgcn_exp2f(m - M);
    l *= sc; l += dpp<ROR8>(l); l = tr16(l, l); l = tr32(l, l);
    float acc[24];
#pragma unroll
    for (int k = 0; k < 12; ++k) { const f32x2 t = ac2[k] * sc; acc[2 * k] = t[0]; acc[2 * k + 1] = t[1]; }
#pragma unroll
    for (int k = 0; k < 12; ++k) acc[k] = tr32(acc[k], acc[k + 12]);
#pragma unroll
    for (int k = 0; k < 6; ++k) acc[k] = tr16(acc[k], acc[k + 6]);
#pragma unroll
    for (int k = 0; k < 3; ++k) acc[k] = tr8(acc[k], acc[k + 3], r & 1);
    LAS float* mg = (LAS float*)(L + L_MG) + w * 200;
#pragma unroll
    for (int k = 0; k < 3; ++k) mg[k * 64 + lane] = acc[k];
    if (lane == 0) { mg[192] = M; mg[193] = l; }
  } else {
    __builtin_amdgcn_s_setprio(3);
    const int a = w - NS;
    const unsigned epoch = __hip_atomic_load((GAS unsigned*)(ws + WS_EPOCH), RLX_AGENT);
    const int xb = lane >> 5, xl = lane & 31;
    const __amdgpu_buffer_rsrc_t xr = __builtin_amdgcn_make_buffer_rsrc((void*)x, 0, NB * D * 2, RSRC3);
    const int xo = xb * (D * 2) + xl * 40;
    const v4u x0 = __builtin_amdgcn_raw_buffer_load_b128(xr, xo, 0, 0), x1 = __builtin_amdgcn_raw_buffer_load_b128(xr, xo + 16, 0, 0);
    const v2u x2 = __builtin_amdgcn_raw_buffer_load_b64(xr, xo + 32, 0, 0);
    v4u wa[10], wb[10];
    { const __amdgpu_buffer_rsrc_t ra = __builtin_amdgcn_make_buffer_rsrc((void*)wq, 0, D * H * DH * 2, RSRC3);
      const __amdgpu_buffer_rsrc_t rb = __builtin_amdgcn_make_buffer_rsrc((void*)(a ? w1 : wq), 0, a ? D * F * 2 : D * H * DH * 2, RSRC3);
      const int sa = H * DH * 2, sb = a ? F * 2 : H * DH * 2;
      const int va = r * sa + c * 16, vb = (a && c >= 6) ? 0x7ffffff0 : r * sb + c * 16;
      const int oa = 80 * j * sa + h * (DH * 2) + a * 256, ob = 80 * j * sb + (a ? h * 96 : h * (DH * 2) + 128);
#pragma unroll
      for (int u = 0; u < 10; ++u) { wa[u] = __builtin_amdgcn_raw_buffer_load_b128(ra, va, oa + u * 8 * sa, AUX_NT); wb[u] = __builtin_amdgcn_raw_buffer_load_b128(rb, vb, ob + u * 8 * sb, AUX_NT); }
    }
    SCHED();
    barrier();
    STAMP(1);
    tag = (unsigned)__builtin_amdgcn_readfirstlane(epoch) * 0x9E3779B1u + 0x7F4A7C15u;
    if (a == 0 && lane == 0) ctr[1] = tag;
    {
      float v[20];
#pragma unroll
      for (int k = 0; k < 4; ++k) { v[2 * k] = lo16(x0[k]); v[2 * k + 1] = hi16(x0[k]); v[8 + 2 * k] = lo16(x1[k]); v[9 + 2 * k] = hi16(x1[k]); }
      v[16] = lo16(x2[0]); v[17] = hi16(x2[0]); v[18] = lo16(x2[1]); v[19] = hi16(x2[1]);
      float s = 0.f;
#pragma unroll
      for (int k = 0; k < 20; ++k) s += v[k];
      s += dpp<ROR1>(s); s += dpp<ROR2>(s); s += dpp<ROR4>(s); s += dpp<ROR8>(s); s = tr16(s, s);
      const float mean = s * (1.f / D);
      float q = 0.f;
#pragma unroll
      for (int k = 0; k < 20; ++k) { const float d = v[k] - mean; q = fmaf(d, d, q); }
      q += dpp<ROR1>(q); q += dpp<ROR2>(q); q += dpp<ROR4>(q); q += dpp<ROR8>(q); q = tr16(q, q);
      const float mb = rbf(mean), sd = rbf(sqrtf(rbf(rbf(q * (1.f / D)) + 1e-5f)));
      LAS float* xn = (LAS float*)(L + L_XN) + xb * D + xl * 20;
#pragma unroll
      for (int k = 0; k < 20; k += 4) *(LAS f32x4*)(xn + k) = f32x4{rbf(rbf(v[k] - mb) / sd), rbf(rbf(v[k + 1] - mb) / sd), rbf(rbf(v[k + 2] - mb) / sd), rbf(rbf(v[k + 3] - mb) / sd)};
    }
    asm volatile("s_waitcnt lgkmcnt(0)" ::: "memory");
    float pa[32];
#pragma unroll
    for (int k = 0; k < 32; ++k) pa[k] = 0.f;
    { const LAS float* xn = (const LAS float*)(L + L_XN) + 80 * j + r;
#pragma unroll
      for (int u = 0; u < 10; ++u) { const float xa = xn[8 * u], xc = xn[D + 8 * u];
#pragma unroll
        for (int k = 0; k < 4; ++k) {
          const float a0 = lo16(wa[u][k]), a1 = hi16(wa[u][k]), b0 = lo16(wb[u][k]), b1v = hi16(wb[u][k]);
          pa[2 * k] = fmaf(xa, a0, pa[2 * k]); pa[2 * k + 1] = fmaf(xa, a1, pa[2 * k + 1]); pa[8 + 2 * k] = fmaf(xa, b0, pa[8 + 2 * k]); pa[9 + 2 * k] = fmaf(xa, b1v, pa[9 + 2 * k]);
          pa[16 + 2 * k] = fmaf(xc, a0, pa[16 + 2 * k]); pa[17 + 2 * k] = fmaf(xc, a1, pa[17 + 2 * k]); pa[24 + 2 * k] = fmaf(xc, b0, pa[24 + 2 * k]); pa[25 + 2 * k] = fmaf(xc, b1v, pa[25 + 2 * k]);
        } } }
#pragma unroll
    for (int k = 0; k < 16; ++k) pa[k] = tr32(pa[k], pa[k + 16]);
#pragma unroll
    for (int k = 0; k < 8; ++k) pa[k] = tr16(pa[k], pa[k + 8]);
#pragma unroll
    for (int k = 0; k < 4; ++k) pa[k] = tr8(pa[k], pa[k + 4], r & 1);
    STAMP(2);
    { gu64* qx = (gu64*)(ws + WS_QX) + bid * 512 + (lane >> 5) * 256 + (2 * a + ((lane >> 4) & 1)) * 64 + (lane & 15);
#pragma unroll
      for (int t = 0; t < 4; ++t) put(qx + t * 16, tag, pa[t]); }
    const int col = (lane & 7) * 8 + ((lane >> 3) & 1) * 4 + (lane >> 4);
    if (a == 0) {
      const gu64* qx = (const gu64*)(ws + WS_QX) + (h * 8) * 512 + b * 256 + lane;
      float qs[3];
      for (;;) { bool ok = true; u64 g[24];
#pragma unroll
        for (int p = 0; p < 8; ++p)
#pragma unroll
          for (int u = 0; u < 3; ++u) g[p * 3 + u] = get(qx + p * 512 + u * 64);
        qs[0] = qs[1] = qs[2] = 0.f;
#pragma unroll
        for (int p = 0; p < 8; ++p)
#pragma unroll
          for (int u = 0; u < 3; ++u) { ok &= (unsigned)(g[p * 3 + u] >> 32) == tag; qs[u] += asf((unsigned)g[p * 3 + u]); }
        if (__all(ok) || now() - ts > SPIN_TICKS) break;
      }
#pragma unroll
      for (int u = 0; u < 3; ++u) *(LAS u16*)(L + L_Q + (u * 64 + col) * 2) = f2bf(qs[u]);
    }
    STAMP(3);
    barrier();
    __builtin_amdgcn_s_setprio(0);
    if (a == 0) {
      const gu64* qx = (const gu64*)(ws + WS_QX) + (h * 8) * 512 + 192 + lane;
      const bool live = (lane & 7) < 6;
      const float bias = bf2f(b1[48 * h + (live ? col : 0)]);
      float hs[2];
      for (;;) { bool ok = true; u64 g[16];
#pragma unroll
        for (int p = 0; p < 8; ++p) { g[2 * p] = get(qx + p * 512); g[2 * p + 1] = get(qx + p * 512 + 256); }
        hs[0] = hs[1] = 0.f;
#pragma unroll
        for (int p = 0; p < 16; ++p) { ok &= (unsigned)(g[p] >> 32) == tag; hs[p & 1] += asf((unsigned)g[p]); }
        if (__all(ok) || now() - ts > SPIN_TICKS) break;
        __builtin_amdgcn_s_sleep(16);
      }
      if (live) {
#pragma unroll
        for (int u = 0; u < 2; ++u) { float t = rbf(rbf(hs[u]) + bias); t = t > 0.f ? t : 0.f; *(LAS u16*)(L + L_HA + (u * 48 + col) * 2) = f2bf(t * t); } }
    }
    else {
      constexpr int M0 = NS * P0 + 12, DM = 48, RD = 16;
      for (int rd = 0; M0 + rd * DM + RD < NU - 8; ++rd) {
        const unsigned Mi = M0 + rd * DM;
        gu64* cl = (gu64*)(ws + WS_CT) + rd * 544;
        while (lds_ld(ctr) < Mi && now() - ts < SPIN_TICKS) __builtin_amdgcn_s_sleep(8);
        if (lane == 0) __hip_atomic_store(cl + bid, ((u64)tag << 32) | now(), RLX_AGENT);
        while (lds_ld(ctr) < Mi + RD && now() - ts < SPIN_TICKS) __builtin_amdgcn_s_sleep(8);
        float s = 0.f; int n = 0;
#pragma unroll
        for (int k = 0; k < 4; ++k) { const u64 v = get(cl + lane + 64 * k); const bool ok = (unsigned)(v >> 32) == tag;
          s += ok ? (float)(int)((unsigned)v - ts) : 0.f; n += __builtin_popcountll(__ballot(ok)); }
        s += dpp<ROR1>(s); s += dpp<ROR2>(s); s += dpp<ROR4>(s); s += dpp<ROR8>(s); s = tr16(s, s); s = tr32(s, s);
        const float tm = s / (float)n, tau = tm / ((float)Mi + 6.25f);
        if (lane == 0 && n > 0) __hip_atomic_store(pace, ((u64)asu(tau) << 32) | asu(tm - (float)Mi * tau), RLX_WG);
      }
    }
    while (lds_ld(ctr) < NU && now() - ts < SPIN_TICKS) __builtin_amdgcn_s_sleep(4);
    load_w();
    SCHED();
  }
  const int oi = 5 * h + (w < 5 ? w : 0), ob = oi / 80, ocol = 80 * j + oi - ob * 80;
  const float xres = bf2f(x[ob * D + ocol]), b2v = bf2f(b2[ocol]);
  barrier();
  STAMP(5);
  if (w < 3) {
    const LAS float* mg = (const LAS float*)(L + L_MG);
    float MM = -INFINITY;
#pragma unroll
    for (int i = 0; i < NS; ++i) MM = fmaxf(MM, mg[i * 200 + 192]);
    float Ls = 0.f, o = 0.f;
#pragma unroll
    for (int i = 0; i < NS; ++i) { const float e = __builtin_amdgcn_exp2f(mg[i * 200 + 192] - MM); Ls = fmaf(e, mg[i * 200 + 193], Ls); o = fmaf(e, mg[i * 200 + tid], o); }
    gu64* px = (gu64*)(ws + WS_PX) + bid * 256;
    put(px + tid, tag, o / Ls);
    if (tid == 0) put(px + 192, tag, MM + __builtin_amdgcn_logf(Ls));
  }
  STAMP(6);
#pragma unroll
  for (int i = 0; i < WL; ++i) { const int n = w * WL + i; if (n * 64 + lane < 2400) *(LAS v4u*)(L + L_WO + (n * 64 + lane) * 16) = wreg[i]; }
  barrier();
  STAMP(7);
  v4s Bf[15];
  if (w < 5) {
    const LAS unsigned char* base = L + L_WO + (((lane & 15) >> 2) + 8 * (lane >> 4)) * 160 + w * 32 + (lane & 3) * 8;
#pragma unroll
    for (int k = 0; k < 7; ++k) {
      Bf[2 * k] = __builtin_amdgcn_ds_read_tr16_b64_v4i16((LAS v4s*)(base + k * 32 * 160));
      Bf[2 * k + 1] = __builtin_amdgcn_ds_read_tr16_b64_v4i16((LAS v4s*)(base + k * 32 * 160 + 4 * 160));
    }
    Bf[14] = __builtin_amdgcn_ds_read_tr16_b64_v4i16((LAS v4s*)(base + 7 * 32 * 160 - 4 * (lane >> 4) * 160));
  }
  if (w < 6) {
    const int bb = w >= 3, pos = tid - bb * 192;
    const gu64* px = (const gu64*)(ws + WS_PX) + (h * 8 + bb * 4) * 256;
    u64 g[8];
    for (;;) { bool ok = true;
#pragma unroll
      for (int s = 0; s < 4; ++s) { g[s] = get(px + s * 256 + pos); g[4 + s] = get(px + s * 256 + 192); }
#pragma unroll
      for (int s = 0; s < 8; ++s) ok &= (unsigned)(g[s] >> 32) == tag;
      if (__all(ok) || now() - ts > SPIN_TICKS) break;
    }
    const float e0 = asf((unsigned)g[4]), e1 = asf((unsigned)g[5]), e2 = asf((unsigned)g[6]), e3 = asf((unsigned)g[7]);
    const float em = fmaxf(fmaxf(e0, e1), fmaxf(e2, e3));
    const float w0 = __builtin_amdgcn_exp2f(e0 - em), w1v = __builtin_amdgcn_exp2f(e1 - em), w2v = __builtin_amdgcn_exp2f(e2 - em), w3 = __builtin_amdgcn_exp2f(e3 - em);
    const float o = (w0 * asf((unsigned)g[0]) + w1v * asf((unsigned)g[1]) + w2v * asf((unsigned)g[2]) + w3 * asf((unsigned)g[3])) / (w0 + w1v + w2v + w3);
    const int i = (lane >> 5) * 12 + ((lane >> 4) & 1) * 6 + ((lane >> 3) & 1) * 3 + (w - 3 * bb);
    *(LAS u16*)(L + L_OA + (bb * DH + (i >> 3) * 64 + c * 8 + (i & 7)) * 2) = f2bf(o);
  }
  STAMP(8);
  barrier();
  if (w < 5) {
    f32x4 S = {0.f, 0.f, 0.f, 0.f}, G = {0.f, 0.f, 0.f, 0.f};
    const LAS unsigned char* ao = L + L_OA + (lane & 1) * (DH * 2) + (lane >> 4) * 16;
#pragma unroll
    for (int k = 0; k < 6; ++k) {
      const v8s bf = __builtin_shufflevector(Bf[2 * k], Bf[2 * k + 1], 0, 1, 2, 3, 4, 5, 6, 7);
      S = __builtin_amdgcn_mfma_f32_16x16x32_bf16(*(const LAS v8bf*)(ao + k * 64), __builtin_bit_cast(v8bf, bf), S, 0, 0, 0);
    }
    { const LAS unsigned char* ah = L + L_HA + (lane & 1) * 96;
      const v8s bf = __builtin_shufflevector(Bf[12], Bf[13], 0, 1, 2, 3, 4, 5, 6, 7);
      G = __builtin_amdgcn_mfma_f32_16x16x32_bf16(*(const LAS v8bf*)(ah + (lane >> 4) * 16), __builtin_bit_cast(v8bf, bf), G, 0, 0, 0);
      G = __builtin_amdgcn_mfma_f32_16x16x16bf16_1k(*(const LAS v4s*)(ah + 64 + (lane >> 4) * 8), Bf[14], G, 0, 0, 0);
    }
    const int gq = lane >> 4, vb = gq & 1, which = gq >> 1;
    const float val = which ? (vb ? G[1] : G[0]) : (vb ? S[1] : S[0]);
    const int i = vb * 80 + 16 * w + (lane & 15), hp = i / 5;
    put((gu64*)(ws + WS_RX) + ((j * 32 + hp) * 32 + h) * 10 + which * 5 + (i - hp * 5), tag, val);
    STAMP(9);
    const gu64* rx = (const gu64*)(ws + WS_RX) + ((j * 32 + h) * 32 + (lane >> 1)) * 10 + (lane & 1) * 5 + w;
    u64 g;
    for (;;) { g = get(rx); if (__all((unsigned)(g >> 32) == tag) || now() - ts > SPIN_TICKS) break; }
    float s = asf((unsigned)g);
    s += dpp<ROR2>(s); s += dpp<ROR4>(s); s += dpp<ROR8>(s); s = tr16(s, s); s = tr32(s, s);
    const float t = rbf(rbf(dpp<XOR1>(s)) + b2v);
    const float gate = rbf(t / (1.f + __expf(-t)));
    if (lane == 0) out[ob * D + ocol] = f2bf(xres + rbf(rbf(s) * gate));
    if (bid == 0 && tid == 0) __hip_atomic_store((GAS unsigned*)(ws + WS_EPOCH), tag, RLX_AGENT);
    STAMP(10);
  }
}

extern "C" void kernel_launch(void* const* d_in, const int* in_sizes, int n_in, void* d_out, int out_size, void* d_ws, size_t ws_size, hipStream_t stream) {
  fused_decode<<<256, NTHR, 0, stream>>>((const u16*)d_in[0], (const u16*)d_in[1], (const u16*)d_in[2], (const u16*)d_in[3], (const u16*)d_in[4],
                                         (const u16*)d_in[6], (const u16*)d_in[7], (const u16*)d_in[8], (const u16*)d_in[9], (u16*)d_out, (unsigned char*)d_ws);
}
```

```cpp
#include <hip/hip_runtime.h>
#include <hip/hip_bf16.h>
#include <stdint.h>

#define NS 6
#define P0 5
#define PACE 1
#define XCDGROUP 0
#define WPACE 7
#define PEPS 5
#define POFF 300
#define PM0 56
#define PDM 48
#define GD 1
#define GG 5
#define WTRIG 0
#define PRETOUCH 1
#define QPRE 0
#define PCL 64
#define PD0 6
#define PD1 6
#define PD2 16
#define GD2 GD
#define PWIN 0
#define PT0 1300
#define PDT 900
#define PLAG 130
#define CODEWARM 0
#define TLBWARM 0
#define STAMPS 0

#define GAS __attribute__((address_space(1)))
#define LAS __attribute__((address_space(3)))
#define RLX_AGENT __ATOMIC_RELAXED, __HIP_MEMORY_SCOPE_AGENT
#define RLX_WG __ATOMIC_RELAXED, __HIP_MEMORY_SCOPE_WORKGROUP
typedef unsigned v4u __attribute__((ext_vector_type(4)));
typedef unsigned v2u __attribute__((ext_vector_type(2)));
typedef short v4s __attribute__((ext_vector_type(4)));
typedef short v8s __attribute__((ext_vector_type(8)));
typedef __bf16 v8bf __attribute__((ext_vector_type(8)));
typedef float f32x4 __attribute__((ext_vector_type(4)));
typedef float f32x2 __attribute__((ext_vector_type(2)));
typedef __bf16 bf16x2 __attribute__((ext_vector_type(2)));
typedef unsigned short u16;
typedef unsigned long long u64;
typedef GAS u64 gu64;

constexpr int NB = 2, D = 640, H = 32, SK = 8192, DH = 192, F = 1536;
constexpr int NWAVE = NS + 2, NTHR = NWAVE * 64;
constexpr int NU = 256;
constexpr int KVBYTES = NB * H * SK * DH * 2;
constexpr int AUX_NT = 2;
#define KAUX 2
#define VAUX 2
constexpr int AUX_kr = KAUX, AUX_vr = VAUX;
constexpr int RSRC3 = 0x00020000;
constexpr size_t WS_EPOCH = 0;
constexpr size_t WS_QX = 4096;
constexpr size_t WS_PX = WS_QX + 256 * 512 * 8;
constexpr size_t WS_RX = WS_PX + 256 * 256 * 8;
constexpr size_t WS_CT = WS_RX + 8 * 32 * 320 * 8;
constexpr size_t WS_ST = WS_CT + 8 * 544 * 8;
constexpr int L_XN = 0;
constexpr int L_Q = L_XN + 4 * 96 * 4;
constexpr int L_HA = L_Q + 384;
constexpr int L_OA = L_HA + 192;
constexpr int L_CT = L_OA + 768;
constexpr int L_MG = L_CT + 32;
constexpr int L_WO = L_MG + NS * 800;
constexpr int L_TL = L_WO + 240 * 160;
constexpr int L_END = L_TL + (STAMPS ? 1024 : 0);
static_assert(L_WO % 16 == 0 && L_OA % 16 == 0 && L_HA % 16 == 0 && L_Q % 16 == 0 && L_CT % 16 == 0, "alignment");
constexpr unsigned SPIN_TICKS = 2000000u;

__device__ __forceinline__ float bf2f(u16 b) { return __builtin_bit_cast(float, (unsigned)b << 16); }
__device__ __forceinline__ u16 f2bf(float f) { return __builtin_bit_cast(u16, (__bf16)f); }
__device__ __forceinline__ float rbf(float f) { return bf2f(f2bf(f)); }
__device__ __forceinline__ float lo16(unsigned v) { return __builtin_bit_cast(float, v << 16); }
__device__ __forceinline__ float hi16(unsigned v) { return __builtin_bit_cast(float, v & 0xffff0000u); }
__device__ __forceinline__ float asf(unsigned v) { return __builtin_bit_cast(float, v); }
__device__ __forceinline__ unsigned asu(float v) { return __builtin_bit_cast(unsigned, v); }
__device__ __forceinline__ float dot2(unsigned k, unsigned q, float acc) {
  return __builtin_amdgcn_fdot2_f32_bf16(__builtin_bit_cast(bf16x2, k), __builtin_bit_cast(bf16x2, q), acc, false);
}
template <int CTRL> __device__ __forceinline__ float dpp(float x) {
  return __builtin_bit_cast(float, __builtin_amdgcn_mov_dpp(__builtin_bit_cast(int, x), CTRL, 0xf, 0xf, true));
}
constexpr int XOR1 = 0xB1, XOR2 = 0x4E, XOR7 = 0x141, ROR8 = 0x128, ROR4 = 0x124, ROR2 = 0x122, ROR1 = 0x121;
__device__ __forceinline__ float tr32(float x, float y) {
  auto s = __builtin_amdgcn_permlane32_swap(asu(x), asu(y), false, false); return asf(s[0]) + asf(s[1]);
}
__device__ __forceinline__ float tr16(float x, float y) {
  auto s = __builtin_amdgcn_permlane16_swap(asu(x), asu(y), false, false); return asf(s[0]) + asf(s[1]);
}
__device__ __forceinline__ float tr8(float x, float y, bool up) {
  const float keep = up ? y : x, send = up ? x : y; return keep + dpp<ROR8>(send);
}
__device__ __forceinline__ float max16(float x) { auto s = __builtin_amdgcn_permlane16_swap(asu(x), asu(x), false, false); return fmaxf(asf(s[0]), asf(s[1])); }
__device__ __forceinline__ float max32(float x) { auto s = __builtin_amdgcn_permlane32_swap(asu(x), asu(x), false, false); return fmaxf(asf(s[0]), asf(s[1])); }
__device__ __forceinline__ unsigned now() { return (unsigned)__builtin_amdgcn_s_memrealtime(); }
__device__ __forceinline__ void barrier() { asm volatile("s_waitcnt lgkmcnt(0)\n\ts_barrier" ::: "memory"); }
__device__ __forceinline__ void put(gu64* g, unsigned tag, float v) { __hip_atomic_store(g, ((u64)tag << 32) | asu(v), RLX_AGENT); }
__device__ __forceinline__ u64 get(const gu64* g) { return __hip_atomic_load(g, RLX_AGENT); }
__device__ __forceinline__ unsigned lds_ld(LAS unsigned* p) { return __builtin_amdgcn_readfirstlane(__hip_atomic_load(p, RLX_WG)); }
constexpr int OOB = 0x7ffffff0, AUX_SC1 = 16;
template <int SLEEP, int N> __device__ __forceinline__ void gather(unsigned (&val)[N], int (&off)[N], __amdgpu_buffer_rsrc_t rs, unsigned tag, unsigned ts) {
#pragma unroll
  for (int i = 0; i < N; ++i) val[i] = 0u;
  for (;;) {
    v2u in[N];
    asm volatile("" ::: "memory");
#pragma unroll
    for (int i = 0; i < N; ++i) in[i] = __builtin_amdgcn_raw_buffer_load_b64(rs, off[i], 0, AUX_SC1);
    int left = OOB;
#pragma unroll
    for (int i = 0; i < N; ++i) { const bool got = in[i][1] == tag; val[i] = got ? in[i][0] : val[i]; off[i] = got ? OOB : off[i]; left = min(left, off[i]); }
    if (__all(left == OOB) || now() - ts > SPIN_TICKS) break;
    if (SLEEP) __builtin_amdgcn_s_sleep(SLEEP);
  }
}
template <int DEPTH, int GAP> __device__ __forceinline__ void gather8(unsigned (&val)[8], int (&off)[8], __amdgpu_buffer_rsrc_t rs, unsigned tag, unsigned ts) {
#pragma unroll
  for (int i = 0; i < 8; ++i) val[i] = 0u;
  unsigned t0, t1; u64 m; const int oob = OOB;
  if constexpr (DEPTH == 1) {
    asm volatile(
      "buffer_load_dwordx2 v[208:209], %8, %[rs], 0 offen sc1\n\t"
      "buffer_load_dwordx2 v[210:211], %9, %[rs], 0 offen sc1\n\t"
      "buffer_load_dwordx2 v[212:213], %10, %[rs], 0 offen sc1\n\t"
      "buffer_load_dwordx2 v[214:215], %11, %[rs], 0 offen sc1\n\t"
      "buffer_load_dwordx2 v[216:217], %12, %[rs], 0 offen sc1\n\t"
      "buffer_load_dwordx2 v[218:219], %13, %[rs], 0 offen sc1\n\t"
      "buffer_load_dwordx2 v[220:221], %14, %[rs], 0 offen sc1\n\t"
      "buffer_load_dwordx2 v[222:223], %15, %[rs], 0 offen sc1\n\t"
      ".Lgloop%=:\n\t"
      "s_waitcnt vmcnt(0)\n\t"
      "v_cmp_eq_u32_e32 vcc, %[tag], v209\n\tv_cmp_eq_u32_e64 %[m], %[tag], v211\n\ts_nop 0\n\t"
      "v_cndmask_b32_e32 %8, %8, %[oob], vcc\n\tv_cndmask_b32_e64 %9, %9, %[oob], %[m]\n\tv_cndmask_b32_e32 %0, %0, v208, vcc\n\tv_cndmask_b32_e64 %1, %1, v210, %[m]\n\t"
      "v_cmp_eq_u32_e32 vcc, %[tag], v213\n\tv_cmp_eq_u32_e64 %[m], %[tag], v215\n\ts_nop 0\n\t"
      "v_cndmask_b32_e32 %10, %10, %[oob], vcc\n\tv_cndmask_b32_e64 %11, %11, %[oob], %[m]\n\tv_cndmask_b32_e32 %2, %2, v212, vcc\n\tv_cndmask_b32_e64 %3, %3, v214, %[m]\n\t"
      "v_cmp_eq_u32_e32 vcc, %[tag], v217\n\tv_cmp_eq_u32_e64 %[m], %[tag], v219\n\ts_nop 0\n\t"
      "v_cndmask_b32_e32 %12, %12, %[oob], vcc\n\tv_cndmask_b32_e64 %13, %13, %[oob], %[m]\n\tv_cndmask_b32_e32 %4, %4, v216, vcc\n\tv_cndmask_b32_e64 %5, %5, v218, %[m]\n\t"
      "v_cmp_eq_u32_e32 vcc, %[tag], v221\n\tv_cmp_eq_u32_e64 %[m], %[tag], v223\n\ts_nop 0\n\t"
      "v_cndmask_b32_e32 %14, %14, %[oob], vcc\n\tv_cndmask_b32_e64 %15, %15, %[oob], %[m]\n\tv_cndmask_b32_e32 %6, %6, v220, vcc\n\tv_cndmask_b32_e64 %7, %7, v222, %[m]\n\t"
      "v_min3_u32 %[t0], %8, %9, %10\n\tv_min3_u32 %[t1], %11, %12, %13\n\tv_min3_u32 %[t0], %[t0], %14, %15\n\tv_min_u32_e32 %[t0], %[t0], %[t1]\n\t"
      "v_cmp_ne_u32_e32 vcc, %[t0], %[oob]\n\ts_nop 1\n\ts_cbranch_vccz .Lgdone%=\n\t"
      "buffer_load_dwordx2 v[208:209], %8, %[rs], 0 offen sc1\n\t"
      "buffer_load_dwordx2 v[210:211], %9, %[rs], 0 offen sc1\n\t"
      "buffer_load_dwordx2 v[212:213], %10, %[rs], 0 offen sc1\n\t"
      "buffer_load_dwordx2 v[214:215], %11, %[rs], 0 offen sc1\n\t"
      "buffer_load_dwordx2 v[216:217], %12, %[rs], 0 offen sc1\n\t"
      "buffer_load_dwordx2 v[218:219], %13, %[rs], 0 offen sc1\n\t"
      "buffer_load_dwordx2 v[220:221], %14, %[rs], 0 offen sc1\n\t"
      "buffer_load_dwordx2 v[222:223], %15, %[rs], 0 offen sc1\n\t"
      "s_memrealtime vcc\n\ts_waitcnt lgkmcnt(0)\n\ts_sub_u32 vcc_lo, vcc_lo, %[ts]\n\ts_cmp_lt_u32 vcc_lo, %[lim]\n\ts_cbranch_scc1 .Lgloop%=\n"
      ".Lgdone%=:"
      : "+v"(val[0]), "+v"(val[1]), "+v"(val[2]), "+v"(val[3]), "+v"(val[4]), "+v"(val[5]), "+v"(val[6]), "+v"(val[7]),
        "+v"(off[0]), "+v"(off[1]), "+v"(off[2]), "+v"(off[3]), "+v"(off[4]), "+v"(off[5]), "+v"(off[6]), "+v"(off[7]),
        [t0] "=&v"(t0), [t1] "=&v"(t1), [m] "=&s"(m)
      : [rs] "s"(rs), [tag] "s"(tag), [oob] "v"(oob), [ts] "s"(ts), [lim] "s"(SPIN_TICKS), [gap] "n"(GAP)
      : "memory", "vcc", "scc", "v208", "v209", "v210", "v211", "v212", "v213", "v214", "v215", "v216", "v217", "v218", "v219", "v220", "v221", "v222", "v223");
  } else if constexpr (DEPTH == 2) {
    asm volatile(
      "buffer_load_dwordx2 v[208:209], %8, %[rs], 0 offen sc1\n\t"
      "buffer_load_dwordx2 v[210:211], %9, %[rs], 0 offen sc1\n\t"
      "buffer_load_dwordx2 v[212:213], %10, %[rs], 0 offen sc1\n\t"
      "buffer_load_dwordx2 v[214:215], %11, %[rs], 0 offen sc1\n\t"
      "buffer_load_dwordx2 v[216:217], %12, %[rs], 0 offen sc1\n\t"
      "buffer_load_dwordx2 v[218:219], %13, %[rs], 0 offen sc1\n\t"
      "buffer_load_dwordx2 v[220:221], %14, %[rs], 0 offen sc1\n\t"
      "buffer_load_dwordx2 v[222:223], %15, %[rs], 0 offen sc1\n\t"
      "s_sleep %[gap]\n\t"
      "buffer_load_dwordx2 v[224:225], %8, %[rs], 0 offen sc1\n\t"
      "buffer_load_dwordx2 v[226:227], %9, %[rs], 0 offen sc1\n\t"
      "buffer_load_dwordx2 v[228:229], %10, %[rs], 0 offen sc1\n\t"
      "buffer_load_dwordx2 v[230:231], %11, %[rs], 0 offen sc1\n\t"
      "buffer_load_dwordx2 v[232:233], %12, %[rs], 0 offen sc1\n\t"
      "buffer_load_dwordx2 v[234:235], %13, %[rs], 0 offen sc1\n\t"
      "buffer_load_dwordx2 v[236:237], %14, %[rs], 0 offen sc1\n\t"
      "buffer_load_dwordx2 v[238:239], %15, %[rs], 0 offen sc1\n\t"
      ".Lgloop%=:\n\t"
      "s_waitcnt vmcnt(8)\n\t"
      "v_cmp_eq_u32_e32 vcc, %[tag], v209\n\tv_cmp_eq_u32_e64 %[m], %[tag], v211\n\ts_nop 0\n\t"
      "v_cndmask_b32_e32 %8, %8, %[oob], vcc\n\tv_cndmask_b32_e64 %9, %9, %[oob], %[m]\n\tv_cndmask_b32_e32 %0, %0, v208, vcc\n\tv_cndmask_b32_e64 %1, %1, v210, %[m]\n\t"
      "v_cmp_eq_u32_e32 vcc, %[tag], v213\n\tv_cmp_eq_u32_e64 %[m], %[tag], v215\n\ts_nop 0\n\t"
      "v_cndmask_b32_e32 %10, %10, %[oob], vcc\n\tv_cndmask_b32_e64 %11, %11, %[oob], %[m]\n\tv_cndmask_b32_e32 %2, %2, v212, vcc\n\tv_cndmask_b32_e64 %3, %3, v214, %[m]\n\t"
      "v_cmp_eq_u32_e32 vcc, %[tag], v217\n\tv_cmp_eq_u32_e64 %[m], %[tag], v219\n\ts_nop 0\n\t"
      "v_cndmask_b32_e32 %12, %12, %[oob], vcc\n\tv_cndmask_b32_e64 %13, %13, %[oob], %[m]\n\tv_cndmask_b32_e32 %4, %4, v216, vcc\n\tv_cndmask_b32_e64 %5, %5, v218, %[m]\n\t"
      "v_cmp_eq_u32_e32 vcc, %[tag], v221\n\tv_cmp_eq_u32_e64 %[m], %[tag], v223\n\ts_nop 0\n\t"
      "v_cndmask_b32_e32 %14, %14, %[oob], vcc\n\tv_cndmask_b32_e64 %15, %15, %[oob], %[m]\n\tv_cndmask_b32_e32 %6, %6, v220, vcc\n\tv_cndmask_b32_e64 %7, %7, v222, %[m]\n\t"
      "v_min3_u32 %[t0], %8, %9, %10\n\tv_min3_u32 %[t1], %11, %12, %13\n\tv_min3_u32 %[t0], %[t0], %14, %15\n\tv_min_u32_e32 %[t0], %[t0], %[t1]\n\t"
      "v_cmp_ne_u32_e32 vcc, %[t0], %[oob]\n\ts_nop 1\n\ts_cbranch_vccz .Lgdone%=\n\t"
      "buffer_load_dwordx2 v[208:209], %8, %[rs], 0 offen sc1\n\t"
      "buffer_load_dwordx2 v[210:211], %9, %[rs], 0 offen sc1\n\t"
      "buffer_load_dwordx2 v[212:213], %10, %[rs], 0 offen sc1\n\t"
      "buffer_load_dwordx2 v[214:215], %11, %[rs], 0 offen sc1\n\t"
      "buffer_load_dwordx2 v[216:217], %12, %[rs], 0 offen sc1\n\t"
      "buffer_load_dwordx2 v[218:219], %13, %[rs], 0 offen sc1\n\t"
      "buffer_load_dwordx2 v[220:221], %14, %[rs], 0 offen sc1\n\t"
      "buffer_load_dwordx2 v[222:223], %15, %[rs], 0 offen sc1\n\t"
      "s_waitcnt vmcnt(8)\n\t"
      "v_cmp_eq_u32_e32 vcc, %[tag], v225\n\tv_cmp_eq_u32_e64 %[m], %[tag], v227\n\ts_nop 0\n\t"
      "v_cndmask_b32_e32 %8, %8, %[oob], vcc\n\tv_cndmask_b32_e64 %9, %9, %[oob], %[m]\n\tv_cndmask_b32_e32 %0, %0, v224, vcc\n\tv_cndmask_b32_e64 %1, %1, v226, %[m]\n\t"
      "v_cmp_eq_u32_e32 vcc, %[tag], v229\n\tv_cmp_eq_u32_e64 %[m], %[tag], v231\n\ts_nop 0\n\t"
      "v_cndmask_b32_e32 %10, %10, %[oob], vcc\n\tv_cndmask_b32_e64 %11, %11, %[oob], %[m]\n\tv_cndmask_b32_e32 %2, %2, v228, vcc\n\tv_cndmask_b32_e64 %3, %3, v230, %[m]\n\t"
      "v_cmp_eq_u32_e32 vcc, %[tag], v233\n\tv_cmp_eq_u32_e64 %[m], %[tag], v235\n\ts_nop 0\n\t"
      "v_cndmask_b32_e32 %12, %12, %[oob], vcc\n\tv_cndmask_b32_e64 %13, %13, %[oob], %[m]\n\tv_cndmask_b32_e32 %4, %4, v232, vcc\n\tv_cndmask_b32_e64 %5, %5, v234, %[m]\n\t"
      "v_cmp_eq_u32_e32 vcc, %[tag], v237\n\tv_cmp_eq_u32_e64 %[m], %[tag], v239\n\ts_nop 0\n\t"
      "v_cndmask_b32_e32 %14, %14, %[oob], vcc\n\tv_cndmask_b32_e64 %15, %15, %[oob], %[m]\n\tv_cndmask_b32_e32 %6, %6, v236, vcc\n\tv_cndmask_b32_e64 %7, %7, v238, %[m]\n\t"
      "v_min3_u32 %[t0], %8, %9, %10\n\tv_min3_u32 %[t1], %11, %12, %13\n\tv_min3_u32 %[t0], %[t0], %14, %15\n\tv_min_u32_e32 %[t0], %[t0], %[t1]\n\t"
      "v_cmp_ne_u32_e32 vcc, %[t0], %[oob]\n\ts_nop 1\n\ts_cbranch_vccz .Lgdone%=\n\t"
      "buffer_load_dwordx2 v[224:225], %8, %[rs], 0 offen sc1\n\t"
      "buffer_load_dwordx2 v[226:227], %9, %[rs], 0 offen sc1\n\t"
      "buffer_load_dwordx2 v[228:229], %10, %[rs], 0 offen sc1\n\t"
      "buffer_load_dwordx2 v[230:231], %11, %[rs], 0 offen sc1\n\t"
      "buffer_load_dwordx2 v[232:233], %12, %[rs], 0 offen sc1\n\t"
      "buffer_load_dwordx2 v[234:235], %13, %[rs], 0 offen sc1\n\t"
      "buffer_load_dwordx2 v[236:237], %14, %[rs], 0 offen sc1\n\t"
      "buffer_load_dwordx2 v[238:239], %15, %[rs], 0 offen sc1\n\t"
      "s_memrealtime vcc\n\ts_waitcnt lgkmcnt(0)\n\ts_sub_u32 vcc_lo, vcc_lo, %[ts]\n\ts_cmp_lt_u32 vcc_lo, %[lim]\n\ts_cbranch_scc1 .Lgloop%=\n"
      ".Lgdone%=:"
      : "+v"(val[0]), "+v"(val[1]), "+v"(val[2]), "+v"(val[3]), "+v"(val[4]), "+v"(val[5]), "+v"(val[6]), "+v"(val[7]),
        "+v"(off[0]), "+v"(off[1]), "+v"(off[2]), "+v"(off[3]), "+v"(off[4]), "+v"(off[5]), "+v"(off[6]), "+v"(off[7]),
        [t0] "=&v"(t0), [t1] "=&v"(t1), [m] "=&s"(m)
      : [rs] "s"(rs), [tag] "s"(tag), [oob] "v"(oob), [ts] "s"(ts), [lim] "s"(SPIN_TICKS), [gap] "n"(GAP)
      : "memory", "vcc", "scc", "v208", "v209", "v210", "v211", "v212", "v213", "v214", "v215", "v216", "v217", "v218", "v219", "v220", "v221", "v222", "v223", "v224", "v225", "v226", "v227", "v228", "v229", "v230", "v231", "v232", "v233", "v234", "v235", "v236", "v237", "v238", "v239");
  } else {
    asm volatile(
      "buffer_load_dwordx2 v[208:209], %8, %[rs], 0 offen sc1\n\t"
      "buffer_load_dwordx2 v[210:211], %9, %[rs], 0 offen sc1\n\t"
      "buffer_load_dwordx2 v[212:213], %10, %[rs], 0 offen sc1\n\t"
      "buffer_load_dwordx2 v[214:215], %11, %[rs], 0 offen sc1\n\t"
      "buffer_load_dwordx2 v[216:217], %12, %[rs], 0 offen sc1\n\t"
      "buffer_load_dwordx2 v[218:219], %13, %[rs], 0 offen sc1\n\t"
      "buffer_load_dwordx2 v[220:221], %14, %[rs], 0 offen sc1\n\t"
      "buffer_load_dwordx2 v[222:223], %15, %[rs], 0 offen sc1\n\t"
      "s_sleep %[gap]\n\t"
      "buffer_load_dwordx2 v[224:225], %8, %[rs], 0 offen sc1\n\t"
      "buffer_load_dwordx2 v[226:227], %9, %[rs], 0 offen sc1\n\t"
      "buffer_load_dwordx2 v[228:229], %10, %[rs], 0 offen sc1\n\t"
      "buffer_load_dwordx2 v[230:231], %11, %[rs], 0 offen sc1\n\t"
      "buffer_load_dwordx2 v[232:233], %12, %[rs], 0 offen sc1\n\t"
      "buffer_load_dwordx2 v[234:235], %13, %[rs], 0 offen sc1\n\t"
      "buffer_load_dwordx2 v[236:237], %14, %[rs], 0 offen sc1\n\t"
      "buffer_load_dwordx2 v[238:239], %15, %[rs], 0 offen sc1\n\t"
      "s_sleep %[gap]\n\t"
      "buffer_load_dwordx2 v[240:241], %8, %[rs], 0 offen sc1\n\t"
      "buffer_load_dwordx2 v[242:243], %9, %[rs], 0 offen sc1\n\t"
      "buffer_load_dwordx2 v[244:245], %10, %[rs], 0 offen sc1\n\t"
      "buffer_load_dwordx2 v[246:247], %11, %[rs], 0 offen sc1\n\t"
      "buffer_load_dwordx2 v[248:249], %12, %[rs], 0 offen sc1\n\t"
      "buffer_load_dwordx2 v[250:251], %13, %[rs], 0 offen sc1\n\t"
      "buffer_load_dwordx2 v[252:253], %14, %[rs], 0 offen sc1\n\t"
      "buffer_load_dwordx2 v[254:255], %15, %[rs], 0 offen sc1\n\t"
      ".Lgloop%=:\n\t"
      "s_waitcnt vmcnt(16)\n\t"
      "v_cmp_eq_u32_e32 vcc, %[tag], v209\n\tv_cmp_eq_u32_e64 %[m], %[tag], v211\n\ts_nop 0\n\t"
      "v_cndmask_b32_e32 %8, %8, %[oob], vcc\n\tv_cndmask_b32_e64 %9, %9, %[oob], %[m]\n\tv_cndmask_b32_e32 %0, %0, v208, vcc\n\tv_cndmask_b32_e64 %1, %1, v210, %[m]\n\t"
      "v_cmp_eq_u32_e32 vcc, %[tag], v213\n\tv_cmp_eq_u32_e64 %[m], %[tag], v215\n\ts_nop 0\n\t"
      "v_cndmask_b32_e32 %10, %10, %[oob], vcc\n\tv_cndmask_b32_e64 %11, %11, %[oob], %[m]\n\tv_cndmask_b32_e32 %2, %2, v212, vcc\n\tv_cndmask_b32_e64 %3, %3, v214, %[m]\n\t"
      "v_cmp_eq_u32_e32 vcc, %[tag], v217\n\tv_cmp_eq_u32_e64 %[m], %[tag], v219\n\ts_nop 0\n\t"
      "v_cndmask_b32_e32 %12, %12, %[oob], vcc\n\tv_cndmask_b32_e64 %13, %13, %[oob], %[m]\n\tv_cndmask_b32_e32 %4, %4, v216, vcc\n\tv_cndmask_b32_e64 %5, %5, v218, %[m]\n\t"
      "v_cmp_eq_u32_e32 vcc, %[tag], v221\n\tv_cmp_eq_u32_e64 %[m], %[tag], v223\n\ts_nop 0\n\t"
      "v_cndmask_b32_e32 %14, %14, %[oob], vcc\n\tv_cndmask_b32_e64 %15, %15, %[oob], %[m]\n\tv_cndmask_b32_e32 %6, %6, v220, vcc\n\tv_cndmask_b32_e64 %7, %7, v222, %[m]\n\t"
      "v_min3_u32 %[t0], %8, %9, %10\n\tv_min3_u32 %[t1], %11, %12, %13\n\tv_min3_u32 %[t0], %[t0], %14, %15\n\tv_min_u32_e32 %[t0], %[t0], %[t1]\n\t"
      "v_cmp_ne_u32_e32 vcc, %[t0], %[oob]\n\ts_nop 1\n\ts_cbranch_vccz .Lgdone%=\n\t"
      "buffer_load_dwordx2 v[208:209], %8, %[rs], 0 offen sc1\n\t"
      "buffer_load_dwordx2 v[210:211], %9, %[rs], 0 offen sc1\n\t"
      "buffer_load_dwordx2 v[212:213], %10, %[rs], 0 offen sc1\n\t"
      "buffer_load_dwordx2 v[214:215], %11, %[rs], 0 offen sc1\n\t"
      "buffer_load_dwordx2 v[216:217], %12, %[rs], 0 offen sc1\n\t"
      "buffer_load_dwordx2 v[218:219], %13, %[rs], 0 offen sc1\n\t"
      "buffer_load_dwordx2 v[220:221], %14, %[rs], 0 offen sc1\n\t"
      "buffer_load_dwordx2 v[222:223], %15, %[rs], 0 offen sc1\n\t"
      "s_waitcnt vmcnt(16)\n\t"
      "v_cmp_eq_u32_e32 vcc, %[tag], v225\n\tv_cmp_eq_u32_e64 %[m], %[tag], v227\n\ts_nop 0\n\t"
      "v_cndmask_b32_e32 %8, %8, %[oob], vcc\n\tv_cndmask_b32_e64 %9, %9, %[oob], %[m]\n\tv_cndmask_b32_e32 %0, %0, v224, vcc\n\tv_cndmask_b32_e64 %1, %1, v226, %[m]\n\t"
      "v_cmp_eq_u32_e32 vcc, %[tag], v229\n\tv_cmp_eq_u32_e64 %[m], %[tag], v231\n\ts_nop 0\n\t"
      "v_cndmask_b32_e32 %10, %10, %[oob], vcc\n\tv_cndmask_b32_e64 %11, %11, %[oob], %[m]\n\tv_cndmask_b32_e32 %2, %2, v228, vcc\n\tv_cndmask_b32_e64 %3, %3, v230, %[m]\n\t"
      "v_cmp_eq_u32_e32 vcc, %[tag], v233\n\tv_cmp_eq_u32_e64 %[m], %[tag], v235\n\ts_nop 0\n\t"
      "v_cndmask_b32_e32 %12, %12, %[oob], vcc\n\tv_cndmask_b32_e64 %13, %13, %[oob], %[m]\n\tv_cndmask_b32_e32 %4, %4, v232, vcc\n\tv_cndmask_b32_e64 %5, %5, v234, %[m]\n\t"
      "v_cmp_eq_u32_e32 vcc, %[tag], v237\n\tv_cmp_eq_u32_e64 %[m], %[tag], v239\n\ts_nop 0\n\t"
      "v_cndmask_b32_e32 %14, %14, %[oob], vcc\n\tv_cndmask_b32_e64 %15, %15, %[oob], %[m]\n\tv_cndmask_b32_e32 %6, %6, v236, vcc\n\tv_cndmask_b32_e64 %7, %7, v238, %[m]\n\t"
      "v_min3_u32 %[t0], %8, %9, %10\n\tv_min3_u32 %[t1], %11, %12, %13\n\tv_min3_u32 %[t0], %[t0], %14, %15\n\tv_min_u32_e32 %[t0], %[t0], %[t1]\n\t"
      "v_cmp_ne_u32_e32 vcc, %[t0], %[oob]\n\ts_nop 1\n\ts_cbranch_vccz .Lgdone%=\n\t"
      "buffer_load_dwordx2 v[224:225], %8, %[rs], 0 offen sc1\n\t"
      "buffer_load_dwordx2 v[226:227], %9, %[rs], 0 offen sc1\n\t"
      "buffer_load_dwordx2 v[228:229], %10, %[rs], 0 offen sc1\n\t"
      "buffer_load_dwordx2 v[230:231], %11, %[rs], 0 offen sc1\n\t"
      "buffer_load_dwordx2 v[232:233], %12, %[rs], 0 offen sc1\n\t"
      "buffer_load_dwordx2 v[234:235], %13, %[rs], 0 offen sc1\n\t"
      "buffer_load_dwordx2 v[236:237], %14, %[rs], 0 offen sc1\n\t"
      "buffer_load_dwordx2 v[238:239], %15, %[rs], 0 offen sc1\n\t"
      "s_waitcnt vmcnt(16)\n\t"
      "v_cmp_eq_u32_e32 vcc, %[tag], v241\n\tv_cmp_eq_u32_e64 %[m], %[tag], v243\n\ts_nop 0\n\t"
      "v_cndmask_b32_e32 %8, %8, %[oob], vcc\n\tv_cndmask_b32_e64 %9, %9, %[oob], %[m]\n\tv_cndmask_b32_e32 %0, %0, v240, vcc\n\tv_cndmask_b32_e64 %1, %1, v242, %[m]\n\t"
      "v_cmp_eq_u32_e32 vcc, %[tag], v245\n\tv_cmp_eq_u32_e64 %[m], %[tag], v247\n\ts_nop 0\n\t"
      "v_cndmask_b32_e32 %10, %10, %[oob], vcc\n\tv_cndmask_b32_e64 %11, %11, %[oob], %[m]\n\tv_cndmask_b32_e32 %2, %2, v244, vcc\n\tv_cndmask_b32_e64 %3, %3, v246, %[m]\n\t"
      "v_cmp_eq_u32_e32 vcc, %[tag], v249\n\tv_cmp_eq_u32_e64 %[m], %[tag], v251\n\ts_nop 0\n\t"
      "v_cndmask_b32_e32 %12, %12, %[oob], vcc\n\tv_cndmask_b32_e64 %13, %13, %[oob], %[m]\n\tv_cndmask_b32_e32 %4, %4, v248, vcc\n\tv_cndmask_b32_e64 %5, %5, v250, %[m]\n\t"
      "v_cmp_eq_u32_e32 vcc, %[tag], v253\n\tv_cmp_eq_u32_e64 %[m], %[tag], v255\n\ts_nop 0\n\t"
      "v_cndmask_b32_e32 %14, %14, %[oob], vcc\n\tv_cndmask_b32_e64 %15, %15, %[oob], %[m]\n\tv_cndmask_b32_e32 %6, %6, v252, vcc\n\tv_cndmask_b32_e64 %7, %7, v254, %[m]\n\t"
      "v_min3_u32 %[t0], %8, %9, %10\n\tv_min3_u32 %[t1], %11, %12, %13\n\tv_min3_u32 %[t0], %[t0], %14, %15\n\tv_min_u32_e32 %[t0], %[t0], %[t1]\n\t"
      "v_cmp_ne_u32_e32 vcc, %[t0], %[oob]\n\ts_nop 1\n\ts_cbranch_vccz .Lgdone%=\n\t"
      "buffer_load_dwordx2 v[240:241], %8, %[rs], 0 offen sc1\n\t"
      "buffer_load_dwordx2 v[242:243], %9, %[rs], 0 offen sc1\n\t"
      "buffer_load_dwordx2 v[244:245], %10, %[rs], 0 offen sc1\n\t"
      "buffer_load_dwordx2 v[246:247], %11, %[rs], 0 offen sc1\n\t"
      "buffer_load_dwordx2 v[248:249], %12, %[rs], 0 offen sc1\n\t"
      "buffer_load_dwordx2 v[250:251], %13, %[rs], 0 offen sc1\n\t"
      "buffer_load_dwordx2 v[252:253], %14, %[rs], 0 offen sc1\n\t"
      "buffer_load_dwordx2 v[254:255], %15, %[rs], 0 offen sc1\n\t"
      "s_memrealtime vcc\n\ts_waitcnt lgkmcnt(0)\n\ts_sub_u32 vcc_lo, vcc_lo, %[ts]\n\ts_cmp_lt_u32 vcc_lo, %[lim]\n\ts_cbranch_scc1 .Lgloop%=\n"
      ".Lgdone%=:"
      : "+v"(val[0]), "+v"(val[1]), "+v"(val[2]), "+v"(val[3]), "+v"(val[4]), "+v"(val[5]), "+v"(val[6]), "+v"(val[7]),
        "+v"(off[0]), "+v"(off[1]), "+v"(off[2]), "+v"(off[3]), "+v"(off[4]), "+v"(off[5]), "+v"(off[6]), "+v"(off[7]),
        [t0] "=&v"(t0), [t1] "=&v"(t1), [m] "=&s"(m)
      : [rs] "s"(rs), [tag] "s"(tag), [oob] "v"(oob), [ts] "s"(ts), [lim] "s"(SPIN_TICKS), [gap] "n"(GAP)
      : "memory", "vcc", "scc", "v208", "v209", "v210", "v211", "v212", "v213", "v214", "v215", "v216", "v217", "v218", "v219", "v220", "v221", "v222", "v223", "v224", "v225", "v226", "v227", "v228", "v229", "v230", "v231", "v232", "v233", "v234", "v235", "v236", "v237", "v238", "v239", "v240", "v241", "v242", "v243", "v244", "v245", "v246", "v247", "v248", "v249", "v250", "v251", "v252", "v253", "v254", "v255");
  }
}
#define PW 2
#define STR2(x) #x
#define STR(x) STR2(x)
#define PWAIT() asm volatile("s_waitcnt vmcnt(" STR(PW) ")" ::: "memory")
#define SCHED() do { asm volatile("" ::: "memory"); __builtin_amdgcn_sched_barrier(0); } while (0)
#define STAMP(i) do {} while (0)
#define UT(u) do {} while (0)

__global__ void __launch_bounds__(NTHR, 2)
fused_decode(const u16* x, const u16* kc, const u16* vc, const u16* wq, const u16* wo,
             const u16* w1, const u16* b1, const u16* w2, const u16* b2, u16* out, unsigned char* ws) {
  __shared__ __attribute__((aligned(16))) unsigned char lds_raw[L_END];
  LAS unsigned char* L = (LAS unsigned char*)lds_raw;
  const int bid = blockIdx.x;
  const int h = bid >> 3, j = bid & 7, b = j >> 2, sp = j & 3;
  const int tid = threadIdx.x, lane = tid & 63, w = __builtin_amdgcn_readfirstlane(tid >> 6), r = lane >> 3, c = lane & 7;
  unsigned warm = 0;
  if (CODEWARM && w == 0) {
    u64 pc; unsigned len;
    asm volatile("s_getpc_b64 %0\n.Lfd_here:\n\ts_mov_b32 %1, .Lfd_end-.Lfd_here" : "=s"(pc), "=s"(len));
    const unsigned off = min((unsigned)(((blockIdx.x >> 3) & 3) * 8192 + lane * 128), len - 4u) & ~3u;
    warm = *(const GAS unsigned*)((pc & ~127ull) + off);
  }
  SCHED();
  LAS unsigned* ctr = (LAS unsigned*)(L + L_CT);
  LAS u64* pace = (LAS u64*)(L + L_CT + 8);
  LAS unsigned* done = (LAS unsigned*)(L + L_CT + 16);
  const unsigned ts = now();
  STAMP(0);
  unsigned tag;
  constexpr float C2 = 0.07216878364870322f * 1.4426950408889634f;

  const __amdgpu_buffer_rsrc_t wsr = __builtin_amdgcn_make_buffer_rsrc((void*)ws, 0, (int)WS_ST, RSRC3);

  if (w < NS) {
    if (tid == 0) { *ctr = NS * P0; *pace = 0ull; *done = 0u; }
    const int rbase = ((b * H + h) * SK + sp * 8) * (DH * 2);
    constexpr int USTR = 4 * 8 * DH * 2;
    const int voff = r * (DH * 2) + c * 16;
    v4u Kb[P0][3], Vb[P0][3];
    const __amdgpu_buffer_rsrc_t kr = __builtin_amdgcn_make_buffer_rsrc((void*)kc, 0, KVBYTES, RSRC3), vr = __builtin_amdgcn_make_buffer_rsrc((void*)vc, 0, KVBYTES, RSRC3);
#define LD(dst, rs, t) { const int so_ = rbase + (t) * USTR; \
    _Pragma("unroll") for (int u = 0; u < 3; ++u) dst[u] = __builtin_amdgcn_raw_buffer_load_b128(rs, voff + u * 128, so_, AUX_##rs); }
#pragma unroll
    for (int i = 0; i < P0; ++i) {
      LD(Kb[i], kr, w + i * NS) UT(w + i * NS); SCHED(); PWAIT(); LD(Vb[i], vr, w + i * NS) SCHED(); if (i + 1 < P0) PWAIT(); }
    SCHED();
    STAMP(1);
    barrier();
    STAMP(2);
    asm volatile("" :: "v"(warm));
    tag = lds_ld(ctr + 1);
    v4u qv[3];
#pragma unroll
    for (int u = 0; u < 3; ++u) qv[u] = *(LAS v4u*)(L + L_Q + u * 128 + c * 16);
    float m = -INFINITY, l = 0.f; f32x2 ac2[12];
#pragma unroll
    for (int k = 0; k < 12; ++k) ac2[k] = f32x2{0.f, 0.f};
#define QK(Kx, s) { float d0 = 0.f, d1 = 0.f; \
      _Pragma("unroll") for (int u = 0; u < 3; ++u) { d0 = dot2(Kx[u][0], qv[u][0], d0); d1 = dot2(Kx[u][1], qv[u][1], d1); d0 = dot2(Kx[u][2], qv[u][2], d0); d1 = dot2(Kx[u][3], qv[u][3], d1); } \
      s = d0 + d1; asm volatile("" : "+v"(s)); }
#define PV(Vx, s) { s += dpp<XOR1>(s); s += dpp<XOR2>(s); s += dpp<XOR7>(s); s *= C2; \
      const float mn = fmaxf(m, s), al = __builtin_amdgcn_exp2f(m - mn), p = __builtin_amdgcn_exp2f(s - mn); m = mn; l = fmaf(l, al, p); \
      _Pragma("unroll") for (int u = 0; u < 3; ++u) { SCHED(); _Pragma("unroll") for (int k = 0; k < 4; ++k) \
          ac2[u * 4 + k] = __builtin_elementwise_fma(f32x2{p, p}, f32x2{lo16(Vx[u][k]), hi16(Vx[u][k])}, ac2[u * 4 + k] * al); \
        asm volatile("" : "+v"(ac2[u * 4]), "+v"(ac2[u * 4 + 1]), "+v"(ac2[u * 4 + 2]), "+v"(ac2[u * 4 + 3])); } }
    const unsigned ctra = L_CT + (unsigned)(uintptr_t)L, one = 1u;
#define COUNT() asm volatile("s_mov_b64 exec, 1\n\tds_add_u32 %0, %1\n\ts_mov_b64 exec, -1" :: "v"(ctra), "v"(one) : "memory")
    float s;
    int nxt = w + NS * P0;
#define STEP() { QK(Kb[0], s) SCHED(); LD(Kb[0], kr, nxt) COUNT(); UT(nxt); SCHED(); PV(Vb[0], s) SCHED(); LD(Vb[0], vr, nxt) SCHED(); nxt += NS; }
    static_assert(NS * (P0 + P0 / 2 + 1) <= NU, "the first steps load real units");
    STEP()
#pragma unroll
    for (int i = 1; i < P0; ++i) { QK(Kb[i], s) PV(Vb[i], s) SCHED(); if (i % 2 == 0) STEP() }
    STAMP(3);
    while (nxt < NU) {
      QK(Kb[0], s)
#pragma unroll 1
      for (int spn = 0; spn < 256; ++spn) {
        const u64 pv = __hip_atomic_load(pace, RLX_WG);
        const float t0 = asf(__builtin_amdgcn_readfirstlane((unsigned)pv)), tau = asf(__builtin_amdgcn_readfirstlane((unsigned)(pv >> 32)));
        if (fmaf((float)nxt, tau, t0) <= (float)(int)(now() - ts)) break;
        __builtin_amdgcn_s_sleep(2);
      }
      SCHED(); LD(Kb[0], kr, nxt) COUNT(); UT(nxt); SCHED();
      PV(Vb[0], s)
      SCHED(); LD(Vb[0], vr, nxt) SCHED();
      nxt += NS;
    }
    QK(Kb[0], s)
    PV(Vb[0], s)
    STAMP(4);
    float M = fmaxf(m, dpp<ROR8>(m)); M = max16(M); M = max32(M);
    const float sc = __builtin_amdgcn_exp2f(m - M);
    l *= sc; l += dpp<ROR8>(l); l = tr16(l, l); l = tr32(l, l);
    float acc[24];
#pragma unroll
    for (int k = 0; k < 12; ++k) { const f32x2 t = ac2[k] * sc; acc[2 * k] = t[0]; acc[2 * k + 1] = t[1]; }
#pragma unroll
    for (int k = 0; k < 12; ++k) acc[k] = tr32(acc[k], acc[k + 12]);
#pragma unroll
    for (int k = 0; k < 6; ++k) acc[k] = tr16(acc[k], acc[k + 6]);
#pragma unroll
    for (int k = 0; k < 3; ++k) acc[k] = tr8(acc[k], acc[k + 3], r & 1);
    LAS float* mg = (LAS float*)(L + L_MG) + w * 200;
#pragma unroll
    for (int k = 0; k < 3; ++k) mg[k * 64 + lane] = acc[k];
    if (lane == 0) { mg[192] = M; mg[193] = l; }
    asm volatile("s_waitcnt lgkmcnt(0)\n\ts_mov_b64 exec, 1\n\tds_add_u32 %0, %1 offset:16\n\ts_mov_b64 exec, -1" :: "v"(ctra), "v"(one) : "memory");
    while (lds_ld(done) < NS && now() - ts < SPIN_TICKS) {}
    asm volatile("" ::: "memory");
    if (w >= 3) __builtin_amdgcn_s_sleep(PD0);
    else {
      STAMP(5);
      const LAS float* mg0 = (const LAS float*)(L + L_MG);
      float MM = -INFINITY;
#pragma unroll
      for (int i = 0; i < NS; ++i) MM = fmaxf(MM, mg0[i * 200 + 192]);
      float Ls = 0.f, o = 0.f;
#pragma unroll
      for (int i = 0; i < NS; ++i) { const float e = __builtin_amdgcn_exp2f(mg0[i * 200 + 192] - MM); Ls = fmaf(e, mg0[i * 200 + 193], Ls); o = fmaf(e, mg0[i * 200 + tid], o); }
      gu64* px = (gu64*)(ws + WS_PX) + bid * 256;
      put(px + tid, tag, o / Ls);
      if (tid == 0) put(px + 192, tag, MM + __builtin_amdgcn_logf(Ls));
    }
    STAMP(6);
    static_assert(NS == 6, "waves 0..2 | 3..5 gather batch row 0 | 1");
    const int bb = w >= 3, pos = tid - bb * 192;
    const int vo1 = (int)WS_PX + (h * 8 + bb * 4) * 2048;
    const int gq = lane >> 4, vb = gq & 1, which = gq >> 1, oi = vb * 80 + 16 * w + (lane & 15), hp = oi / 5;
    gu64* rx = (gu64*)(ws + WS_RX) + ((j * 32 + hp) * 32 + h) * 10 + which * 5 + (oi - hp * 5);
    const int di = (lane >> 5) * 12 + ((lane >> 4) & 1) * 6 + ((lane >> 3) & 1) * 3 + (w - 3 * bb);
    LAS u16* oa = (LAS u16*)(L + L_OA + (bb * DH + (di >> 3) * 64 + c * 8 + (di & 7)) * 2);
    unsigned gv[8]; int go[8];
#pragma unroll
    for (int i = 0; i < 8; ++i) go[i] = vo1 + (i & 3) * 2048 + (i < 4 ? pos : 192) * 8;
    __builtin_amdgcn_s_sleep(PD1);
    gather8<GD, GG>(gv, go, wsr, tag, ts);
    { const float e0 = asf(gv[4]), e1 = asf(gv[5]), e2 = asf(gv[6]), e3 = asf(gv[7]);
      const float em = fmaxf(fmaxf(e0, e1), fmaxf(e2, e3));
      const float w0 = __builtin_amdgcn_exp2f(e0 - em), w1v = __builtin_amdgcn_exp2f(e1 - em), w2v = __builtin_amdgcn_exp2f(e2 - em), w3 = __builtin_amdgcn_exp2f(e3 - em);
      *oa = f2bf((w0 * asf(gv[0]) + w1v * asf(gv[1]) + w2v * asf(gv[2]) + w3 * asf(gv[3])) / (w0 + w1v + w2v + w3));
    }
    STAMP(8);
    barrier();
    STAMP(7);
    if (w < 5) {
      const LAS unsigned char* base = L + L_WO + (((lane & 15) >> 2) + 8 * (lane >> 4)) * 160 + w * 32 + (lane & 3) * 8;
      const LAS unsigned char* ao = L + L_OA + (lane & 1) * (DH * 2) + (lane >> 4) * 16;
      const LAS unsigned char* ah = L + L_HA + (lane & 1) * 96;
#define TRB(off) __builtin_amdgcn_ds_read_tr16_b64_v4i16((LAS v4s*)(base + (off)))
#define BFRAG(k) __builtin_bit_cast(v8bf, __builtin_shufflevector(TRB((k) * 32 * 160), TRB((k) * 32 * 160 + 4 * 160), 0, 1, 2, 3, 4, 5, 6, 7))
      const f32x4 Z = {0.f, 0.f, 0.f, 0.f};
      f32x4 S = Z;
#pragma unroll
      for (int k = 0; k < 6; ++k) S = __builtin_amdgcn_mfma_f32_16x16x32_bf16(*(const LAS v8bf*)(ao + k * 64), BFRAG(k), S, 0, 0, 0);
      f32x4 Ga = __builtin_amdgcn_mfma_f32_16x16x32_bf16(*(const LAS v8bf*)(ah + (lane >> 4) * 16), BFRAG(6), Z, 0, 0, 0);
      f32x4 Gb = __builtin_amdgcn_mfma_f32_16x16x16bf16_1k(*(const LAS v4s*)(ah + 64 + (lane >> 4) * 8), TRB(7 * 32 * 160 - 4 * (lane >> 4) * 160), Z, 0, 0, 0);
      asm volatile("" : "+v"(S), "+v"(Ga), "+v"(Gb));
      put(rx, tag, which ? (vb ? Ga[1] + Gb[1] : Ga[0] + Gb[0]) : (vb ? S[1] : S[0]));
      STAMP(9);
    }
  } else {
    __builtin_amdgcn_s_setprio(3);
    const int a = w - NS;
    const unsigned epoch = __hip_atomic_load((GAS unsigned*)(ws + WS_EPOCH), RLX_AGENT);
    const unsigned pre = QPRE < 0 ? 0u : __builtin_amdgcn_raw_buffer_load_b32(wsr, lane < 16 ? (int)WS_QX + (bid * 512 + (lane >> 3) * 256 + a * 128) * 8 + (lane & 7) * 128 : (a && lane < 24  && PACE ? (int)WS_CT + ((lane - 16) * 544 + blockIdx.x) * 8 : OOB), 0, QPRE < 0 ? 0 : QPRE);
    const int xb = lane >> 5, xl = lane & 31;
    const __amdgpu_buffer_rsrc_t xr = __builtin_amdgcn_make_buffer_rsrc((void*)x, 0, NB * D * 2, RSRC3);
    const int xo = xb * (D * 2) + xl * 40;
    const v4u x0 = __builtin_amdgcn_raw_buffer_load_b128(xr, xo, 0, 0), x1 = __builtin_amdgcn_raw_buffer_load_b128(xr, xo + 16, 0, 0);
    const v2u x2 = __builtin_amdgcn_raw_buffer_load_b64(xr, xo + 32, 0, 0);
    u16 xs[3];
#pragma unroll
    for (int i = 0; i < 3; ++i) xs[i] = __builtin_amdgcn_raw_buffer_load_b16(xr, xl + 32 * i < 80 ? xb * (D * 2) + (80 * j + xl + 32 * i) * 2 : OOB, 0, 0);
    const int oi = 5 * h + (lane < 5 ? lane : 0), ocol = 80 * j + oi % 80, oidx = (oi / 80) * D + ocol;
    const u16 xres = x[oidx], b2v = b2[ocol];
    v4u wa[10], wb[10];
    { const __amdgpu_buffer_rsrc_t ra = __builtin_amdgcn_make_buffer_rsrc((void*)wq, 0, D * H * DH * 2, RSRC3);
      const __amdgpu_buffer_rsrc_t rb = __builtin_amdgcn_make_buffer_rsrc((void*)(a ? w1 : wq), 0, a ? D * F * 2 : D * H * DH * 2, RSRC3);
      const int sa = H * DH * 2, sb = a ? F * 2 : H * DH * 2;
      const int va = r * sa + c * 16, vb = (a && c >= 6) ? OOB : r * sb + c * 16;
      const int oa = 80 * j * sa + h * (DH * 2) + a * 256, ob = 80 * j * sb + (a ? h * 96 : h * (DH * 2) + 128);
#pragma unroll
      for (int u = 0; u < 10; ++u) { wa[u] = __builtin_amdgcn_raw_buffer_load_b128(ra, va, oa + u * 8 * sa, AUX_NT); wb[u] = __builtin_amdgcn_raw_buffer_load_b128(rb, vb, ob + u * 8 * sb, AUX_NT); }
    }
    SCHED();
    STAMP(1);
    tag = (unsigned)__builtin_amdgcn_readfirstlane(epoch) * 0x9E3779B1u + 0x7F4A7C15u;
    tag = tag ? tag : 1u;
    if (a == 0 && lane == 0) ctr[1] = tag;
    {
      float v[20];
#pragma unroll
      for (int k = 0; k < 4; ++k) { v[2 * k] = lo16(x0[k]); v[2 * k + 1] = hi16(x0[k]); v[8 + 2 * k] = lo16(x1[k]); v[9 + 2 * k] = hi16(x1[k]); }
      v[16] = lo16(x2[0]); v[17] = hi16(x2[0]); v[18] = lo16(x2[1]); v[19] = hi16(x2[1]);
      float s = 0.f;
#pragma unroll
      for (int k = 0; k < 20; ++k) s += v[k];
      s += dpp<ROR1>(s); s += dpp<ROR2>(s); s += dpp<ROR4>(s); s += dpp<ROR8>(s); s = tr16(s, s);
      const float mean = s * (1.f / D);
      float q = 0.f;
#pragma unroll
      for (int k = 0; k < 20; ++k) { const float d = v[k] - mean; q = fmaf(d, d, q); }
      q += dpp<ROR1>(q); q += dpp<ROR2>(q); q += dpp<ROR4>(q); q += dpp<ROR8>(q); q = tr16(q, q);
      const float mb = rbf(mean), sd = rbf(sqrtf(rbf(rbf(q * (1.f / D)) + 1e-5f)));
      LAS float* xn = (LAS float*)(L + L_XN) + (a * 2 + xb) * 96 + xl;
#pragma unroll
      for (int i = 0; i < 3; ++i) xn[32 * i] = rbf(rbf(bf2f(xs[i]) - mb) / sd);
    }
    asm volatile("s_waitcnt lgkmcnt(0)" :: "v"(pre) : "memory");
    f32x2 p2[16];
#pragma unroll
    for (int k = 0; k < 16; ++k) p2[k] = f32x2{0.f, 0.f};
    { const LAS float* xn = (const LAS float*)(L + L_XN) + a * 192 + r;
#pragma unroll
      for (int u = 0; u < 10; ++u) { const float xa = xn[8 * u], xc = xn[96 + 8 * u];
#pragma unroll
        for (int k = 0; k < 4; ++k) {
          const f32x2 fa = {lo16(wa[u][k]), hi16(wa[u][k])}, fb = {lo16(wb[u][k]), hi16(wb[u][k])};
          p2[k] = __builtin_elementwise_fma(f32x2{xa, xa}, fa, p2[k]); p2[4 + k] = __builtin_elementwise_fma(f32x2{xa, xa}, fb, p2[4 + k]);
          p2[8 + k] = __builtin_elementwise_fma(f32x2{xc, xc}, fa, p2[8 + k]); p2[12 + k] = __builtin_elementwise_fma(f32x2{xc, xc}, fb, p2[12 + k]);
        } } }
    float pa[32];
#pragma unroll
    for (int k = 0; k < 16; ++k) { pa[2 * k] = p2[k][0]; pa[2 * k + 1] = p2[k][1]; }
#pragma unroll
    for (int k = 0; k < 16; ++k) pa[k] = tr32(pa[k], pa[k + 16]);
#pragma unroll
    for (int k = 0; k < 8; ++k) pa[k] = tr16(pa[k], pa[k + 8]);
#pragma unroll
    for (int k = 0; k < 4; ++k) pa[k] = tr8(pa[k], pa[k + 4], r & 1);
    STAMP(2);
    { gu64* qx = (gu64*)(ws + WS_QX) + bid * 512 + (lane >> 5) * 256 + (2 * a + ((lane >> 4) & 1)) * 64 + (lane & 15);
#pragma unroll
      for (int t = 0; t < 4; ++t) put(qx + t * 16, tag, pa[t]); }
    const int col = (lane & 7) * 8 + ((lane >> 3) & 1) * 4 + (lane >> 4);
    if (a == 0) {
      unsigned gv[24]; int go[24];
#pragma unroll
      for (int i = 0; i < 24; ++i) go[i] = (int)WS_QX + ((h * 8 + i / 3) * 512 + b * 256 + (i % 3) * 64 + lane) * 8;
      gather<0>(gv, go, wsr, tag, ts);
#pragma unroll
      for (int u = 0; u < 3; ++u) { float q = 0.f;
#pragma unroll
        for (int p = 0; p < 8; ++p) q += asf(gv[p * 3 + u]);
        *(LAS u16*)(L + L_Q + (u * 64 + col) * 2) = f2bf(q); }
    }
    STAMP(3);
    barrier();
    __builtin_amdgcn_s_setprio(0);
    if (a == 0) {
      const bool live = (lane & 7) < 6;
      const float bias = bf2f(b1[48 * h + (live ? col : 0)]);
      unsigned gv[16]; int go[16];
#pragma unroll
      for (int i = 0; i < 16; ++i) go[i] = live ? (int)WS_QX + ((h * 8) * 512 + i * 256 + 192 + lane) * 8 : OOB;
      gather<16>(gv, go, wsr, tag, ts);
      float hs[2] = {0.f, 0.f};
#pragma unroll
      for (int p = 0; p < 16; ++p) hs[p & 1] += asf(gv[p]);
      if (live) {
#pragma unroll
        for (int u = 0; u < 2; ++u) { float t = rbf(rbf(hs[u]) + bias); t = t > 0.f ? t : 0.f; *(LAS u16*)(L + L_HA + (u * 48 + col) * 2) = f2bf(t * t); } }
    }
    else {
      float u0 = 0.f;
      for (int rd = 0; rd < 8; ++rd) {
        const unsigned Tk = PT0 + rd * PDT;
        gu64* cl = (gu64*)(ws + WS_CT) + rd * 544;
        while (now() - ts < Tk && lds_ld(ctr) < NU - 24) __builtin_amdgcn_s_sleep(8);
        const unsigned um = lds_ld(ctr);
        if (lane == 0) __hip_atomic_store(cl + blockIdx.x, ((u64)tag << 32) | um, RLX_AGENT);
        if (um >= NU - 24) break;
        while (now() - ts < Tk + PLAG) __builtin_amdgcn_s_sleep(8);
        float s = 0.f; int n = 0;
#pragma unroll
        for (int k = 0; k < (PCL + 63) / 64; ++k) { const u64 v = get(cl + (blockIdx.x & ~(PCL - 1)) + (lane & (PCL - 1)) + 64 * k); const bool ok = (unsigned)(v >> 32) == tag;
          s += ok ? (float)(unsigned)v : 0.f; n += __builtin_popcountll(__ballot(ok)); }
        s += dpp<ROR1>(s); s += dpp<ROR2>(s); s += dpp<ROR4>(s); s += dpp<ROR8>(s); s = tr16(s, s); s = tr32(s, s);
        const float ub = s / (float)n;
        const float tau = rd ? (float)(rd * PDT) / (ub - u0) * (1.f - PEPS * 0.001f) : (float)(PT0 - POFF) / (ub + 6.25f);
        if (rd == 0) u0 = ub;
        const float tend = (float)Tk + ((float)NU - ub) * tau, tnow = (float)(int)(now() - ts), ti = (tend - tnow) / ((float)NU - (float)lds_ld(ctr));
        if (lane == 0 && n > 0 && ti > 0.f && ti < 1000.f) __hip_atomic_store(pace, ((u64)asu(ti) << 32) | asu(tend - (float)NU * ti), RLX_WG);
      }
    }
    int vw[5], v2[4];
#pragma unroll
    for (int i = 0; i < 5; ++i) { const int id = i * 64 + lane, row = id / 10; vw[i] = row * (D * 2) + (id - row * 10) * 16; }
#pragma unroll
    for (int i = 0; i < 4; ++i) { const int id = (4 * a + i) * 64 + lane, row = id / 10; v2[i] = id < 480 ? row * (D * 2) + (id - row * 10) * 16 : OOB; }
    const __amdgpu_buffer_rsrc_t ro = __builtin_amdgcn_make_buffer_rsrc((void*)wo, 0, H * DH * D * 2, RSRC3), r2 = __builtin_amdgcn_make_buffer_rsrc((void*)w2, 0, F * D * 2, RSRC3);
    const int so = (DH * h + 96 * a) * (D * 2) + 160 * j, s2 = 48 * h * (D * 2) + 160 * j;
    SCHED();
    if (PRETOUCH) {
      const unsigned t = __builtin_amdgcn_raw_buffer_load_b32(wsr, a ? (lane < 20 ? (int)WS_RX + (j * 32 + h) * 2560 + lane * 128 : OOB) : (int)WS_PX + (h * 8 + (lane >> 3)) * 2048 + (lane & 7) * 128, 0, AUX_SC1);
      const unsigned t2 = __builtin_amdgcn_raw_buffer_load_b32(wsr, a || (lane & 7) >= 5 ? OOB : (int)WS_PX + (h * 8 + (lane >> 3)) * 2048 + (8 + (lane & 7)) * 128, 0, AUX_SC1);
      asm volatile("" :: "v"(t), "v"(t2)); }
    while (lds_ld(ctr) < NU - WTRIG && now() - ts < SPIN_TICKS) __builtin_amdgcn_s_sleep(2);
    v4u wr[19];
#pragma unroll
    for (int i = 0; i < 19; ++i) {
      wr[i] = i < 15 ? __builtin_amdgcn_raw_buffer_load_b128(ro, vw[i % 5], so + (i / 5) * 32 * (D * 2), AUX_NT) : __builtin_amdgcn_raw_buffer_load_b128(r2, v2[i < 15 ? 0 : i - 15], s2, AUX_NT);
      SCHED(); asm volatile("s_waitcnt vmcnt(" STR(WPACE) ")" ::: "memory"); }
    SCHED();
    STAMP(5);
    LAS unsigned char* wi = L + L_WO + lane * 16;
#pragma unroll
    for (int i = 0; i < 15; ++i) *(LAS v4u*)(wi + (15 * a + i) * 1024) = wr[i];
#pragma unroll
    for (int i = 0; i < 4; ++i) if (i < 3 || v2[3] != OOB) *(LAS v4u*)(wi + (30 + 4 * a + i) * 1024) = wr[15 + i];
    STAMP(6);
    barrier();
    if (a == 1) {
      unsigned gv[8]; int go[8];
#pragma unroll
      for (int i = 0; i < 8; ++i) go[i] = (lane & 15) < 10 ? (int)WS_RX + (j * 32 + h) * 2560 + i * 320 + (lane >> 4) * 80 + (lane & 15) * 8 : OOB;
      __builtin_amdgcn_s_sleep(PD2);
      gather8<GD2, GG>(gv, go, wsr, tag, ts);
      float s = 0.f;
#pragma unroll
      for (int i = 0; i < 8; ++i) s += asf(gv[i]);
      s = tr16(s, s); s = tr32(s, s);
      const float t = rbf(rbf(dpp<0x105>(s)) + bf2f(b2v));
      const float gate = rbf(t / (1.f + __expf(-t)));
      if (lane < 5) out[oidx] = f2bf(bf2f(xres) + rbf(rbf(s) * gate));
      if (bid == 0 && lane == 0) __hip_atomic_store((GAS unsigned*)(ws + WS_EPOCH), tag, RLX_AGENT);
      STAMP(10);
    }
  }
  if (CODEWARM) asm volatile(".Lfd_end:");
}

extern "C" void kernel_launch(void* const* d_in, const int* in_sizes, int n_in, void* d_out, int out_size, void* d_ws, size_t ws_size, hipStream_t stream) {
  fused_decode<<<256, NTHR, 0, stream>>>((const u16*)d_in[0], (const u16*)d_in[1], (const u16*)d_in[2], (const u16*)d_in[3], (const u16*)d_in[4],
                                         (const u16*)d_in[6], (const u16*)d_in[7], (const u16*)d_in[8], (const u16*)d_in[9], (u16*)d_out, (unsigned char*)d_ws);
}
```

```cpp
#define DIAG 0
#include <hip/hip_runtime.h>
#include <hip/hip_bf16.h>
#include <stdint.h>

#define NS 6
#define P0 5
#define PACE 1
#define XCDGROUP 0
#define WPACE 7
#define PEPS 5
#define POFF 300
#define PM0 56
#define PDM 48
#define GD 1
#define GG 5
#define WTRIG 0
#define PRETOUCH 1
#define QPRE 0
#define PCL 64
#define PD0 6
#define PD1 6
#define PD2 16
#define GD2 GD
#define PWIN 0
#define PT0 1300
#define PDT 900
#define PLAG 130
#define CODEWARM 1
#define TLBWARM 0
#define STAMPS 0

#define GAS __attribute__((address_space(1)))
#define LAS __attribute__((address_space(3)))
#define RLX_AGENT __ATOMIC_RELAXED, __HIP_MEMORY_SCOPE_AGENT
#define RLX_WG __ATOMIC_RELAXED, __HIP_MEMORY_SCOPE_WORKGROUP
typedef unsigned v4u __attribute__((ext_vector_type(4)));
typedef unsigned v2u __attribute__((ext_vector_type(2)));
typedef short v4s __attribute__((ext_vector_type(4)));
typedef short v8s __attribute__((ext_vector_type(8)));
typedef __bf16 v8bf __attribute__((ext_vector_type(8)));
typedef float f32x4 __attribute__((ext_vector_type(4)));
typedef float f32x2 __attribute__((ext_vector_type(2)));
typedef __bf16 bf16x2 __attribute__((ext_vector_type(2)));
typedef unsigned short u16;
typedef unsigned long long u64;
typedef GAS u64 gu64;

constexpr int NB = 2, D = 640, H = 32, SK = 8192, DH = 192, F = 1536;
constexpr int NWAVE = NS + 2, NTHR = NWAVE * 64;
constexpr int NU = 256;
constexpr int KVBYTES = NB * H * SK * DH * 2;
constexpr int AUX_NT = 2;
#define KAUX 2
#define VAUX 2
constexpr int AUX_kr = KAUX, AUX_vr = VAUX;
constexpr int RSRC3 = 0x00020000;
constexpr size_t WS_EPOCH = 0;
constexpr size_t WS_QX = 4096;
constexpr size_t WS_PX = WS_QX + 256 * 512 * 8;
constexpr size_t WS_RX = WS_PX + 256 * 256 * 8;
constexpr size_t WS_CT = WS_RX + 8 * 32 * 320 * 8;
constexpr size_t WS_ST = WS_CT + 8 * 544 * 8;
constexpr int L_XN = 0;
constexpr int L_Q = L_XN + 4 * 96 * 4;
constexpr int L_HA = L_Q + 384;
constexpr int L_OA = L_HA + 192;
constexpr int L_CT = L_OA + 768;
constexpr int L_MG = L_CT + 32;
constexpr int L_WO = L_MG + NS * 800;
constexpr int L_TL = L_WO + 240 * 160;
constexpr int L_END = L_TL + (STAMPS ? 1024 : 0);
static_assert(L_WO % 16 == 0 && L_OA % 16 == 0 && L_HA % 16 == 0 && L_Q % 16 == 0 && L_CT % 16 == 0, "alignment");
constexpr unsigned SPIN_TICKS = 2000000u;

__device__ __forceinline__ float bf2f(u16 b) { return __builtin_bit_cast(float, (unsigned)b << 16); }
__device__ __forceinline__ u16 f2bf(float f) { return __builtin_bit_cast(u16, (__bf16)f); }
__device__ __forceinline__ float rbf(float f) { return bf2f(f2bf(f)); }
__device__ __forceinline__ float lo16(unsigned v) { return __builtin_bit_cast(float, v << 16); }
__device__ __forceinline__ float hi16(unsigned v) { return __builtin_bit_cast(float, v & 0xffff0000u); }
__device__ __forceinline__ float asf(unsigned v) { return __builtin_bit_cast(float, v); }
__device__ __forceinline__ unsigned asu(float v) { return __builtin_bit_cast(unsigned, v); }
__device__ __forceinline__ float dot2(unsigned k, unsigned q, float acc) {
  return __builtin_amdgcn_fdot2_f32_bf16(__builtin_bit_cast(bf16x2, k), __builtin_bit_cast(bf16x2, q), acc, false);
}
template <int CTRL> __device__ __forceinline__ float dpp(float x) {
  return __builtin_bit_cast(float, __builtin_amdgcn_mov_dpp(__builtin_bit_cast(int, x), CTRL, 0xf, 0xf, true));
}
constexpr int XOR1 = 0xB1, XOR2 = 0x4E, XOR7 = 0x141, ROR8 = 0x128, ROR4 = 0x124, ROR2 = 0x122, ROR1 = 0x121;
__device__ __forceinline__ float tr32(float x, float y) {
  auto s = __builtin_amdgcn_permlane32_swap(asu(x), asu(y), false, false); return asf(s[0]) + asf(s[1]);
}
__device__ __forceinline__ float tr16(float x, float y) {
  auto s = __builtin_amdgcn_permlane16_swap(asu(x), asu(y), false, false); return asf(s[0]) + asf(s[1]);
}
__device__ __forceinline__ float tr8(float x, float y, bool up) {
  const float keep = up ? y : x, send = up ? x : y; return keep + dpp<ROR8>(send);
}
__device__ __forceinline__ float max16(float x) { auto s = __builtin_amdgcn_permlane16_swap(asu(x), asu(x), false, false); return fmaxf(asf(s[0]), asf(s[1])); }
__device__ __forceinline__ float max32(float x) { auto s = __builtin_amdgcn_permlane32_swap(asu(x), asu(x), false, false); return fmaxf(asf(s[0]), asf(s[1])); }
__device__ __forceinline__ unsigned now() { return (unsigned)__builtin_amdgcn_s_memrealtime(); }
__device__ __forceinline__ void barrier() { asm volatile("s_waitcnt lgkmcnt(0)\n\ts_barrier" ::: "memory"); }
__device__ __forceinline__ void put(gu64* g, unsigned tag, float v) { __hip_atomic_store(g, ((u64)tag << 32) | asu(v), RLX_AGENT); }
__device__ __forceinline__ u64 get(const gu64* g) { return __hip_atomic_load(g, RLX_AGENT); }
__device__ __forceinline__ unsigned lds_ld(LAS unsigned* p) { return __builtin_amdgcn_readfirstlane(__hip_atomic_load(p, RLX_WG)); }
constexpr int OOB = 0x7ffffff0, AUX_SC1 = 16;
template <int SLEEP, int N> __device__ __forceinline__ void gather(unsigned (&val)[N], int (&off)[N], __amdgpu_buffer_rsrc_t rs, unsigned tag, unsigned ts) {
#pragma unroll
  for (int i = 0; i < N; ++i) val[i] = 0u;
  for (;;) {
    v2u in[N];
    asm volatile("" ::: "memory");
#pragma unroll
    for (int i = 0; i < N; ++i) in[i] = __builtin_amdgcn_raw_buffer_load_b64(rs, off[i], 0, AUX_SC1);
    int left = OOB;
#pragma unroll
    for (int i = 0; i < N; ++i) { const bool got = in[i][1] == tag; val[i] = got ? in[i][0] : val[i]; off[i] = got ? OOB : off[i]; left = min(left, off[i]); }
    if (__all(left == OOB) || now() - ts > SPIN_TICKS) break;
    if (SLEEP) __builtin_amdgcn_s_sleep(SLEEP);
  }
}
template <int DEPTH, int GAP> __device__ __forceinline__ void gather8(unsigned (&val)[8], int (&off)[8], __amdgpu_buffer_rsrc_t rs, unsigned tag, unsigned ts) {
#pragma unroll
  for (int i = 0; i < 8; ++i) val[i] = 0u;
  unsigned t0, t1; u64 m; const int oob = OOB;
  if constexpr (DEPTH == 1) {
    asm volatile(
      "buffer_load_dwordx2 v[208:209], %8, %[rs], 0 offen sc1\n\t"
      "buffer_load_dwordx2 v[210:211], %9, %[rs], 0 offen sc1\n\t"
      "buffer_load_dwordx2 v[212:213], %10, %[rs], 0 offen sc1\n\t"
      "buffer_load_dwordx2 v[214:215], %11, %[rs], 0 offen sc1\n\t"
      "buffer_load_dwordx2 v[216:217], %12, %[rs], 0 offen sc1\n\t"
      "buffer_load_dwordx2 v[218:219], %13, %[rs], 0 offen sc1\n\t"
      "buffer_load_dwordx2 v[220:221], %14, %[rs], 0 offen sc1\n\t"
      "buffer_load_dwordx2 v[222:223], %15, %[rs], 0 offen sc1\n\t"
      ".Lgloop%=:\n\t"
      "s_waitcnt vmcnt(0)\n\t"
      "v_cmp_eq_u32_e32 vcc, %[tag], v209\n\tv_cmp_eq_u32_e64 %[m], %[tag], v211\n\ts_nop 0\n\t"
      "v_cndmask_b32_e32 %8, %8, %[oob], vcc\n\tv_cndmask_b32_e64 %9, %9, %[oob], %[m]\n\tv_cndmask_b32_e32 %0, %0, v208, vcc\n\tv_cndmask_b32_e64 %1, %1, v210, %[m]\n\t"
      "v_cmp_eq_u32_e32 vcc, %[tag], v213\n\tv_cmp_eq_u32_e64 %[m], %[tag], v215\n\ts_nop 0\n\t"
      "v_cndmask_b32_e32 %10, %10, %[oob], vcc\n\tv_cndmask_b32_e64 %11, %11, %[oob], %[m]\n\tv_cndmask_b32_e32 %2, %2, v212, vcc\n\tv_cndmask_b32_e64 %3, %3, v214, %[m]\n\t"
      "v_cmp_eq_u32_e32 vcc, %[tag], v217\n\tv_cmp_eq_u32_e64 %[m], %[tag], v219\n\ts_nop 0\n\t"
      "v_cndmask_b32_e32 %12, %12, %[oob], vcc\n\tv_cndmask_b32_e64 %13, %13, %[oob], %[m]\n\tv_cndmask_b32_e32 %4, %4, v216, vcc\n\tv_cndmask_b32_e64 %5, %5, v218, %[m]\n\t"
      "v_cmp_eq_u32_e32 vcc, %[tag], v221\n\tv_cmp_eq_u32_e64 %[m], %[tag], v223\n\ts_nop 0\n\t"
      "v_cndmask_b32_e32 %14, %14, %[oob], vcc\n\tv_cndmask_b32_e64 %15, %15, %[oob], %[m]\n\tv_cndmask_b32_e32 %6, %6, v220, vcc\n\tv_cndmask_b32_e64 %7, %7, v222, %[m]\n\t"
      "v_min3_u32 %[t0], %8, %9, %10\n\tv_min3_u32 %[t1], %11, %12, %13\n\tv_min3_u32 %[t0], %[t0], %14, %15\n\tv_min_u32_e32 %[t0], %[t0], %[t1]\n\t"
      "v_cmp_ne_u32_e32 vcc, %[t0], %[oob]\n\ts_nop 1\n\ts_cbranch_vccz .Lgdone%=\n\t"
      "buffer_load_dwordx2 v[208:209], %8, %[rs], 0 offen sc1\n\t"
      "buffer_load_dwordx2 v[210:211], %9, %[rs], 0 offen sc1\n\t"
      "buffer_load_dwordx2 v[212:213], %10, %[rs], 0 offen sc1\n\t"
      "buffer_load_dwordx2 v[214:215], %11, %[rs], 0 offen sc1\n\t"
      "buffer_load_dwordx2 v[216:217], %12, %[rs], 0 offen sc1\n\t"
      "buffer_load_dwordx2 v[218:219], %13, %[rs], 0 offen sc1\n\t"
      "buffer_load_dwordx2 v[220:221], %14, %[rs], 0 offen sc1\n\t"
      "buffer_load_dwordx2 v[222:223], %15, %[rs], 0 offen sc1\n\t"
      "s_memrealtime vcc\n\ts_waitcnt lgkmcnt(0)\n\ts_sub_u32 vcc_lo, vcc_lo, %[ts]\n\ts_cmp_lt_u32 vcc_lo, %[lim]\n\ts_cbranch_scc1 .Lgloop%=\n"
      ".Lgdone%=:"
      : "+v"(val[0]), "+v"(val[1]), "+v"(val[2]), "+v"(val[3]), "+v"(val[4]), "+v"(val[5]), "+v"(val[6]), "+v"(val[7]),
        "+v"(off[0]), "+v"(off[1]), "+v"(off[2]), "+v"(off[3]), "+v"(off[4]), "+v"(off[5]), "+v"(off[6]), "+v"(off[7]),
        [t0] "=&v"(t0), [t1] "=&v"(t1), [m] "=&s"(m)
      : [rs] "s"(rs), [tag] "s"(tag), [oob] "v"(oob), [ts] "s"(ts), [lim] "s"(SPIN_TICKS), [gap] "n"(GAP)
      : "memory", "vcc", "scc", "v208", "v209", "v210", "v211", "v212", "v213", "v214", "v215", "v216", "v217", "v218", "v219", "v220", "v221", "v222", "v223");
  } else if constexpr (DEPTH == 2) {
    asm volatile(
      "buffer_load_dwordx2 v[208:209], %8, %[rs], 0 offen sc1\n\t"
      "buffer_load_dwordx2 v[210:211], %9, %[rs], 0 offen sc1\n\t"
      "buffer_load_dwordx2 v[212:213], %10, %[rs], 0 offen sc1\n\t"
      "buffer_load_dwordx2 v[214:215], %11, %[rs], 0 offen sc1\n\t"
      "buffer_load_dwordx2 v[216:217], %12, %[rs], 0 offen sc1\n\t"
      "buffer_load_dwordx2 v[218:219], %13, %[rs], 0 offen sc1\n\t"
      "buffer_load_dwordx2 v[220:221], %14, %[rs], 0 offen sc1\n\t"
      "buffer_load_dwordx2 v[222:223], %15, %[rs], 0 offen sc1\n\t"
      "s_sleep %[gap]\n\t"
      "buffer_load_dwordx2 v[224:225], %8, %[rs], 0 offen sc1\n\t"
      "buffer_load_dwordx2 v[226:227], %9, %[rs], 0 offen sc1\n\t"
      "buffer_load_dwordx2 v[228:229], %10, %[rs], 0 offen sc1\n\t"
      "buffer_load_dwordx2 v[230:231], %11, %[rs], 0 offen sc1\n\t"
      "buffer_load_dwordx2 v[232:233], %12, %[rs], 0 offen sc1\n\t"
      "buffer_load_dwordx2 v[234:235], %13, %[rs], 0 offen sc1\n\t"
      "buffer_load_dwordx2 v[236:237], %14, %[rs], 0 offen sc1\n\t"
      "buffer_load_dwordx2 v[238:239], %15, %[rs], 0 offen sc1\n\t"
      ".Lgloop%=:\n\t"
      "s_waitcnt vmcnt(8)\n\t"
      "v_cmp_eq_u32_e32 vcc, %[tag], v209\n\tv_cmp_eq_u32_e64 %[m], %[tag], v211\n\ts_nop 0\n\t"
      "v_cndmask_b32_e32 %8, %8, %[oob], vcc\n\tv_cndmask_b32_e64 %9, %9, %[oob], %[m]\n\tv_cndmask_b32_e32 %0, %0, v208, vcc\n\tv_cndmask_b32_e64 %1, %1, v210, %[m]\n\t"
      "v_cmp_eq_u32_e32 vcc, %[tag], v213\n\tv_cmp_eq_u32_e64 %[m], %[tag], v215\n\ts_nop 0\n\t"
      "v_cndmask_b32_e32 %10, %10, %[oob], vcc\n\tv_cndmask_b32_e64 %11, %11, %[oob], %[m]\n\tv_cndmask_b32_e32 %2, %2, v212, vcc\n\tv_cndmask_b32_e64 %3, %3, v214, %[m]\n\t"
      "v_cmp_eq_u32_e32 vcc, %[tag], v217\n\tv_cmp_eq_u32_e64 %[m], %[tag], v219\n\ts_nop 0\n\t"
      "v_cndmask_b32_e32 %12, %12, %[oob], vcc\n\tv_cndmask_b32_e64 %13, %13, %[oob], %[m]\n\tv_cndmask_b32_e32 %4, %4, v216, vcc\n\tv_cndmask_b32_e64 %5, %5, v218, %[m]\n\t"
      "v_cmp_eq_u32_e32 vcc, %[tag], v221\n\tv_cmp_eq_u32_e64 %[m], %[tag], v223\n\ts_nop 0\n\t"
      "v_cndmask_b32_e32 %14, %14, %[oob], vcc\n\tv_cndmask_b32_e64 %15, %15, %[oob], %[m]\n\tv_cndmask_b32_e32 %6, %6, v220, vcc\n\tv_cndmask_b32_e64 %7, %7, v222, %[m]\n\t"
      "v_min3_u32 %[t0], %8, %9, %10\n\tv_min3_u32 %[t1], %11, %12, %13\n\tv_min3_u32 %[t0], %[t0], %14, %15\n\tv_min_u32_e32 %[t0], %[t0], %[t1]\n\t"
      "v_cmp_ne_u32_e32 vcc, %[t0], %[oob]\n\ts_nop 1\n\ts_cbranch_vccz .Lgdone%=\n\t"
      "buffer_load_dwordx2 v[208:209], %8, %[rs], 0 offen sc1\n\t"
      "buffer_load_dwordx2 v[210:211], %9, %[rs], 0 offen sc1\n\t"
      "buffer_load_dwordx2 v[212:213], %10, %[rs], 0 offen sc1\n\t"
      "buffer_load_dwordx2 v[214:215], %11, %[rs], 0 offen sc1\n\t"
      "buffer_load_dwordx2 v[216:217], %12, %[rs], 0 offen sc1\n\t"
      "buffer_load_dwordx2 v[218:219], %13, %[rs], 0 offen sc1\n\t"
      "buffer_load_dwordx2 v[220:221], %14, %[rs], 0 offen sc1\n\t"
      "buffer_load_dwordx2 v[222:223], %15, %[rs], 0 offen sc1\n\t"
      "s_waitcnt vmcnt(8)\n\t"
      "v_cmp_eq_u32_e32 vcc, %[tag], v225\n\tv_cmp_eq_u32_e64 %[m], %[tag], v227\n\ts_nop 0\n\t"
      "v_cndmask_b32_e32 %8, %8, %[oob], vcc\n\tv_cndmask_b32_e64 %9, %9, %[oob], %[m]\n\tv_cndmask_b32_e32 %0, %0, v224, vcc\n\tv_cndmask_b32_e64 %1, %1, v226, %[m]\n\t"
      "v_cmp_eq_u32_e32 vcc, %[tag], v229\n\tv_cmp_eq_u32_e64 %[m], %[tag], v231\n\ts_nop 0\n\t"
      "v_cndmask_b32_e32 %10, %10, %[oob], vcc\n\tv_cndmask_b32_e64 %11, %11, %[oob], %[m]\n\tv_cndmask_b32_e32 %2, %2, v228, vcc\n\tv_cndmask_b32_e64 %3, %3, v230, %[m]\n\t"
      "v_cmp_eq_u32_e32 vcc, %[tag], v233\n\tv_cmp_eq_u32_e64 %[m], %[tag], v235\n\ts_nop 0\n\t"
      "v_cndmask_b32_e32 %12, %12, %[oob], vcc\n\tv_cndmask_b32_e64 %13, %13, %[oob], %[m]\n\tv_cndmask_b32_e32 %4, %4, v232, vcc\n\tv_cndmask_b32_e64 %5, %5, v234, %[m]\n\t"
      "v_cmp_eq_u32_e32 vcc, %[tag], v237\n\tv_cmp_eq_u32_e64 %[m], %[tag], v239\n\ts_nop 0\n\t"
      "v_cndmask_b32_e32 %14, %14, %[oob], vcc\n\tv_cndmask_b32_e64 %15, %15, %[oob], %[m]\n\tv_cndmask_b32_e32 %6, %6, v236, vcc\n\tv_cndmask_b32_e64 %7, %7, v238, %[m]\n\t"
      "v_min3_u32 %[t0], %8, %9, %10\n\tv_min3_u32 %[t1], %11, %12, %13\n\tv_min3_u32 %[t0], %[t0], %14, %15\n\tv_min_u32_e32 %[t0], %[t0], %[t1]\n\t"
      "v_cmp_ne_u32_e32 vcc, %[t0], %[oob]\n\ts_nop 1\n\ts_cbranch_vccz .Lgdone%=\n\t"
      "buffer_load_dwordx2 v[224:225], %8, %[rs], 0 offen sc1\n\t"
      "buffer_load_dwordx2 v[226:227], %9, %[rs], 0 offen sc1\n\t"
      "buffer_load_dwordx2 v[228:229], %10, %[rs], 0 offen sc1\n\t"
      "buffer_load_dwordx2 v[230:231], %11, %[rs], 0 offen sc1\n\t"
      "buffer_load_dwordx2 v[232:233], %12, %[rs], 0 offen sc1\n\t"
      "buffer_load_dwordx2 v[234:235], %13, %[rs], 0 offen sc1\n\t"
      "buffer_load_dwordx2 v[236:237], %14, %[rs], 0 offen sc1\n\t"
      "buffer_load_dwordx2 v[238:239], %15, %[rs], 0 offen sc1\n\t"
      "s_memrealtime vcc\n\ts_waitcnt lgkmcnt(0)\n\ts_sub_u32 vcc_lo, vcc_lo, %[ts]\n\ts_cmp_lt_u32 vcc_lo, %[lim]\n\ts_cbranch_scc1 .Lgloop%=\n"
      ".Lgdone%=:"
      : "+v"(val[0]), "+v"(val[1]), "+v"(val[2]), "+v"(val[3]), "+v"(val[4]), "+v"(val[5]), "+v"(val[6]), "+v"(val[7]),
        "+v"(off[0]), "+v"(off[1]), "+v"(off[2]), "+v"(off[3]), "+v"(off[4]), "+v"(off[5]), "+v"(off[6]), "+v"(off[7]),
        [t0] "=&v"(t0), [t1] "=&v"(t1), [m] "=&s"(m)
      : [rs] "s"(rs), [tag] "s"(tag), [oob] "v"(oob), [ts] "s"(ts), [lim] "s"(SPIN_TICKS), [gap] "n"(GAP)
      : "memory", "vcc", "scc", "v208", "v209", "v210", "v211", "v212", "v213", "v214", "v215", "v216", "v217", "v218", "v219", "v220", "v221", "v222", "v223", "v224", "v225", "v226", "v227", "v228", "v229", "v230", "v231", "v232", "v233", "v234", "v235", "v236", "v237", "v238", "v239");
  } else {
    asm volatile(
      "buffer_load_dwordx2 v[208:209], %8, %[rs], 0 offen sc1\n\t"
      "buffer_load_dwordx2 v[210:211], %9, %[rs], 0 offen sc1\n\t"
      "buffer_load_dwordx2 v[212:213], %10, %[rs], 0 offen sc1\n\t"
      "buffer_load_dwordx2 v[214:215], %11, %[rs], 0 offen sc1\n\t"
      "buffer_load_dwordx2 v[216:217], %12, %[rs], 0 offen sc1\n\t"
      "buffer_load_dwordx2 v[218:219], %13, %[rs], 0 offen sc1\n\t"
      "buffer_load_dwordx2 v[220:221], %14, %[rs], 0 offen sc1\n\t"
      "buffer_load_dwordx2 v[222:223], %15, %[rs], 0 offen sc1\n\t"
      "s_sleep %[gap]\n\t"
      "buffer_load_dwordx2 v[224:225], %8, %[rs], 0 offen sc1\n\t"
      "buffer_load_dwordx2 v[226:227], %9, %[rs], 0 offen sc1\n\t"
      "buffer_load_dwordx2 v[228:229], %10, %[rs], 0 offen sc1\n\t"
      "buffer_load_dwordx2 v[230:231], %11, %[rs], 0 offen sc1\n\t"
      "buffer_load_dwordx2 v[232:233], %12, %[rs], 0 offen sc1\n\t"
      "buffer_load_dwordx2 v[234:235], %13, %[rs], 0 offen sc1\n\t"
      "buffer_load_dwordx2 v[236:237], %14, %[rs], 0 offen sc1\n\t"
      "buffer_load_dwordx2 v[238:239], %15, %[rs], 0 offen sc1\n\t"
      "s_sleep %[gap]\n\t"
      "buffer_load_dwordx2 v[240:241], %8, %[rs], 0 offen sc1\n\t"
      "buffer_load_dwordx2 v[242:243], %9, %[rs], 0 offen sc1\n\t"
      "buffer_load_dwordx2 v[244:245], %10, %[rs], 0 offen sc1\n\t"
      "buffer_load_dwordx2 v[246:247], %11, %[rs], 0 offen sc1\n\t"
      "buffer_load_dwordx2 v[248:249], %12, %[rs], 0 offen sc1\n\t"
      "buffer_load_dwordx2 v[250:251], %13, %[rs], 0 offen sc1\n\t"
      "buffer_load_dwordx2 v[252:253], %14, %[rs], 0 offen sc1\n\t"
      "buffer_load_dwordx2 v[254:255], %15, %[rs], 0 offen sc1\n\t"
      ".Lgloop%=:\n\t"
      "s_waitcnt vmcnt(16)\n\t"
      "v_cmp_eq_u32_e32 vcc, %[tag], v209\n\tv_cmp_eq_u32_e64 %[m], %[tag], v211\n\ts_nop 0\n\t"
      "v_cndmask_b32_e32 %8, %8, %[oob], vcc\n\tv_cndmask_b32_e64 %9, %9, %[oob], %[m]\n\tv_cndmask_b32_e32 %0, %0, v208, vcc\n\tv_cndmask_b32_e64 %1, %1, v210, %[m]\n\t"
      "v_cmp_eq_u32_e32 vcc, %[tag], v213\n\tv_cmp_eq_u32_e64 %[m], %[tag], v215\n\ts_nop 0\n\t"
      "v_cndmask_b32_e32 %10, %10, %[oob], vcc\n\tv_cndmask_b32_e64 %11, %11, %[oob], %[m]\n\tv_cndmask_b32_e32 %2, %2, v212, vcc\n\tv_cndmask_b32_e64 %3, %3, v214, %[m]\n\t"
      "v_cmp_eq_u32_e32 vcc, %[tag], v217\n\tv_cmp_eq_u32_e64 %[m], %[tag], v219\n\ts_nop 0\n\t"
      "v_cndmask_b32_e32 %12, %12, %[oob], vcc\n\tv_cndmask_b32_e64 %13, %13, %[oob], %[m]\n\tv_cndmask_b32_e32 %4, %4, v216, vcc\n\tv_cndmask_b32_e64 %5, %5, v218, %[m]\n\t"
      "v_cmp_eq_u32_e32 vcc, %[tag], v221\n\tv_cmp_eq_u32_e64 %[m], %[tag], v223\n\ts_nop 0\n\t"
      "v_cndmask_b32_e32 %14, %14, %[oob], vcc\n\tv_cndmask_b32_e64 %15, %15, %[oob], %[m]\n\tv_cndmask_b32_e32 %6, %6, v220, vcc\n\tv_cndmask_b32_e64 %7, %7, v222, %[m]\n\t"
      "v_min3_u32 %[t0], %8, %9, %10\n\tv_min3_u32 %[t1], %11, %12, %13\n\tv_min3_u32 %[t0], %[t0], %14, %15\n\tv_min_u32_e32 %[t0], %[t0], %[t1]\n\t"
      "v_cmp_ne_u32_e32 vcc, %[t0], %[oob]\n\ts_nop 1\n\ts_cbranch_vccz .Lgdone%=\n\t"
      "buffer_load_dwordx2 v[208:209], %8, %[rs], 0 offen sc1\n\t"
      "buffer_load_dwordx2 v[210:211], %9, %[rs], 0 offen sc1\n\t"
      "buffer_load_dwordx2 v[212:213], %10, %[rs], 0 offen sc1\n\t"
      "buffer_load_dwordx2 v[214:215], %11, %[rs], 0 offen sc1\n\t"
      "buffer_load_dwordx2 v[216:217], %12, %[rs], 0 offen sc1\n\t"
      "buffer_load_dwordx2 v[218:219], %13, %[rs], 0 offen sc1\n\t"
      "buffer_load_dwordx2 v[220:221], %14, %[rs], 0 offen sc1\n\t"
      "buffer_load_dwordx2 v[222:223], %15, %[rs], 0 offen sc1\n\t"
      "s_waitcnt vmcnt(16)\n\t"
      "v_cmp_eq_u32_e32 vcc, %[tag], v225\n\tv_cmp_eq_u32_e64 %[m], %[tag], v227\n\ts_nop 0\n\t"
      "v_cndmask_b32_e32 %8, %8, %[oob], vcc\n\tv_cndmask_b32_e64 %9, %9, %[oob], %[m]\n\tv_cndmask_b32_e32 %0, %0, v224, vcc\n\tv_cndmask_b32_e64 %1, %1, v226, %[m]\n\t"
      "v_cmp_eq_u32_e32 vcc, %[tag], v229\n\tv_cmp_eq_u32_e64 %[m], %[tag], v231\n\ts_nop 0\n\t"
      "v_cndmask_b32_e32 %10, %10, %[oob], vcc\n\tv_cndmask_b32_e64 %11, %11, %[oob], %[m]\n\tv_cndmask_b32_e32 %2, %2, v228, vcc\n\tv_cndmask_b32_e64 %3, %3, v230, %[m]\n\t"
      "v_cmp_eq_u32_e32 vcc, %[tag], v233\n\tv_cmp_eq_u32_e64 %[m], %[tag], v235\n\ts_nop 0\n\t"
      "v_cndmask_b32_e32 %12, %12, %[oob], vcc\n\tv_cndmask_b32_e64 %13, %13, %[oob], %[m]\n\tv_cndmask_b32_e32 %4, %4, v232, vcc\n\tv_cndmask_b32_e64 %5, %5, v234, %[m]\n\t"
      "v_cmp_eq_u32_e32 vcc, %[tag], v237\n\tv_cmp_eq_u32_e64 %[m], %[tag], v239\n\ts_nop 0\n\t"
      "v_cndmask_b32_e32 %14, %14, %[oob], vcc\n\tv_cndmask_b32_e64 %15, %15, %[oob], %[m]\n\tv_cndmask_b32_e32 %6, %6, v236, vcc\n\tv_cndmask_b32_e64 %7, %7, v238, %[m]\n\t"
      "v_min3_u32 %[t0], %8, %9, %10\n\tv_min3_u32 %[t1], %11, %12, %13\n\tv_min3_u32 %[t0], %[t0], %14, %15\n\tv_min_u32_e32 %[t0], %[t0], %[t1]\n\t"
      "v_cmp_ne_u32_e32 vcc, %[t0], %[oob]\n\ts_nop 1\n\ts_cbranch_vccz .Lgdone%=\n\t"
      "buffer_load_dwordx2 v[224:225], %8, %[rs], 0 offen sc1\n\t"
      "buffer_load_dwordx2 v[226:227], %9, %[rs], 0 offen sc1\n\t"
      "buffer_load_dwordx2 v[228:229], %10, %[rs], 0 offen sc1\n\t"
      "buffer_load_dwordx2 v[230:231], %11, %[rs], 0 offen sc1\n\t"
      "buffer_load_dwordx2 v[232:233], %12, %[rs], 0 offen sc1\n\t"
      "buffer_load_dwordx2 v[234:235], %13, %[rs], 0 offen sc1\n\t"
      "buffer_load_dwordx2 v[236:237], %14, %[rs], 0 offen sc1\n\t"
      "buffer_load_dwordx2 v[238:239], %15, %[rs], 0 offen sc1\n\t"
      "s_waitcnt vmcnt(16)\n\t"
      "v_cmp_eq_u32_e32 vcc, %[tag], v241\n\tv_cmp_eq_u32_e64 %[m], %[tag], v243\n\ts_nop 0\n\t"
      "v_cndmask_b32_e32 %8, %8, %[oob], vcc\n\tv_cndmask_b32_e64 %9, %9, %[oob], %[m]\n\tv_cndmask_b32_e32 %0, %0, v240, vcc\n\tv_cndmask_b32_e64 %1, %1, v242, %[m]\n\t"
      "v_cmp_eq_u32_e32 vcc, %[tag], v245\n\tv_cmp_eq_u32_e64 %[m], %[tag], v247\n\ts_nop 0\n\t"
      "v_cndmask_b32_e32 %10, %10, %[oob], vcc\n\tv_cndmask_b32_e64 %11, %11, %[oob], %[m]\n\tv_cndmask_b32_e32 %2, %2, v244, vcc\n\tv_cndmask_b32_e64 %3, %3, v246, %[m]\n\t"
      "v_cmp_eq_u32_e32 vcc, %[tag], v249\n\tv_cmp_eq_u32_e64 %[m], %[tag], v251\n\ts_nop 0\n\t"
      "v_cndmask_b32_e32 %12, %12, %[oob], vcc\n\tv_cndmask_b32_e64 %13, %13, %[oob], %[m]\n\tv_cndmask_b32_e32 %4, %4, v248, vcc\n\tv_cndmask_b32_e64 %5, %5, v250, %[m]\n\t"
      "v_cmp_eq_u32_e32 vcc, %[tag], v253\n\tv_cmp_eq_u32_e64 %[m], %[tag], v255\n\ts_nop 0\n\t"
      "v_cndmask_b32_e32 %14, %14, %[oob], vcc\n\tv_cndmask_b32_e64 %15, %15, %[oob], %[m]\n\tv_cndmask_b32_e32 %6, %6, v252, vcc\n\tv_cndmask_b32_e64 %7, %7, v254, %[m]\n\t"
      "v_min3_u32 %[t0], %8, %9, %10\n\tv_min3_u32 %[t1], %11, %12, %13\n\tv_min3_u32 %[t0], %[t0], %14, %15\n\tv_min_u32_e32 %[t0], %[t0], %[t1]\n\t"
      "v_cmp_ne_u32_e32 vcc, %[t0], %[oob]\n\ts_nop 1\n\ts_cbranch_vccz .Lgdone%=\n\t"
      "buffer_load_dwordx2 v[240:241], %8, %[rs], 0 offen sc1\n\t"
      "buffer_load_dwordx2 v[242:243], %9, %[rs], 0 offen sc1\n\t"
      "buffer_load_dwordx2 v[244:245], %10, %[rs], 0 offen sc1\n\t"
      "buffer_load_dwordx2 v[246:247], %11, %[rs], 0 offen sc1\n\t"
      "buffer_load_dwordx2 v[248:249], %12, %[rs], 0 offen sc1\n\t"
      "buffer_load_dwordx2 v[250:251], %13, %[rs], 0 offen sc1\n\t"
      "buffer_load_dwordx2 v[252:253], %14, %[rs], 0 offen sc1\n\t"
      "buffer_load_dwordx2 v[254:255], %15, %[rs], 0 offen sc1\n\t"
      "s_memrealtime vcc\n\ts_waitcnt lgkmcnt(0)\n\ts_sub_u32 vcc_lo, vcc_lo, %[ts]\n\ts_cmp_lt_u32 vcc_lo, %[lim]\n\ts_cbranch_scc1 .Lgloop%=\n"
      ".Lgdone%=:"
      : "+v"(val[0]), "+v"(val[1]), "+v"(val[2]), "+v"(val[3]), "+v"(val[4]), "+v"(val[5]), "+v"(val[6]), "+v"(val[7]),
        "+v"(off[0]), "+v"(off[1]), "+v"(off[2]), "+v"(off[3]), "+v"(off[4]), "+v"(off[5]), "+v"(off[6]), "+v"(off[7]),
        [t0] "=&v"(t0), [t1] "=&v"(t1), [m] "=&s"(m)
      : [rs] "s"(rs), [tag] "s"(tag), [oob] "v"(oob), [ts] "s"(ts), [lim] "s"(SPIN_TICKS), [gap] "n"(GAP)
      : "memory", "vcc", "scc", "v208", "v209", "v210", "v211", "v212", "v213", "v214", "v215", "v216", "v217", "v218", "v219", "v220", "v221", "v222", "v223", "v224", "v225", "v226", "v227", "v228", "v229", "v230", "v231", "v232", "v233", "v234", "v235", "v236", "v237", "v238", "v239", "v240", "v241", "v242", "v243", "v244", "v245", "v246", "v247", "v248", "v249", "v250", "v251", "v252", "v253", "v254", "v255");
  }
}
#define PW 2
#define STR2(x) #x
#define STR(x) STR2(x)
#define PWAIT() asm volatile("s_waitcnt vmcnt(" STR(PW) ")" ::: "memory")
#define SCHED() do { asm volatile("" ::: "memory"); __builtin_amdgcn_sched_barrier(0); } while (0)
#define STAMP(i) do {} while (0)
#define UT(u) do {} while (0)

__global__ void __launch_bounds__(NTHR, 2)
fused_decode(const u16* x, const u16* kc, const u16* vc, const u16* wq, const u16* wo,
             const u16* w1, const u16* b1, const u16* w2, const u16* b2, u16* out, unsigned char* ws) {
  __shared__ __attribute__((aligned(16))) unsigned char lds_raw[L_END];
  LAS unsigned char* L = (LAS unsigned char*)lds_raw;
  const int bid = blockIdx.x;
  const int h = bid >> 3, j = bid & 7, b = j >> 2, sp = j & 3;
  const int tid = threadIdx.x, lane = tid & 63, w = __builtin_amdgcn_readfirstlane(tid >> 6), r = lane >> 3, c = lane & 7;
  unsigned warm = 0;
  if (CODEWARM && w == 0) {
    u64 pc; unsigned len;
    asm volatile("s_getpc_b64 %0\n.Lfd_here:\n\ts_mov_b32 %1, .Lfd_end-.Lfd_here" : "=s"(pc), "=s"(len));
    const unsigned off = min((unsigned)(((blockIdx.x >> 3) & 3) * 8192 + lane * 128), len - 4u) & ~3u;
    warm = *(const GAS unsigned*)((pc & ~127ull) + off);
  }
  SCHED();
  LAS unsigned* ctr = (LAS unsigned*)(L + L_CT);
  LAS u64* pace = (LAS u64*)(L + L_CT + 8);
  LAS unsigned* done = (LAS unsigned*)(L + L_CT + 16);
  const unsigned ts = now();
  const u64 c0 = __builtin_amdgcn_s_memtime();
  STAMP(0);
  unsigned tag;
  constexpr float C2 = 0.07216878364870322f * 1.4426950408889634f;

  const __amdgpu_buffer_rsrc_t wsr = __builtin_amdgcn_make_buffer_rsrc((void*)ws, 0, (int)WS_ST, RSRC3);

  if (w < NS) {
    if (tid == 0) { *ctr = NS * P0; *pace = 0ull; *done = 0u; }
    const int rbase = ((b * H + h) * SK + sp * 8) * (DH * 2);
    constexpr int USTR = 4 * 8 * DH * 2;
    const int voff = r * (DH * 2) + c * 16;
    v4u Kb[P0][3], Vb[P0][3];
    const __amdgpu_buffer_rsrc_t kr = __builtin_amdgcn_make_buffer_rsrc((void*)kc, 0, KVBYTES, RSRC3), vr = __builtin_amdgcn_make_buffer_rsrc((void*)vc, 0, KVBYTES, RSRC3);
#define LD(dst, rs, t) { const int so_ = rbase + (t) * USTR; \
    _Pragma("unroll") for (int u = 0; u < 3; ++u) dst[u] = __builtin_amdgcn_raw_buffer_load_b128(rs, voff + u * 128, so_, AUX_##rs); }
#pragma unroll
    for (int i = 0; i < P0; ++i) {
      LD(Kb[i], kr, w + i * NS) UT(w + i * NS); SCHED(); PWAIT(); LD(Vb[i], vr, w + i * NS) SCHED(); if (i + 1 < P0) PWAIT(); }
    SCHED();
    STAMP(1);
    barrier();
    STAMP(2);
    asm volatile("" :: "v"(warm));
    tag = lds_ld(ctr + 1);
    v4u qv[3];
#pragma unroll
    for (int u = 0; u < 3; ++u) qv[u] = *(LAS v4u*)(L + L_Q + u * 128 + c * 16);
    float m = -INFINITY, l = 0.f; f32x2 ac2[12];
#pragma unroll
    for (int k = 0; k < 12; ++k) ac2[k] = f32x2{0.f, 0.f};
#define QK(Kx, s) { float d0 = 0.f, d1 = 0.f; \
      _Pragma("unroll") for (int u = 0; u < 3; ++u) { d0 = dot2(Kx[u][0], qv[u][0], d0); d1 = dot2(Kx[u][1], qv[u][1], d1); d0 = dot2(Kx[u][2], qv[u][2], d0); d1 = dot2(Kx[u][3], qv[u][3], d1); } \
      s = d0 + d1; asm volatile("" : "+v"(s)); }
#define PV(Vx, s) { s += dpp<XOR1>(s); s += dpp<XOR2>(s); s += dpp<XOR7>(s); s *= C2; \
      const float mn = fmaxf(m, s), al = __builtin_amdgcn_exp2f(m - mn), p = __builtin_amdgcn_exp2f(s - mn); m = mn; l = fmaf(l, al, p); \
      _Pragma("unroll") for (int u = 0; u < 3; ++u) { SCHED(); _Pragma("unroll") for (int k = 0; k < 4; ++k) \
          ac2[u * 4 + k] = __builtin_elementwise_fma(f32x2{p, p}, f32x2{lo16(Vx[u][k]), hi16(Vx[u][k])}, ac2[u * 4 + k] * al); \
        asm volatile("" : "+v"(ac2[u * 4]), "+v"(ac2[u * 4 + 1]), "+v"(ac2[u * 4 + 2]), "+v"(ac2[u * 4 + 3])); } }
    const unsigned ctra = L_CT + (unsigned)(uintptr_t)L, one = 1u;
#define COUNT() asm volatile("s_mov_b64 exec, 1\n\tds_add_u32 %0, %1\n\ts_mov_b64 exec, -1" :: "v"(ctra), "v"(one) : "memory")
    float s;
    int nxt = w + NS * P0;
#define STEP() { QK(Kb[0], s) SCHED(); LD(Kb[0], kr, nxt) COUNT(); UT(nxt); SCHED(); PV(Vb[0], s) SCHED(); LD(Vb[0], vr, nxt) SCHED(); nxt += NS; }
    static_assert(NS * (P0 + P0 / 2 + 1) <= NU, "the first steps load real units");
    STEP()
#pragma unroll
    for (int i = 1; i < P0; ++i) { QK(Kb[i], s) PV(Vb[i], s) SCHED(); if (i % 2 == 0) STEP() }
    STAMP(3);
    while (nxt < NU) {
      QK(Kb[0], s)
#pragma unroll 1
      for (int spn = 0; spn < 256; ++spn) {
        const u64 pv = __hip_atomic_load(pace, RLX_WG);
        const float t0 = asf(__builtin_amdgcn_readfirstlane((unsigned)pv)), tau = asf(__builtin_amdgcn_readfirstlane((unsigned)(pv >> 32)));
        if (fmaf((float)nxt, tau, t0) <= (float)(int)(now() - ts)) break;
        __builtin_amdgcn_s_sleep(2);
      }
      SCHED(); LD(Kb[0], kr, nxt) COUNT(); UT(nxt); SCHED();
      PV(Vb[0], s)
      SCHED(); LD(Vb[0], vr, nxt) SCHED();
      nxt += NS;
    }
    QK(Kb[0], s)
    PV(Vb[0], s)
    STAMP(4);
    float M = fmaxf(m, dpp<ROR8>(m)); M = max16(M); M = max32(M);
    const float sc = __builtin_amdgcn_exp2f(m - M);
    l *= sc; l += dpp<ROR8>(l); l = tr16(l, l); l = tr32(l, l);
    float acc[24];
#pragma unroll
    for (int k = 0; k < 12; ++k) { const f32x2 t = ac2[k] * sc; acc[2 * k] = t[0]; acc[2 * k + 1] = t[1]; }
#pragma unroll
    for (int k = 0; k < 12; ++k) acc[k] = tr32(acc[k], acc[k + 12]);
#pragma unroll
    for (int k = 0; k < 6; ++k) acc[k] = tr16(acc[k], acc[k + 6]);
#pragma unroll
    for (int k = 0; k < 3; ++k) acc[k] = tr8(acc[k], acc[k + 3], r & 1);
    LAS float* mg = (LAS float*)(L + L_MG) + w * 200;
#pragma unroll
    for (int k = 0; k < 3; ++k) mg[k * 64 + lane] = acc[k];
    if (lane == 0) { mg[192] = M; mg[193] = l; }
    asm volatile("s_waitcnt lgkmcnt(0)\n\ts_mov_b64 exec, 1\n\tds_add_u32 %0, %1 offset:16\n\ts_mov_b64 exec, -1" :: "v"(ctra), "v"(one) : "memory");
    while (lds_ld(done) < NS && now() - ts < SPIN_TICKS) {}
    asm volatile("" ::: "memory");
    if (w >= 3) __builtin_amdgcn_s_sleep(PD0);
    else {
      STAMP(5);
      const LAS float* mg0 = (const LAS float*)(L + L_MG);
      float MM = -INFINITY;
#pragma unroll
      for (int i = 0; i < NS; ++i) MM = fmaxf(MM, mg0[i * 200 + 192]);
      float Ls = 0.f, o = 0.f;
#pragma unroll
      for (int i = 0; i < NS; ++i) { const float e = __builtin_amdgcn_exp2f(mg0[i * 200 + 192] - MM); Ls = fmaf(e, mg0[i * 200 + 193], Ls); o = fmaf(e, mg0[i * 200 + tid], o); }
      gu64* px = (gu64*)(ws + WS_PX) + bid * 256;
      put(px + tid, tag, o / Ls);
      if (tid == 0) put(px + 192, tag, MM + __builtin_amdgcn_logf(Ls));
    }
    STAMP(6);
    static_assert(NS == 6, "waves 0..2 | 3..5 gather batch row 0 | 1");
    const int bb = w >= 3, pos = tid - bb * 192;
    const int vo1 = (int)WS_PX + (h * 8 + bb * 4) * 2048;
    const int gq = lane >> 4, vb = gq & 1, which = gq >> 1, oi = vb * 80 + 16 * w + (lane & 15), hp = oi / 5;
    gu64* rx = (gu64*)(ws + WS_RX) + ((j * 32 + hp) * 32 + h) * 10 + which * 5 + (oi - hp * 5);
    const int di = (lane >> 5) * 12 + ((lane >> 4) & 1) * 6 + ((lane >> 3) & 1) * 3 + (w - 3 * bb);
    LAS u16* oa = (LAS u16*)(L + L_OA + (bb * DH + (di >> 3) * 64 + c * 8 + (di & 7)) * 2);
    unsigned gv[8]; int go[8];
#pragma unroll
    for (int i = 0; i < 8; ++i) go[i] = vo1 + (i & 3) * 2048 + (i < 4 ? pos : 192) * 8;
    __builtin_amdgcn_s_sleep(PD1);
    gather8<GD, GG>(gv, go, wsr, tag, ts);
    { const float e0 = asf(gv[4]), e1 = asf(gv[5]), e2 = asf(gv[6]), e3 = asf(gv[7]);
      const float em = fmaxf(fmaxf(e0, e1), fmaxf(e2, e3));
      const float w0 = __builtin_amdgcn_exp2f(e0 - em), w1v = __builtin_amdgcn_exp2f(e1 - em), w2v = __builtin_amdgcn_exp2f(e2 - em), w3 = __builtin_amdgcn_exp2f(e3 - em);
      *oa = f2bf((w0 * asf(gv[0]) + w1v * asf(gv[1]) + w2v * asf(gv[2]) + w3 * asf(gv[3])) / (w0 + w1v + w2v + w3));
    }
    STAMP(8);
    barrier();
    STAMP(7);
    if (w < 5) {
      const LAS unsigned char* base = L + L_WO + (((lane & 15) >> 2) + 8 * (lane >> 4)) * 160 + w * 32 + (lane & 3) * 8;
      const LAS unsigned char* ao = L + L_OA + (lane & 1) * (DH * 2) + (lane >> 4) * 16;
      const LAS unsigned char* ah = L + L_HA + (lane & 1) * 96;
#define TRB(off) __builtin_amdgcn_ds_read_tr16_b64_v4i16((LAS v4s*)(base + (off)))
#define BFRAG(k) __builtin_bit_cast(v8bf, __builtin_shufflevector(TRB((k) * 32 * 160), TRB((k) * 32 * 160 + 4 * 160), 0, 1, 2, 3, 4, 5, 6, 7))
      const f32x4 Z = {0.f, 0.f, 0.f, 0.f};
      f32x4 S = Z;
#pragma unroll
      for (int k = 0; k < 6; ++k) S = __builtin_amdgcn_mfma_f32_16x16x32_bf16(*(const LAS v8bf*)(ao + k * 64), BFRAG(k), S, 0, 0, 0);
      f32x4 Ga = __builtin_amdgcn_mfma_f32_16x16x32_bf16(*(const LAS v8bf*)(ah + (lane >> 4) * 16), BFRAG(6), Z, 0, 0, 0);
      f32x4 Gb = __builtin_amdgcn_mfma_f32_16x16x16bf16_1k(*(const LAS v4s*)(ah + 64 + (lane >> 4) * 8), TRB(7 * 32 * 160 - 4 * (lane >> 4) * 160), Z, 0, 0, 0);
      asm volatile("" : "+v"(S), "+v"(Ga), "+v"(Gb));
      put(rx, tag, which ? (vb ? Ga[1] + Gb[1] : Ga[0] + Gb[0]) : (vb ? S[1] : S[0]));
      STAMP(9);
    }
  } else {
    __builtin_amdgcn_s_setprio(3);
    const int a = w - NS;
    const unsigned epoch = __hip_atomic_load((GAS unsigned*)(ws + WS_EPOCH), RLX_AGENT);
    const unsigned pre = QPRE < 0 ? 0u : __builtin_amdgcn_raw_buffer_load_b32(wsr, lane < 16 ? (int)WS_QX + (bid * 512 + (lane >> 3) * 256 + a * 128) * 8 + (lane & 7) * 128 : (a && lane < 24  && PACE ? (int)WS_CT + ((lane - 16) * 544 + blockIdx.x) * 8 : OOB), 0, QPRE < 0 ? 0 : QPRE);
    const int xb = lane >> 5, xl = lane & 31;
    const __amdgpu_buffer_rsrc_t xr = __builtin_amdgcn_make_buffer_rsrc((void*)x, 0, NB * D * 2, RSRC3);
    const int xo = xb * (D * 2) + xl * 40;
    const v4u x0 = __builtin_amdgcn_raw_buffer_load_b128(xr, xo, 0, 0), x1 = __builtin_amdgcn_raw_buffer_load_b128(xr, xo + 16, 0, 0);
    const v2u x2 = __builtin_amdgcn_raw_buffer_load_b64(xr, xo + 32, 0, 0);
    u16 xs[3];
#pragma unroll
    for (int i = 0; i < 3; ++i) xs[i] = __builtin_amdgcn_raw_buffer_load_b16(xr, xl + 32 * i < 80 ? xb * (D * 2) + (80 * j + xl + 32 * i) * 2 : OOB, 0, 0);
    const int oi = 5 * h + (lane < 5 ? lane : 0), ocol = 80 * j + oi % 80, oidx = (oi / 80) * D + ocol;
    const u16 xres = x[oidx], b2v = b2[ocol];
    v4u wa[10], wb[10];
    { const __amdgpu_buffer_rsrc_t ra = __builtin_amdgcn_make_buffer_rsrc((void*)wq, 0, D * H * DH * 2, RSRC3);
      const __amdgpu_buffer_rsrc_t rb = __builtin_amdgcn_make_buffer_rsrc((void*)(a ? w1 : wq), 0, a ? D * F * 2 : D * H * DH * 2, RSRC3);
      const int sa = H * DH * 2, sb = a ? F * 2 : H * DH * 2;
      const int va = r * sa + c * 16, vb = (a && c >= 6) ? OOB : r * sb + c * 16;
      const int oa = 80 * j * sa + h * (DH * 2) + a * 256, ob = 80 * j * sb + (a ? h * 96 : h * (DH * 2) + 128);
#pragma unroll
      for (int u = 0; u < 10; ++u) { wa[u] = __builtin_amdgcn_raw_buffer_load_b128(ra, va, oa + u * 8 * sa, AUX_NT); wb[u] = __builtin_amdgcn_raw_buffer_load_b128(rb, vb, ob + u * 8 * sb, AUX_NT); }
    }
    SCHED();
    STAMP(1);
    tag = (unsigned)__builtin_amdgcn_readfirstlane(epoch) * 0x9E3779B1u + 0x7F4A7C15u;
    tag = tag ? tag : 1u;
    const unsigned tX = now() - ts;
    if (a == 0 && lane == 0) ctr[1] = tag;
    {
      float v[20];
#pragma unroll
      for (int k = 0; k < 4; ++k) { v[2 * k] = lo16(x0[k]); v[2 * k + 1] = hi16(x0[k]); v[8 + 2 * k] = lo16(x1[k]); v[9 + 2 * k] = hi16(x1[k]); }
      v[16] = lo16(x2[0]); v[17] = hi16(x2[0]); v[18] = lo16(x2[1]); v[19] = hi16(x2[1]);
      float s = 0.f;
#pragma unroll
      for (int k = 0; k < 20; ++k) s += v[k];
      s += dpp<ROR1>(s); s += dpp<ROR2>(s); s += dpp<ROR4>(s); s += dpp<ROR8>(s); s = tr16(s, s);
      const float mean = s * (1.f / D);
      float q = 0.f;
#pragma unroll
      for (int k = 0; k < 20; ++k) { const float d = v[k] - mean; q = fmaf(d, d, q); }
      q += dpp<ROR1>(q); q += dpp<ROR2>(q); q += dpp<ROR4>(q); q += dpp<ROR8>(q); q = tr16(q, q);
      const float mb = rbf(mean), sd = rbf(sqrtf(rbf(rbf(q * (1.f / D)) + 1e-5f)));
      LAS float* xn = (LAS float*)(L + L_XN) + (a * 2 + xb) * 96 + xl;
#pragma unroll
      for (int i = 0; i < 3; ++i) xn[32 * i] = rbf(rbf(bf2f(xs[i]) - mb) / sd);
    }
    asm volatile("s_waitcnt lgkmcnt(0)" :: "v"(pre) : "memory");
    f32x2 p2[16];
#pragma unroll
    for (int k = 0; k < 16; ++k) p2[k] = f32x2{0.f, 0.f};
    { const LAS float* xn = (const LAS float*)(L + L_XN) + a * 192 + r;
#pragma unroll
      for (int u = 0; u < 10; ++u) { const float xa = xn[8 * u], xc = xn[96 + 8 * u];
#pragma unroll
        for (int k = 0; k < 4; ++k) {
          const f32x2 fa = {lo16(wa[u][k]), hi16(wa[u][k])}, fb = {lo16(wb[u][k]), hi16(wb[u][k])};
          p2[k] = __builtin_elementwise_fma(f32x2{xa, xa}, fa, p2[k]); p2[4 + k] = __builtin_elementwise_fma(f32x2{xa, xa}, fb, p2[4 + k]);
          p2[8 + k] = __builtin_elementwise_fma(f32x2{xc, xc}, fa, p2[8 + k]); p2[12 + k] = __builtin_elementwise_fma(f32x2{xc, xc}, fb, p2[12 + k]);
        } } }
    float pa[32];
#pragma unroll
    for (int k = 0; k < 16; ++k) { pa[2 * k] = p2[k][0]; pa[2 * k + 1] = p2[k][1]; }
#pragma unroll
    for (int k = 0; k < 16; ++k) pa[k] = tr32(pa[k], pa[k + 16]);
#pragma unroll
    for (int k = 0; k < 8; ++k) pa[k] = tr16(pa[k], pa[k + 8]);
#pragma unroll
    for (int k = 0; k < 4; ++k) pa[k] = tr8(pa[k], pa[k + 4], r & 1);
    STAMP(2);
    { gu64* qx = (gu64*)(ws + WS_QX) + bid * 512 + (lane >> 5) * 256 + (2 * a + ((lane >> 4) & 1)) * 64 + (lane & 15);
#pragma unroll
      for (int t = 0; t < 4; ++t) put(qx + t * 16, tag, pa[t]); }
    const int col = (lane & 7) * 8 + ((lane >> 3) & 1) * 4 + (lane >> 4);
    if (a == 0) {
      unsigned gv[24]; int go[24];
#pragma unroll
      for (int i = 0; i < 24; ++i) go[i] = (int)WS_QX + ((h * 8 + i / 3) * 512 + b * 256 + (i % 3) * 64 + lane) * 8;
      gather<0>(gv, go, wsr, tag, ts);
#pragma unroll
      for (int u = 0; u < 3; ++u) { float q = 0.f;
#pragma unroll
        for (int p = 0; p < 8; ++p) q += asf(gv[p * 3 + u]);
        *(LAS u16*)(L + L_Q + (u * 64 + col) * 2) = f2bf(q); }
    }
    STAMP(3);
    barrier();
    const unsigned tB1 = now() - ts;
    __builtin_amdgcn_s_setprio(0);
    if (a == 0) {
      const bool live = (lane & 7) < 6;
      const float bias = bf2f(b1[48 * h + (live ? col : 0)]);
      unsigned gv[16]; int go[16];
#pragma unroll
      for (int i = 0; i < 16; ++i) go[i] = live ? (int)WS_QX + ((h * 8) * 512 + i * 256 + 192 + lane) * 8 : OOB;
      gather<16>(gv, go, wsr, tag, ts);
      float hs[2] = {0.f, 0.f};
#pragma unroll
      for (int p = 0; p < 16; ++p) hs[p & 1] += asf(gv[p]);
      if (live) {
#pragma unroll
        for (int u = 0; u < 2; ++u) { float t = rbf(rbf(hs[u]) + bias); t = t > 0.f ? t : 0.f; *(LAS u16*)(L + L_HA + (u * 48 + col) * 2) = f2bf(t * t); } }
    }
    else {
      float u0 = 0.f;
      for (int rd = 0; rd < 8; ++rd) {
        const unsigned Tk = PT0 + rd * PDT;
        gu64* cl = (gu64*)(ws + WS_CT) + rd * 544;
        while (now() - ts < Tk && lds_ld(ctr) < NU - 24) __builtin_amdgcn_s_sleep(8);
        const unsigned um = lds_ld(ctr);
        if (lane == 0) __hip_atomic_store(cl + blockIdx.x, ((u64)tag << 32) | um, RLX_AGENT);
        if (um >= NU - 24) break;
        while (now() - ts < Tk + PLAG) __builtin_amdgcn_s_sleep(8);
        float s = 0.f; int n = 0;
#pragma unroll
        for (int k = 0; k < (PCL + 63) / 64; ++k) { const u64 v = get(cl + (blockIdx.x & ~(PCL - 1)) + (lane & (PCL - 1)) + 64 * k); const bool ok = (unsigned)(v >> 32) == tag;
          s += ok ? (float)(unsigned)v : 0.f; n += __builtin_popcountll(__ballot(ok)); }
        s += dpp<ROR1>(s); s += dpp<ROR2>(s); s += dpp<ROR4>(s); s += dpp<ROR8>(s); s = tr16(s, s); s = tr32(s, s);
        const float ub = s / (float)n;
        const float tau = rd ? (float)(rd * PDT) / (ub - u0) * (1.f - PEPS * 0.001f) : (float)(PT0 - POFF) / (ub + 6.25f);
        if (rd == 0) u0 = ub;
        const float tend = (float)Tk + ((float)NU - ub) * tau, tnow = (float)(int)(now() - ts), ti = (tend - tnow) / ((float)NU - (float)lds_ld(ctr));
        if (lane == 0 && n > 0 && ti > 0.f && ti < 1000.f) __hip_atomic_store(pace, ((u64)asu(ti) << 32) | asu(tend - (float)NU * ti), RLX_WG);
      }
    }
    int vw[5], v2[4];
#pragma unroll
    for (int i = 0; i < 5; ++i) { const int id = i * 64 + lane, row = id / 10; vw[i] = row * (D * 2) + (id - row * 10) * 16; }
#pragma unroll
    for (int i = 0; i < 4; ++i) { const int id = (4 * a + i) * 64 + lane, row = id / 10; v2[i] = id < 480 ? row * (D * 2) + (id - row * 10) * 16 : OOB; }
    const __amdgpu_buffer_rsrc_t ro = __builtin_amdgcn_make_buffer_rsrc((void*)wo, 0, H * DH * D * 2, RSRC3), r2 = __builtin_amdgcn_make_buffer_rsrc((void*)w2, 0, F * D * 2, RSRC3);
    const int so = (DH * h + 96 * a) * (D * 2) + 160 * j, s2 = 48 * h * (D * 2) + 160 * j;
    SCHED();
    if (PRETOUCH) {
      const unsigned t = __builtin_amdgcn_raw_buffer_load_b32(wsr, a ? (lane < 20 ? (int)WS_RX + (j * 32 + h) * 2560 + lane * 128 : OOB) : (int)WS_PX + (h * 8 + (lane >> 3)) * 2048 + (lane & 7) * 128, 0, AUX_SC1);
      const unsigned t2 = __builtin_amdgcn_raw_buffer_load_b32(wsr, a || (lane & 7) >= 5 ? OOB : (int)WS_PX + (h * 8 + (lane >> 3)) * 2048 + (8 + (lane & 7)) * 128, 0, AUX_SC1);
      asm volatile("" :: "v"(t), "v"(t2)); }
    while (lds_ld(ctr) < NU - WTRIG && now() - ts < SPIN_TICKS) __builtin_amdgcn_s_sleep(2);
    const unsigned tEnd = now() - ts;
    v4u wr[19];
#pragma unroll
    for (int i = 0; i < 19; ++i) {
      wr[i] = i < 15 ? __builtin_amdgcn_raw_buffer_load_b128(ro, vw[i % 5], so + (i / 5) * 32 * (D * 2), AUX_NT) : __builtin_amdgcn_raw_buffer_load_b128(r2, v2[i < 15 ? 0 : i - 15], s2, AUX_NT);
      SCHED(); asm volatile("s_waitcnt vmcnt(" STR(WPACE) ")" ::: "memory"); }
    SCHED();
    STAMP(5);
    LAS unsigned char* wi = L + L_WO + lane * 16;
#pragma unroll
    for (int i = 0; i < 15; ++i) *(LAS v4u*)(wi + (15 * a + i) * 1024) = wr[i];
#pragma unroll
    for (int i = 0; i < 4; ++i) if (i < 3 || v2[3] != OOB) *(LAS v4u*)(wi + (30 + 4 * a + i) * 1024) = wr[15 + i];
    STAMP(6);
    barrier();
    if (a == 1) {
      unsigned gv[8]; int go[8];
#pragma unroll
      for (int i = 0; i < 8; ++i) go[i] = (lane & 15) < 10 ? (int)WS_RX + (j * 32 + h) * 2560 + i * 320 + (lane >> 4) * 80 + (lane & 15) * 8 : OOB;
      __builtin_amdgcn_s_sleep(PD2);
      gather8<GD2, GG>(gv, go, wsr, tag, ts);
      float s = 0.f;
#pragma unroll
      for (int i = 0; i < 8; ++i) s += asf(gv[i]);
      s = tr16(s, s); s = tr32(s, s);
      const float t = rbf(rbf(dpp<0x105>(s)) + bf2f(b2v));
      const float gate = rbf(t / (1.f + __expf(-t)));
      if (lane < 5) out[oidx] = f2bf(bf2f(xres) + rbf(rbf(s) * gate));
      if (bid == 0 && lane == 0) __hip_atomic_store((GAS unsigned*)(ws + WS_EPOCH), tag, RLX_AGENT);
      STAMP(10);
      if (blockIdx.x == 0) {
        const unsigned el = now() - ts; const u64 c1 = __builtin_amdgcn_s_memtime();
        unsigned add = 0;
        if (DIAG == 1) add = el;
        if (DIAG == 2) add = tB1;
        if (DIAG == 3) add = (unsigned)((c1 - c0) * 100ull / el);
        if (DIAG == 4) add = tEnd;
        if (DIAG == 5) add = tX * 10;
        while (now() - ts < 10000u + add) __builtin_amdgcn_s_sleep(1);
      }
    }
  }
  if (CODEWARM) asm volatile(".Lfd_end:");
}

extern "C" void kernel_launch(void* const* d_in, const int* in_sizes, int n_in, void* d_out, int out_size, void* d_ws, size_t ws_size, hipStream_t stream) {
  fused_decode<<<256, NTHR, 0, stream>>>((const u16*)d_in[0], (const u16*)d_in[1], (const u16*)d_in[2], (const u16*)d_in[3], (const u16*)d_in[4],
                                         (const u16*)d_in[6], (const u16*)d_in[7], (const u16*)d_in[8], (const u16*)d_in[9], (u16*)d_out, (unsigned char*)d_ws);
}
```

```cpp
#define DIAG 0
#include <hip/hip_runtime.h>
#include <hip/hip_bf16.h>
#include <stdint.h>

#define NS 6
#define P0 5
#define PACE 1
#define XCDGROUP 0
#define WPACE 7
#define PEPS 5
#define POFF 300
#define PM0 56
#define PDM 48
#define GD 1
#define GG 5
#define WTRIG 0
#define PRETOUCH 1
#define QPRE 0
#define PCL 64
#define PD0 6
#define PD1 6
#define PD2 16
#define GD2 GD
#define PWIN 0
#define PT0 1300
#define PDT 900
#define PLAG 130
#define CODEWARM 1
#define TLBWARM 0
#define STAMPS 0

#define GAS __attribute__((address_space(1)))
#define LAS __attribute__((address_space(3)))
#define RLX_AGENT __ATOMIC_RELAXED, __HIP_MEMORY_SCOPE_AGENT
#define RLX_WG __ATOMIC_RELAXED, __HIP_MEMORY_SCOPE_WORKGROUP
typedef unsigned v4u __attribute__((ext_vector_type(4)));
typedef unsigned v2u __attribute__((ext_vector_type(2)));
typedef short v4s __attribute__((ext_vector_type(4)));
typedef short v8s __attribute__((ext_vector_type(8)));
typedef __bf16 v8bf __attribute__((ext_vector_type(8)));
typedef float f32x4 __attribute__((ext_vector_type(4)));
typedef float f32x2 __attribute__((ext_vector_type(2)));
typedef __bf16 bf16x2 __attribute__((ext_vector_type(2)));
typedef unsigned short u16;
typedef unsigned long long u64;
typedef GAS u64 gu64;

constexpr int NB = 2, D = 640, H = 32, SK = 8192, DH = 192, F = 1536;
constexpr int NWAVE = NS + 2, NTHR = NWAVE * 64;
constexpr int NU = 256;
constexpr int KVBYTES = NB * H * SK * DH * 2;
constexpr int AUX_NT = 2;
#define KAUX 2
#define VAUX 2
constexpr int AUX_kr = KAUX, AUX_vr = VAUX;
constexpr int RSRC3 = 0x00020000;
constexpr size_t WS_EPOCH = 0;
constexpr size_t WS_QX = 4096;
constexpr size_t WS_PX = WS_QX + 256 * 512 * 8;
constexpr size_t WS_RX = WS_PX + 256 * 256 * 8;
constexpr size_t WS_CT = WS_RX + 8 * 32 * 320 * 8;
constexpr size_t WS_ST = WS_CT + 8 * 544 * 8;
constexpr int L_XN = 0;
constexpr int L_Q = L_XN + 4 * 96 * 4;
constexpr int L_HA = L_Q + 384;
constexpr int L_OA = L_HA + 192;
constexpr int L_CT = L_OA + 768;
constexpr int L_MG = L_CT + 32;
constexpr int L_WO = L_MG + NS * 800;
constexpr int L_TL = L_WO + 240 * 160;
constexpr int L_END = L_TL + (STAMPS ? 1024 : 0);
static_assert(L_WO % 16 == 0 && L_OA % 16 == 0 && L_HA % 16 == 0 && L_Q % 16 == 0 && L_CT % 16 == 0, "alignment");
constexpr unsigned SPIN_TICKS = 2000000u;

__device__ __forceinline__ float bf2f(u16 b) { return __builtin_bit_cast(float, (unsigned)b << 16); }
__device__ __forceinline__ u16 f2bf(float f) { return __builtin_bit_cast(u16, (__bf16)f); }
__device__ __forceinline__ float rbf(float f) { return bf2f(f2bf(f)); }
__device__ __forceinline__ float lo16(unsigned v) { return __builtin_bit_cast(float, v << 16); }
__device__ __forceinline__ float hi16(unsigned v) { return __builtin_bit_cast(float, v & 0xffff0000u); }
__device__ __forceinline__ float asf(unsigned v) { return __builtin_bit_cast(float, v); }
__device__ __forceinline__ unsigned asu(float v) { return __builtin_bit_cast(unsigned, v); }
__device__ __forceinline__ float dot2(unsigned k, unsigned q, float acc) {
  return __builtin_amdgcn_fdot2_f32_bf16(__builtin_bit_cast(bf16x2, k), __builtin_bit_cast(bf16x2, q), acc, false);
}
template <int CTRL> __device__ __forceinline__ float dpp(float x) {
  return __builtin_bit_cast(float, __builtin_amdgcn_mov_dpp(__builtin_bit_cast(int, x), CTRL, 0xf, 0xf, true));
}
constexpr int XOR1 = 0xB1, XOR2 = 0x4E, XOR7 = 0x141, ROR8 = 0x128, ROR4 = 0x124, ROR2 = 0x122, ROR1 = 0x121;
__device__ __forceinline__ float tr32(float x, float y) {
  auto s = __builtin_amdgcn_permlane32_swap(asu(x), asu(y), false, false); return asf(s[0]) + asf(s[1]);
}
__device__ __forceinline__ float tr16(float x, float y) {
  auto s = __builtin_amdgcn_permlane16_swap(asu(x), asu(y), false, false); return asf(s[0]) + asf(s[1]);
}
__device__ __forceinline__ float tr8(float x, float y, bool up) {
  const float keep = up ? y : x, send = up ? x : y; return keep + dpp<ROR8>(send);
}
__device__ __forceinline__ float max16(float x) { auto s = __builtin_amdgcn_permlane16_swap(asu(x), asu(x), false, false); return fmaxf(asf(s[0]), asf(s[1])); }
__device__ __forceinline__ float max32(float x) { auto s = __builtin_amdgcn_permlane32_swap(asu(x), asu(x), false, false); return fmaxf(asf(s[0]), asf(s[1])); }
__device__ __forceinline__ unsigned now() { return (unsigned)__builtin_amdgcn_s_memrealtime(); }
__device__ __forceinline__ void barrier() { asm volatile("s_waitcnt lgkmcnt(0)\n\ts_barrier" ::: "memory"); }
__device__ __forceinline__ void put(gu64* g, unsigned tag, float v) { __hip_atomic_store(g, ((u64)tag << 32) | asu(v), RLX_AGENT); }
__device__ __forceinline__ u64 get(const gu64* g) { return __hip_atomic_load(g, RLX_AGENT); }
__device__ __forceinline__ unsigned lds_ld(LAS unsigned* p) { return __builtin_amdgcn_readfirstlane(__hip_atomic_load(p, RLX_WG)); }
constexpr int OOB = 0x7ffffff0, AUX_SC1 = 16;
template <int SLEEP, int N> __device__ __forceinline__ void gather(unsigned (&val)[N], int (&off)[N], __amdgpu_buffer_rsrc_t rs, unsigned tag, unsigned ts) {
#pragma unroll
  for (int i = 0; i < N; ++i) val[i] = 0u;
  for (;;) {
    v2u in[N];
    asm volatile("" ::: "memory");
#pragma unroll
    for (int i = 0; i < N; ++i) in[i] = __builtin_amdgcn_raw_buffer_load_b64(rs, off[i], 0, AUX_SC1);
    int left = OOB;
#pragma unroll
    for (int i = 0; i < N; ++i) { const bool got = in[i][1] == tag; val[i] = got ? in[i][0] : val[i]; off[i] = got ? OOB : off[i]; left = min(left, off[i]); }
    if (__all(left == OOB) || now() - ts > SPIN_TICKS) break;
    if (SLEEP) __builtin_amdgcn_s_sleep(SLEEP);
  }
}
template <int DEPTH, int GAP> __device__ __forceinline__ void gather8(unsigned (&val)[8], int (&off)[8], __amdgpu_buffer_rsrc_t rs, unsigned tag, unsigned ts) {
#pragma unroll
  for (int i = 0; i < 8; ++i) val[i] = 0u;
  unsigned t0, t1; u64 m; const int oob = OOB;
  if constexpr (DEPTH == 1) {
    asm volatile(
      "buffer_load_dwordx2 v[208:209], %8, %[rs], 0 offen sc1\n\t"
      "buffer_load_dwordx2 v[210:211], %9, %[rs], 0 offen sc1\n\t"
      "buffer_load_dwordx2 v[212:213], %10, %[rs], 0 offen sc1\n\t"
      "buffer_load_dwordx2 v[214:215], %11, %[rs], 0 offen sc1\n\t"
      "buffer_load_dwordx2 v[216:217], %12, %[rs], 0 offen sc1\n\t"
      "buffer_load_dwordx2 v[218:219], %13, %[rs], 0 offen sc1\n\t"
      "buffer_load_dwordx2 v[220:221], %14, %[rs], 0 offen sc1\n\t"
      "buffer_load_dwordx2 v[222:223], %15, %[rs], 0 offen sc1\n\t"
      ".Lgloop%=:\n\t"
      "s_waitcnt vmcnt(0)\n\t"
      "v_cmp_eq_u32_e32 vcc, %[tag], v209\n\tv_cmp_eq_u32_e64 %[m], %[tag], v211\n\ts_nop 0\n\t"
      "v_cndmask_b32_e32 %8, %8, %[oob], vcc\n\tv_cndmask_b32_e64 %9, %9, %[oob], %[m]\n\tv_cndmask_b32_e32 %0, %0, v208, vcc\n\tv_cndmask_b32_e64 %1, %1, v210, %[m]\n\t"
      "v_cmp_eq_u32_e32 vcc, %[tag], v213\n\tv_cmp_eq_u32_e64 %[m], %[tag], v215\n\ts_nop 0\n\t"
      "v_cndmask_b32_e32 %10, %10, %[oob], vcc\n\tv_cndmask_b32_e64 %11, %11, %[oob], %[m]\n\tv_cndmask_b32_e32 %2, %2, v212, vcc\n\tv_cndmask_b32_e64 %3, %3, v214, %[m]\n\t"
      "v_cmp_eq_u32_e32 vcc, %[tag], v217\n\tv_cmp_eq_u32_e64 %[m], %[tag], v219\n\ts_nop 0\n\t"
      "v_cndmask_b32_e32 %12, %12, %[oob], vcc\n\tv_cndmask_b32_e64 %13, %13, %[oob], %[m]\n\tv_cndmask_b32_e32 %4, %4, v216, vcc\n\tv_cndmask_b32_e64 %5, %5, v218, %[m]\n\t"
      "v_cmp_eq_u32_e32 vcc, %[tag], v221\n\tv_cmp_eq_u32_e64 %[m], %[tag], v223\n\ts_nop 0\n\t"
      "v_cndmask_b32_e32 %14, %14, %[oob], vcc\n\tv_cndmask_b32_e64 %15, %15, %[oob], %[m]\n\tv_cndmask_b32_e32 %6, %6, v220, vcc\n\tv_cndmask_b32_e64 %7, %7, v222, %[m]\n\t"
      "v_min3_u32 %[t0], %8, %9, %10\n\tv_min3_u32 %[t1], %11, %12, %13\n\tv_min3_u32 %[t0], %[t0], %14, %15\n\tv_min_u32_e32 %[t0], %[t0], %[t1]\n\t"
      "v_cmp_ne_u32_e32 vcc, %[t0], %[oob]\n\ts_nop 1\n\ts_cbranch_vccz .Lgdone%=\n\t"
      "buffer_load_dwordx2 v[208:209], %8, %[rs], 0 offen sc1\n\t"
      "buffer_load_dwordx2 v[210:211], %9, %[rs], 0 offen sc1\n\t"
      "buffer_load_dwordx2 v[212:213], %10, %[rs], 0 offen sc1\n\t"
      "buffer_load_dwordx2 v[214:215], %11, %[rs], 0 offen sc1\n\t"
      "buffer_load_dwordx2 v[216:217], %12, %[rs], 0 offen sc1\n\t"
      "buffer_load_dwordx2 v[218:219], %13, %[rs], 0 offen sc1\n\t"
      "buffer_load_dwordx2 v[220:221], %14, %[rs], 0 offen sc1\n\t"
      "buffer_load_dwordx2 v[222:223], %15, %[rs], 0 offen sc1\n\t"
      "s_memrealtime vcc\n\ts_waitcnt lgkmcnt(0)\n\ts_sub_u32 vcc_lo, vcc_lo, %[ts]\n\ts_cmp_lt_u32 vcc_lo, %[lim]\n\ts_cbranch_scc1 .Lgloop%=\n"
      ".Lgdone%=:"
      : "+v"(val[0]), "+v"(val[1]), "+v"(val[2]), "+v"(val[3]), "+v"(val[4]), "+v"(val[5]), "+v"(val[6]), "+v"(val[7]),
        "+v"(off[0]), "+v"(off[1]), "+v"(off[2]), "+v"(off[3]), "+v"(off[4]), "+v"(off[5]), "+v"(off[6]), "+v"(off[7]),
        [t0] "=&v"(t0), [t1] "=&v"(t1), [m] "=&s"(m)
      : [rs] "s"(rs), [tag] "s"(tag), [oob] "v"(oob), [ts] "s"(ts), [lim] "s"(SPIN_TICKS), [gap] "n"(GAP)
      : "memory", "vcc", "scc", "v208", "v209", "v210", "v211", "v212", "v213", "v214", "v215", "v216", "v217", "v218", "v219", "v220", "v221", "v222", "v223");
  } else if constexpr (DEPTH == 2) {
    asm volatile(
      "buffer_load_dwordx2 v[208:209], %8, %[rs], 0 offen sc1\n\t"
      "buffer_load_dwordx2 v[210:211], %9, %[rs], 0 offen sc1\n\t"
      "buffer_load_dwordx2 v[212:213], %10, %[rs], 0 offen sc1\n\t"
      "buffer_load_dwordx2 v[214:215], %11, %[rs], 0 offen sc1\n\t"
      "buffer_load_dwordx2 v[216:217], %12, %[rs], 0 offen sc1\n\t"
      "buffer_load_dwordx2 v[218:219], %13, %[rs], 0 offen sc1\n\t"
      "buffer_load_dwordx2 v[220:221], %14, %[rs], 0 offen sc1\n\t"
      "buffer_load_dwordx2 v[222:223], %15, %[rs], 0 offen sc1\n\t"
      "s_sleep %[gap]\n\t"
      "buffer_load_dwordx2 v[224:225], %8, %[rs], 0 offen sc1\n\t"
      "buffer_load_dwordx2 v[226:227], %9, %[rs], 0 offen sc1\n\t"
      "buffer_load_dwordx2 v[228:229], %10, %[rs], 0 offen sc1\n\t"
      "buffer_load_dwordx2 v[230:231], %11, %[rs], 0 offen sc1\n\t"
      "buffer_load_dwordx2 v[232:233], %12, %[rs], 0 offen sc1\n\t"
      "buffer_load_dwordx2 v[234:235], %13, %[rs], 0 offen sc1\n\t"
      "buffer_load_dwordx2 v[236:237], %14, %[rs], 0 offen sc1\n\t"
      "buffer_load_dwordx2 v[238:239], %15, %[rs], 0 offen sc1\n\t"
      ".Lgloop%=:\n\t"
      "s_waitcnt vmcnt(8)\n\t"
      "v_cmp_eq_u32_e32 vcc, %[tag], v209\n\tv_cmp_eq_u32_e64 %[m], %[tag], v211\n\ts_nop 0\n\t"
      "v_cndmask_b32_e32 %8, %8, %[oob], vcc\n\tv_cndmask_b32_e64 %9, %9, %[oob], %[m]\n\tv_cndmask_b32_e32 %0, %0, v208, vcc\n\tv_cndmask_b32_e64 %1, %1, v210, %[m]\n\t"
      "v_cmp_eq_u32_e32 vcc, %[tag], v213\n\tv_cmp_eq_u32_e64 %[m], %[tag], v215\n\ts_nop 0\n\t"
      "v_cndmask_b32_e32 %10, %10, %[oob], vcc\n\tv_cndmask_b32_e64 %11, %11, %[oob], %[m]\n\tv_cndmask_b32_e32 %2, %2, v212, vcc\n\tv_cndmask_b32_e64 %3, %3, v214, %[m]\n\t"
      "v_cmp_eq_u32_e32 vcc, %[tag], v217\n\tv_cmp_eq_u32_e64 %[m], %[tag], v219\n\ts_nop 0\n\t"
      "v_cndmask_b32_e32 %12, %12, %[oob], vcc\n\tv_cndmask_b32_e64 %13, %13, %[oob], %[m]\n\tv_cndmask_b32_e32 %4, %4, v216, vcc\n\tv_cndmask_b32_e64 %5, %5, v218, %[m]\n\t"
      "v_cmp_eq_u32_e32 vcc, %[tag], v221\n\tv_cmp_eq_u32_e64 %[m], %[tag], v223\n\ts_nop 0\n\t"
      "v_cndmask_b32_e32 %14, %14, %[oob], vcc\n\tv_cndmask_b32_e64 %15, %15, %[oob], %[m]\n\tv_cndmask_b32_e32 %6, %6, v220, vcc\n\tv_cndmask_b32_e64 %7, %7, v222, %[m]\n\t"
      "v_min3_u32 %[t0], %8, %9, %10\n\tv_min3_u32 %[t1], %11, %12, %13\n\tv_min3_u32 %[t0], %[t0], %14, %15\n\tv_min_u32_e32 %[t0], %[t0], %[t1]\n\t"
      "v_cmp_ne_u32_e32 vcc, %[t0], %[oob]\n\ts_nop 1\n\ts_cbranch_vccz .Lgdone%=\n\t"
      "buffer_load_dwordx2 v[208:209], %8, %[rs], 0 offen sc1\n\t"
      "buffer_load_dwordx2 v[210:211], %9, %[rs], 0 offen sc1\n\t"
      "buffer_load_dwordx2 v[212:213], %10, %[rs], 0 offen sc1\n\t"
      "buffer_load_dwordx2 v[214:215], %11, %[rs], 0 offen sc1\n\t"
      "buffer_load_dwordx2 v[216:217], %12, %[rs], 0 offen sc1\n\t"
      "buffer_load_dwordx2 v[218:219], %13, %[rs], 0 offen sc1\n\t"
      "buffer_load_dwordx2 v[220:221], %14, %[rs], 0 offen sc1\n\t"
      "buffer_load_dwordx2 v[222:223], %15, %[rs], 0 offen sc1\n\t"
      "s_waitcnt vmcnt(8)\n\t"
      "v_cmp_eq_u32_e32 vcc, %[tag], v225\n\tv_cmp_eq_u32_e64 %[m], %[tag], v227\n\ts_nop 0\n\t"
      "v_cndmask_b32_e32 %8, %8, %[oob], vcc\n\tv_cndmask_b32_e64 %9, %9, %[oob], %[m]\n\tv_cndmask_b32_e32 %0, %0, v224, vcc\n\tv_cndmask_b32_e64 %1, %1, v226, %[m]\n\t"
      "v_cmp_eq_u32_e32 vcc, %[tag], v229\n\tv_cmp_eq_u32_e64 %[m], %[tag], v231\n\ts_nop 0\n\t"
      "v_cndmask_b32_e32 %10, %10, %[oob], vcc\n\tv_cndmask_b32_e64 %11, %11, %[oob], %[m]\n\tv_cndmask_b32_e32 %2, %2, v228, vcc\n\tv_cndmask_b32_e64 %3, %3, v230, %[m]\n\t"
      "v_cmp_eq_u32_e32 vcc, %[tag], v233\n\tv_cmp_eq_u32_e64 %[m], %[tag], v235\n\ts_nop 0\n\t"
      "v_cndmask_b32_e32 %12, %12, %[oob], vcc\n\tv_cndmask_b32_e64 %13, %13, %[oob], %[m]\n\tv_cndmask_b32_e32 %4, %4, v232, vcc\n\tv_cndmask_b32_e64 %5, %5, v234, %[m]\n\t"
      "v_cmp_eq_u32_e32 vcc, %[tag], v237\n\tv_cmp_eq_u32_e64 %[m], %[tag], v239\n\ts_nop 0\n\t"
      "v_cndmask_b32_e32 %14, %14, %[oob], vcc\n\tv_cndmask_b32_e64 %15, %15, %[oob], %[m]\n\tv_cndmask_b32_e32 %6, %6, v236, vcc\n\tv_cndmask_b32_e64 %7, %7, v238, %[m]\n\t"
      "v_min3_u32 %[t0], %8, %9, %10\n\tv_min3_u32 %[t1], %11, %12, %13\n\tv_min3_u32 %[t0], %[t0], %14, %15\n\tv_min_u32_e32 %[t0], %[t0], %[t1]\n\t"
      "v_cmp_ne_u32_e32 vcc, %[t0], %[oob]\n\ts_nop 1\n\ts_cbranch_vccz .Lgdone%=\n\t"
      "buffer_load_dwordx2 v[224:225], %8, %[rs], 0 offen sc1\n\t"
      "buffer_load_dwordx2 v[226:227], %9, %[rs], 0 offen sc1\n\t"
      "buffer_load_dwordx2 v[228:229], %10, %[rs], 0 offen sc1\n\t"
      "buffer_load_dwordx2 v[230:231], %11, %[rs], 0 offen sc1\n\t"
      "buffer_load_dwordx2 v[232:233], %12, %[rs], 0 offen sc1\n\t"
      "buffer_load_dwordx2 v[234:235], %13, %[rs], 0 offen sc1\n\t"
      "buffer_load_dwordx2 v[236:237], %14, %[rs], 0 offen sc1\n\t"
      "buffer_load_dwordx2 v[238:239], %15, %[rs], 0 offen sc1\n\t"
      "s_memrealtime vcc\n\ts_waitcnt lgkmcnt(0)\n\ts_sub_u32 vcc_lo, vcc_lo, %[ts]\n\ts_cmp_lt_u32 vcc_lo, %[lim]\n\ts_cbranch_scc1 .Lgloop%=\n"
      ".Lgdone%=:"
      : "+v"(val[0]), "+v"(val[1]), "+v"(val[2]), "+v"(val[3]), "+v"(val[4]), "+v"(val[5]), "+v"(val[6]), "+v"(val[7]),
        "+v"(off[0]), "+v"(off[1]), "+v"(off[2]), "+v"(off[3]), "+v"(off[4]), "+v"(off[5]), "+v"(off[6]), "+v"(off[7]),
        [t0] "=&v"(t0), [t1] "=&v"(t1), [m] "=&s"(m)
      : [rs] "s"(rs), [tag] "s"(tag), [oob] "v"(oob), [ts] "s"(ts), [lim] "s"(SPIN_TICKS), [gap] "n"(GAP)
      : "memory", "vcc", "scc", "v208", "v209", "v210", "v211", "v212", "v213", "v214", "v215", "v216", "v217", "v218", "v219", "v220", "v221", "v222", "v223", "v224", "v225", "v226", "v227", "v228", "v229", "v230", "v231", "v232", "v233", "v234", "v235", "v236", "v237", "v238", "v239");
  } else {
    asm volatile(
      "buffer_load_dwordx2 v[208:209], %8, %[rs], 0 offen sc1\n\t"
      "buffer_load_dwordx2 v[210:211], %9, %[rs], 0 offen sc1\n\t"
      "buffer_load_dwordx2 v[212:213], %10, %[rs], 0 offen sc1\n\t"
      "buffer_load_dwordx2 v[214:215], %11, %[rs], 0 offen sc1\n\t"
      "buffer_load_dwordx2 v[216:217], %12, %[rs], 0 offen sc1\n\t"
      "buffer_load_dwordx2 v[218:219], %13, %[rs], 0 offen sc1\n\t"
      "buffer_load_dwordx2 v[220:221], %14, %[rs], 0 offen sc1\n\t"
      "buffer_load_dwordx2 v[222:223], %15, %[rs], 0 offen sc1\n\t"
      "s_sleep %[gap]\n\t"
      "buffer_load_dwordx2 v[224:225], %8, %[rs], 0 offen sc1\n\t"
      "buffer_load_dwordx2 v[226:227], %9, %[rs], 0 offen sc1\n\t"
      "buffer_load_dwordx2 v[228:229], %10, %[rs], 0 offen sc1\n\t"
      "buffer_load_dwordx2 v[230:231], %11, %[rs], 0 offen sc1\n\t"
      "buffer_load_dwordx2 v[232:233], %12, %[rs], 0 offen sc1\n\t"
      "buffer_load_dwordx2 v[234:235], %13, %[rs], 0 offen sc1\n\t"
      "buffer_load_dwordx2 v[236:237], %14, %[rs], 0 offen sc1\n\t"
      "buffer_load_dwordx2 v[238:239], %15, %[rs], 0 offen sc1\n\t"
      "s_sleep %[gap]\n\t"
      "buffer_load_dwordx2 v[240:241], %8, %[rs], 0 offen sc1\n\t"
      "buffer_load_dwordx2 v[242:243], %9, %[rs], 0 offen sc1\n\t"
      "buffer_load_dwordx2 v[244:245], %10, %[rs], 0 offen sc1\n\t"
      "buffer_load_dwordx2 v[246:247], %11, %[rs], 0 offen sc1\n\t"
      "buffer_load_dwordx2 v[248:249], %12, %[rs], 0 offen sc1\n\t"
      "buffer_load_dwordx2 v[250:251], %13, %[rs], 0 offen sc1\n\t"
      "buffer_load_dwordx2 v[252:253], %14, %[rs], 0 offen sc1\n\t"
      "buffer_load_dwordx2 v[254:255], %15, %[rs], 0 offen sc1\n\t"
      ".Lgloop%=:\n\t"
      "s_waitcnt vmcnt(16)\n\t"
      "v_cmp_eq_u32_e32 vcc, %[tag], v209\n\tv_cmp_eq_u32_e64 %[m], %[tag], v211\n\ts_nop 0\n\t"
      "v_cndmask_b32_e32 %8, %8, %[oob], vcc\n\tv_cndmask_b32_e64 %9, %9, %[oob], %[m]\n\tv_cndmask_b32_e32 %0, %0, v208, vcc\n\tv_cndmask_b32_e64 %1, %1, v210, %[m]\n\t"
      "v_cmp_eq_u32_e32 vcc, %[tag], v213\n\tv_cmp_eq_u32_e64 %[m], %[tag], v215\n\ts_nop 0\n\t"
      "v_cndmask_b32_e32 %10, %10, %[oob], vcc\n\tv_cndmask_b32_e64 %11, %11, %[oob], %[m]\n\tv_cndmask_b32_e32 %2, %2, v212, vcc\n\tv_cndmask_b32_e64 %3, %3, v214, %[m]\n\t"
      "v_cmp_eq_u32_e32 vcc, %[tag], v217\n\tv_cmp_eq_u32_e64 %[m], %[tag], v219\n\ts_nop 0\n\t"
      "v_cndmask_b32_e32 %12, %12, %[oob], vcc\n\tv_cndmask_b32_e64 %13, %13, %[oob], %[m]\n\tv_cndmask_b32_e32 %4, %4, v216, vcc\n\tv_cndmask_b32_e64 %5, %5, v218, %[m]\n\t"
      "v_cmp_eq_u32_e32 vcc, %[tag], v221\n\tv_cmp_eq_u32_e64 %[m], %[tag], v223\n\ts_nop 0\n\t"
      "v_cndmask_b32_e32 %14, %14, %[oob], vcc\n\tv_cndmask_b32_e64 %15, %15, %[oob], %[m]\n\tv_cndmask_b32_e32 %6, %6, v220, vcc\n\tv_cndmask_b32_e64 %7, %7, v222, %[m]\n\t"
      "v_min3_u32 %[t0], %8, %9, %10\n\tv_min3_u32 %[t1], %11, %12, %13\n\tv_min3_u32 %[t0], %[t0], %14, %15\n\tv_min_u32_e32 %[t0], %[t0], %[t1]\n\t"
      "v_cmp_ne_u32_e32 vcc, %[t0], %[oob]\n\ts_nop 1\n\ts_cbranch_vccz .Lgdone%=\n\t"
      "buffer_load_dwordx2 v[208:209], %8, %[rs], 0 offen sc1\n\t"
      "buffer_load_dwordx2 v[210:211], %9, %[rs], 0 offen sc1\n\t"
      "buffer_load_dwordx2 v[212:213], %10, %[rs], 0 offen sc1\n\t"
      "buffer_load_dwordx2 v[214:215], %11, %[rs], 0 offen sc1\n\t"
      "buffer_load_dwordx2 v[216:217], %12, %[rs], 0 offen sc1\n\t"
      "buffer_load_dwordx2 v[218:219], %13, %[rs], 0 offen sc1\n\t"
      "buffer_load_dwordx2 v[220:221], %14, %[rs], 0 offen sc1\n\t"
      "buffer_load_dwordx2 v[222:223], %15, %[rs], 0 offen sc1\n\t"
      "s_waitcnt vmcnt(16)\n\t"
      "v_cmp_eq_u32_e32 vcc, %[tag], v225\n\tv_cmp_eq_u32_e64 %[m], %[tag], v227\n\ts_nop 0\n\t"
      "v_cndmask_b32_e32 %8, %8, %[oob], vcc\n\tv_cndmask_b32_e64 %9, %9, %[oob], %[m]\n\tv_cndmask_b32_e32 %0, %0, v224, vcc\n\tv_cndmask_b32_e64 %1, %1, v226, %[m]\n\t"
      "v_cmp_eq_u32_e32 vcc, %[tag], v229\n\tv_cmp_eq_u32_e64 %[m], %[tag], v231\n\ts_nop 0\n\t"
      "v_cndmask_b32_e32 %10, %10, %[oob], vcc\n\tv_cndmask_b32_e64 %11, %11, %[oob], %[m]\n\tv_cndmask_b32_e32 %2, %2, v228, vcc\n\tv_cndmask_b32_e64 %3, %3, v230, %[m]\n\t"
      "v_cmp_eq_u32_e32 vcc, %[tag], v233\n\tv_cmp_eq_u32_e64 %[m], %[tag], v235\n\ts_nop 0\n\t"
      "v_cndmask_b32_e32 %12, %12, %[oob], vcc\n\tv_cndmask_b32_e64 %13, %13, %[oob], %[m]\n\tv_cndmask_b32_e32 %4, %4, v232, vcc\n\tv_cndmask_b32_e64 %5, %5, v234, %[m]\n\t"
      "v_cmp_eq_u32_e32 vcc, %[tag], v237\n\tv_cmp_eq_u32_e64 %[m], %[tag], v239\n\ts_nop 0\n\t"
      "v_cndmask_b32_e32 %14, %14, %[oob], vcc\n\tv_cndmask_b32_e64 %15, %15, %[oob], %[m]\n\tv_cndmask_b32_e32 %6, %6, v236, vcc\n\tv_cndmask_b32_e64 %7, %7, v238, %[m]\n\t"
      "v_min3_u32 %[t0], %8, %9, %10\n\tv_min3_u32 %[t1], %11, %12, %13\n\tv_min3_u32 %[t0], %[t0], %14, %15\n\tv_min_u32_e32 %[t0], %[t0], %[t1]\n\t"
      "v_cmp_ne_u32_e32 vcc, %[t0], %[oob]\n\ts_nop 1\n\ts_cbranch_vccz .Lgdone%=\n\t"
      "buffer_load_dwordx2 v[224:225], %8, %[rs], 0 offen sc1\n\t"
      "buffer_load_dwordx2 v[226:227], %9, %[rs], 0 offen sc1\n\t"
      "buffer_load_dwordx2 v[228:229], %10, %[rs], 0 offen sc1\n\t"
      "buffer_load_dwordx2 v[230:231], %11, %[rs], 0 offen sc1\n\t"
      "buffer_load_dwordx2 v[232:233], %12, %[rs], 0 offen sc1\n\t"
      "buffer_load_dwordx2 v[234:235], %13, %[rs], 0 offen sc1\n\t"
      "buffer_load_dwordx2 v[236:237], %14, %[rs], 0 offen sc1\n\t"
      "buffer_load_dwordx2 v[238:239], %15, %[rs], 0 offen sc1\n\t"
      "s_waitcnt vmcnt(16)\n\t"
      "v_cmp_eq_u32_e32 vcc, %[tag], v241\n\tv_cmp_eq_u32_e64 %[m], %[tag], v243\n\ts_nop 0\n\t"
      "v_cndmask_b32_e32 %8, %8, %[oob], vcc\n\tv_cndmask_b32_e64 %9, %9, %[oob], %[m]\n\tv_cndmask_b32_e32 %0, %0, v240, vcc\n\tv_cndmask_b32_e64 %1, %1, v242, %[m]\n\t"
      "v_cmp_eq_u32_e32 vcc, %[tag], v245\n\tv_cmp_eq_u32_e64 %[m], %[tag], v247\n\ts_nop 0\n\t"
      "v_cndmask_b32_e32 %10, %10, %[oob], vcc\n\tv_cndmask_b32_e64 %11, %11, %[oob], %[m]\n\tv_cndmask_b32_e32 %2, %2, v244, vcc\n\tv_cndmask_b32_e64 %3, %3, v246, %[m]\n\t"
      "v_cmp_eq_u32_e32 vcc, %[tag], v249\n\tv_cmp_eq_u32_e64 %[m], %[tag], v251\n\ts_nop 0\n\t"
      "v_cndmask_b32_e32 %12, %12, %[oob], vcc\n\tv_cndmask_b32_e64 %13, %13, %[oob], %[m]\n\tv_cndmask_b32_e32 %4, %4, v248, vcc\n\tv_cndmask_b32_e64 %5, %5, v250, %[m]\n\t"
      "v_cmp_eq_u32_e32 vcc, %[tag], v253\n\tv_cmp_eq_u32_e64 %[m], %[tag], v255\n\ts_nop 0\n\t"
      "v_cndmask_b32_e32 %14, %14, %[oob], vcc\n\tv_cndmask_b32_e64 %15, %15, %[oob], %[m]\n\tv_cndmask_b32_e32 %6, %6, v252, vcc\n\tv_cndmask_b32_e64 %7, %7, v254, %[m]\n\t"
      "v_min3_u32 %[t0], %8, %9, %10\n\tv_min3_u32 %[t1], %11, %12, %13\n\tv_min3_u32 %[t0], %[t0], %14, %15\n\tv_min_u32_e32 %[t0], %[t0], %[t1]\n\t"
      "v_cmp_ne_u32_e32 vcc, %[t0], %[oob]\n\ts_nop 1\n\ts_cbranch_vccz .Lgdone%=\n\t"
      "buffer_load_dwordx2 v[240:241], %8, %[rs], 0 offen sc1\n\t"
      "buffer_load_dwordx2 v[242:243], %9, %[rs], 0 offen sc1\n\t"
      "buffer_load_dwordx2 v[244:245], %10, %[rs], 0 offen sc1\n\t"
      "buffer_load_dwordx2 v[246:247], %11, %[rs], 0 offen sc1\n\t"
      "buffer_load_dwordx2 v[248:249], %12, %[rs], 0 offen sc1\n\t"
      "buffer_load_dwordx2 v[250:251], %13, %[rs], 0 offen sc1\n\t"
      "buffer_load_dwordx2 v[252:253], %14, %[rs], 0 offen sc1\n\t"
      "buffer_load_dwordx2 v[254:255], %15, %[rs], 0 offen sc1\n\t"
      "s_memrealtime vcc\n\ts_waitcnt lgkmcnt(0)\n\ts_sub_u32 vcc_lo, vcc_lo, %[ts]\n\ts_cmp_lt_u32 vcc_lo, %[lim]\n\ts_cbranch_scc1 .Lgloop%=\n"
      ".Lgdone%=:"
      : "+v"(val[0]), "+v"(val[1]), "+v"(val[2]), "+v"(val[3]), "+v"(val[4]), "+v"(val[5]), "+v"(val[6]), "+v"(val[7]),
        "+v"(off[0]), "+v"(off[1]), "+v"(off[2]), "+v"(off[3]), "+v"(off[4]), "+v"(off[5]), "+v"(off[6]), "+v"(off[7]),
        [t0] "=&v"(t0), [t1] "=&v"(t1), [m] "=&s"(m)
      : [rs] "s"(rs), [tag] "s"(tag), [oob] "v"(oob), [ts] "s"(ts), [lim] "s"(SPIN_TICKS), [gap] "n"(GAP)
      : "memory", "vcc", "scc", "v208", "v209", "v210", "v211", "v212", "v213", "v214", "v215", "v216", "v217", "v218", "v219", "v220", "v221", "v222", "v223", "v224", "v225", "v226", "v227", "v228", "v229", "v230", "v231", "v232", "v233", "v234", "v235", "v236", "v237", "v238", "v239", "v240", "v241", "v242", "v243", "v244", "v245", "v246", "v247", "v248", "v249", "v250", "v251", "v252", "v253", "v254", "v255");
  }
}
#define PW 2
#define STR2(x) #x
#define STR(x) STR2(x)
#define PWAIT() asm volatile("s_waitcnt vmcnt(" STR(PW) ")" ::: "memory")
#define SCHED() do { asm volatile("" ::: "memory"); __builtin_amdgcn_sched_barrier(0); } while (0)
#define STAMP(i) do {} while (0)
#define UT(u) do {} while (0)

__global__ void __launch_bounds__(NTHR, 2)
fused_decode(const u16* x, const u16* kc, const u16* vc, const u16* wq, const u16* wo,
             const u16* w1, const u16* b1, const u16* w2, const u16* b2, u16* out, unsigned char* ws) {
  __shared__ __attribute__((aligned(16))) unsigned char lds_raw[L_END];
  LAS unsigned char* L = (LAS unsigned char*)lds_raw;
  const int bid = blockIdx.x;
  const int h = bid >> 3, j = bid & 7, b = j >> 2, sp = j & 3;
  const int tid = threadIdx.x, lane = tid & 63, w = __builtin_amdgcn_readfirstlane(tid >> 6), r = lane >> 3, c = lane & 7;
  u64 tfirst; asm volatile("s_memrealtime %0\n\ts_waitcnt lgkmcnt(0)" : "=s"(tfirst) :: "memory");
  SCHED();
  unsigned warm = 0;
  if (CODEWARM && w == 0) {
    u64 pc; unsigned len;
    asm volatile("s_getpc_b64 %0\n.Lfd_here:\n\ts_mov_b32 %1, .Lfd_end-.Lfd_here" : "=s"(pc), "=s"(len));
    const unsigned off = min((unsigned)(((blockIdx.x >> 3) & 3) * 8192 + lane * 128), len - 4u) & ~3u;
    warm = *(const GAS unsigned*)((pc & ~127ull) + off);
  }
  SCHED();
  LAS unsigned* ctr = (LAS unsigned*)(L + L_CT);
  LAS u64* pace = (LAS u64*)(L + L_CT + 8);
  LAS unsigned* done = (LAS unsigned*)(L + L_CT + 16);
  const unsigned ts = now();
  const u64 c0 = __builtin_amdgcn_s_memtime();
  STAMP(0);
  unsigned tag;
  constexpr float C2 = 0.07216878364870322f * 1.4426950408889634f;

  const __amdgpu_buffer_rsrc_t wsr = __builtin_amdgcn_make_buffer_rsrc((void*)ws, 0, (int)WS_ST, RSRC3);

  if (w < NS) {
    if (tid == 0) { *ctr = NS * P0; *pace = 0ull; *done = 0u; }
    const int rbase = ((b * H + h) * SK + sp * 8) * (DH * 2);
    constexpr int USTR = 4 * 8 * DH * 2;
    const int voff = r * (DH * 2) + c * 16;
    v4u Kb[P0][3], Vb[P0][3];
    const __amdgpu_buffer_rsrc_t kr = __builtin_amdgcn_make_buffer_rsrc((void*)kc, 0, KVBYTES, RSRC3), vr = __builtin_amdgcn_make_buffer_rsrc((void*)vc, 0, KVBYTES, RSRC3);
#define LD(dst, rs, t) { const int so_ = rbase + (t) * USTR; \
    _Pragma("unroll") for (int u = 0; u < 3; ++u) dst[u] = __builtin_amdgcn_raw_buffer_load_b128(rs, voff + u * 128, so_, AUX_##rs); }
#pragma unroll
    for (int i = 0; i < P0; ++i) {
      LD(Kb[i], kr, w + i * NS) UT(w + i * NS); SCHED(); PWAIT(); LD(Vb[i], vr, w + i * NS) SCHED(); if (i + 1 < P0) PWAIT(); }
    SCHED();
    STAMP(1);
    barrier();
    STAMP(2);
    asm volatile("" :: "v"(warm));
    tag = lds_ld(ctr + 1);
    v4u qv[3];
#pragma unroll
    for (int u = 0; u < 3; ++u) qv[u] = *(LAS v4u*)(L + L_Q + u * 128 + c * 16);
    float m = -INFINITY, l = 0.f; f32x2 ac2[12];
#pragma unroll
    for (int k = 0; k < 12; ++k) ac2[k] = f32x2{0.f, 0.f};
#define QK(Kx, s) { float d0 = 0.f, d1 = 0.f; \
      _Pragma("unroll") for (int u = 0; u < 3; ++u) { d0 = dot2(Kx[u][0], qv[u][0], d0); d1 = dot2(Kx[u][1], qv[u][1], d1); d0 = dot2(Kx[u][2], qv[u][2], d0); d1 = dot2(Kx[u][3], qv[u][3], d1); } \
      s = d0 + d1; asm volatile("" : "+v"(s)); }
#define PV(Vx, s) { s += dpp<XOR1>(s); s += dpp<XOR2>(s); s += dpp<XOR7>(s); s *= C2; \
      const float mn = fmaxf(m, s), al = __builtin_amdgcn_exp2f(m - mn), p = __builtin_amdgcn_exp2f(s - mn); m = mn; l = fmaf(l, al, p); \
      _Pragma("unroll") for (int u = 0; u < 3; ++u) { SCHED(); _Pragma("unroll") for (int k = 0; k < 4; ++k) \
          ac2[u * 4 + k] = __builtin_elementwise_fma(f32x2{p, p}, f32x2{lo16(Vx[u][k]), hi16(Vx[u][k])}, ac2[u * 4 + k] * al); \
        asm volatile("" : "+v"(ac2[u * 4]), "+v"(ac2[u * 4 + 1]), "+v"(ac2[u * 4 + 2]), "+v"(ac2[u * 4 + 3])); } }
    const unsigned ctra = L_CT + (unsigned)(uintptr_t)L, one = 1u;
#define COUNT() asm volatile("s_mov_b64 exec, 1\n\tds_add_u32 %0, %1\n\ts_mov_b64 exec, -1" :: "v"(ctra), "v"(one) : "memory")
    float s;
    int nxt = w + NS * P0;
#define STEP() { QK(Kb[0], s) SCHED(); LD(Kb[0], kr, nxt) COUNT(); UT(nxt); SCHED(); PV(Vb[0], s) SCHED(); LD(Vb[0], vr, nxt) SCHED(); nxt += NS; }
    static_assert(NS * (P0 + P0 / 2 + 1) <= NU, "the first steps load real units");
    STEP()
#pragma unroll
    for (int i = 1; i < P0; ++i) { QK(Kb[i], s) PV(Vb[i], s) SCHED(); if (i % 2 == 0) STEP() }
    STAMP(3);
    while (nxt < NU) {
      QK(Kb[0], s)
#pragma unroll 1
      for (int spn = 0; spn < 256; ++spn) {
        const u64 pv = __hip_atomic_load(pace, RLX_WG);
        const float t0 = asf(__builtin_amdgcn_readfirstlane((unsigned)pv)), tau = asf(__builtin_amdgcn_readfirstlane((unsigned)(pv >> 32)));
        if (fmaf((float)nxt, tau, t0) <= (float)(int)(now() - ts)) break;
        __builtin_amdgcn_s_sleep(2);
      }
      SCHED(); LD(Kb[0], kr, nxt) COUNT(); UT(nxt); SCHED();
      PV(Vb[0], s)
      SCHED(); LD(Vb[0], vr, nxt) SCHED();
      nxt += NS;
    }
    QK(Kb[0], s)
    PV(Vb[0], s)
    STAMP(4);
    float M = fmaxf(m, dpp<ROR8>(m)); M = max16(M); M = max32(M);
    const float sc = __builtin_amdgcn_exp2f(m - M);
    l *= sc; l += dpp<ROR8>(l); l = tr16(l, l); l = tr32(l, l);
    float acc[24];
#pragma unroll
    for (int k = 0; k < 12; ++k) { const f32x2 t = ac2[k] * sc; acc[2 * k] = t[0]; acc[2 * k + 1] = t[1]; }
#pragma unroll
    for (int k = 0; k < 12; ++k) acc[k] = tr32(acc[k], acc[k + 12]);
#pragma unroll
    for (int k = 0; k < 6; ++k) acc[k] = tr16(acc[k], acc[k + 6]);
#pragma unroll
    for (int k = 0; k < 3; ++k) acc[k] = tr8(acc[k], acc[k + 3], r & 1);
    LAS float* mg = (LAS float*)(L + L_MG) + w * 200;
#pragma unroll
    for (int k = 0; k < 3; ++k) mg[k * 64 + lane] = acc[k];
    if (lane == 0) { mg[192] = M; mg[193] = l; }
    asm volatile("s_waitcnt lgkmcnt(0)\n\ts_mov_b64 exec, 1\n\tds_add_u32 %0, %1 offset:16\n\ts_mov_b64 exec, -1" :: "v"(ctra), "v"(one) : "memory");
    while (lds_ld(done) < NS && now() - ts < SPIN_TICKS) {}
    asm volatile("" ::: "memory");
    if (w >= 3) __builtin_amdgcn_s_sleep(PD0);
    else {
      STAMP(5);
      const LAS float* mg0 = (const LAS float*)(L + L_MG);
      float MM = -INFINITY;
#pragma unroll
      for (int i = 0; i < NS; ++i) MM = fmaxf(MM, mg0[i * 200 + 192]);
      float Ls = 0.f, o = 0.f;
#pragma unroll
      for (int i = 0; i < NS; ++i) { const float e = __builtin_amdgcn_exp2f(mg0[i * 200 + 192] - MM); Ls = fmaf(e, mg0[i * 200 + 193], Ls); o = fmaf(e, mg0[i * 200 + tid], o); }
      gu64* px = (gu64*)(ws + WS_PX) + bid * 256;
      put(px + tid, tag, o / Ls);
      if (tid == 0) put(px + 192, tag, MM + __builtin_amdgcn_logf(Ls));
    }
    STAMP(6);
    static_assert(NS == 6, "waves 0..2 | 3..5 gather batch row 0 | 1");
    const int bb = w >= 3, pos = tid - bb * 192;
    const int vo1 = (int)WS_PX + (h * 8 + bb * 4) * 2048;
    const int gq = lane >> 4, vb = gq & 1, which = gq >> 1, oi = vb * 80 + 16 * w + (lane & 15), hp = oi / 5;
    gu64* rx = (gu64*)(ws + WS_RX) + ((j * 32 + hp) * 32 + h) * 10 + which * 5 + (oi - hp * 5);
    const int di = (lane >> 5) * 12 + ((lane >> 4) & 1) * 6 + ((lane >> 3) & 1) * 3 + (w - 3 * bb);
    LAS u16* oa = (LAS u16*)(L + L_OA + (bb * DH + (di >> 3) * 64 + c * 8 + (di & 7)) * 2);
    unsigned gv[8]; int go[8];
#pragma unroll
    for (int i = 0; i < 8; ++i) go[i] = vo1 + (i & 3) * 2048 + (i < 4 ? pos : 192) * 8;
    __builtin_amdgcn_s_sleep(PD1);
    gather8<GD, GG>(gv, go, wsr, tag, ts);
    { const float e0 = asf(gv[4]), e1 = asf(gv[5]), e2 = asf(gv[6]), e3 = asf(gv[7]);
      const float em = fmaxf(fmaxf(e0, e1), fmaxf(e2, e3));
      const float w0 = __builtin_amdgcn_exp2f(e0 - em), w1v = __builtin_amdgcn_exp2f(e1 - em), w2v = __builtin_amdgcn_exp2f(e2 - em), w3 = __builtin_amdgcn_exp2f(e3 - em);
      *oa = f2bf((w0 * asf(gv[0]) + w1v * asf(gv[1]) + w2v * asf(gv[2]) + w3 * asf(gv[3])) / (w0 + w1v + w2v + w3));
    }
    STAMP(8);
    barrier();
    STAMP(7);
    if (w < 5) {
      const LAS unsigned char* base = L + L_WO + (((lane & 15) >> 2) + 8 * (lane >> 4)) * 160 + w * 32 + (lane & 3) * 8;
      const LAS unsigned char* ao = L + L_OA + (lane & 1) * (DH * 2) + (lane >> 4) * 16;
      const LAS unsigned char* ah = L + L_HA + (lane & 1) * 96;
#define TRB(off) __builtin_amdgcn_ds_read_tr16_b64_v4i16((LAS v4s*)(base + (off)))
#define BFRAG(k) __builtin_bit_cast(v8bf, __builtin_shufflevector(TRB((k) * 32 * 160), TRB((k) * 32 * 160 + 4 * 160), 0, 1, 2, 3, 4, 5, 6, 7))
      const f32x4 Z = {0.f, 0.f, 0.f, 0.f};
      f32x4 S = Z;
#pragma unroll
      for (int k = 0; k < 6; ++k) S = __builtin_amdgcn_mfma_f32_16x16x32_bf16(*(const LAS v8bf*)(ao + k * 64), BFRAG(k), S, 0, 0, 0);
      f32x4 Ga = __builtin_amdgcn_mfma_f32_16x16x32_bf16(*(const LAS v8bf*)(ah + (lane >> 4) * 16), BFRAG(6), Z, 0, 0, 0);
      f32x4 Gb = __builtin_amdgcn_mfma_f32_16x16x16bf16_1k(*(const LAS v4s*)(ah + 64 + (lane >> 4) * 8), TRB(7 * 32 * 160 - 4 * (lane >> 4) * 160), Z, 0, 0, 0);
      asm volatile("" : "+v"(S), "+v"(Ga), "+v"(Gb));
      put(rx, tag, which ? (vb ? Ga[1] + Gb[1] : Ga[0] + Gb[0]) : (vb ? S[1] : S[0]));
      STAMP(9);
    }
  } else {
    __builtin_amdgcn_s_setprio(3);
    const int a = w - NS;
    asm volatile("" :: "s"(x), "s"(ws), "s"(wq));
    u64 tArg64; asm volatile("s_waitcnt lgkmcnt(0)\n\ts_memrealtime %0\n\ts_waitcnt lgkmcnt(0)" : "=s"(tArg64) :: "memory"); const unsigned tArg = (unsigned)tArg64;
    const unsigned epoch = __hip_atomic_load((GAS unsigned*)(ws + WS_EPOCH), RLX_AGENT);
    const unsigned pre = QPRE < 0 ? 0u : __builtin_amdgcn_raw_buffer_load_b32(wsr, lane < 16 ? (int)WS_QX + (bid * 512 + (lane >> 3) * 256 + a * 128) * 8 + (lane & 7) * 128 : (a && lane < 24  && PACE ? (int)WS_CT + ((lane - 16) * 544 + blockIdx.x) * 8 : OOB), 0, QPRE < 0 ? 0 : QPRE);
    const int xb = lane >> 5, xl = lane & 31;
    const __amdgpu_buffer_rsrc_t xr = __builtin_amdgcn_make_buffer_rsrc((void*)x, 0, NB * D * 2, RSRC3);
    const int xo = xb * (D * 2) + xl * 40;
    const v4u x0 = __builtin_amdgcn_raw_buffer_load_b128(xr, xo, 0, 0), x1 = __builtin_amdgcn_raw_buffer_load_b128(xr, xo + 16, 0, 0);
    const v2u x2 = __builtin_amdgcn_raw_buffer_load_b64(xr, xo + 32, 0, 0);
    u16 xs[3];
#pragma unroll
    for (int i = 0; i < 3; ++i) xs[i] = __builtin_amdgcn_raw_buffer_load_b16(xr, xl + 32 * i < 80 ? xb * (D * 2) + (80 * j + xl + 32 * i) * 2 : OOB, 0, 0);
    const int oi = 5 * h + (lane < 5 ? lane : 0), ocol = 80 * j + oi % 80, oidx = (oi / 80) * D + ocol;
    const u16 xres = x[oidx], b2v = b2[ocol];
    v4u wa[10], wb[10];
    { const __amdgpu_buffer_rsrc_t ra = __builtin_amdgcn_make_buffer_rsrc((void*)wq, 0, D * H * DH * 2, RSRC3);
      const __amdgpu_buffer_rsrc_t rb = __builtin_amdgcn_make_buffer_rsrc((void*)(a ? w1 : wq), 0, a ? D * F * 2 : D * H * DH * 2, RSRC3);
      const int sa = H * DH * 2, sb = a ? F * 2 : H * DH * 2;
      const int va = r * sa + c * 16, vb = (a && c >= 6) ? OOB : r * sb + c * 16;
      const int oa = 80 * j * sa + h * (DH * 2) + a * 256, ob = 80 * j * sb + (a ? h * 96 : h * (DH * 2) + 128);
#pragma unroll
      for (int u = 0; u < 10; ++u) { wa[u] = __builtin_amdgcn_raw_buffer_load_b128(ra, va, oa + u * 8 * sa, AUX_NT); wb[u] = __builtin_amdgcn_raw_buffer_load_b128(rb, vb, ob + u * 8 * sb, AUX_NT); }
    }
    SCHED();
    STAMP(1);
    tag = (unsigned)__builtin_amdgcn_readfirstlane(epoch) * 0x9E3779B1u + 0x7F4A7C15u;
    tag = tag ? tag : 1u;
    const unsigned tX = now() - ts;
    if (a == 0 && lane == 0) ctr[1] = tag;
    {
      float v[20];
#pragma unroll
      for (int k = 0; k < 4; ++k) { v[2 * k] = lo16(x0[k]); v[2 * k + 1] = hi16(x0[k]); v[8 + 2 * k] = lo16(x1[k]); v[9 + 2 * k] = hi16(x1[k]); }
      v[16] = lo16(x2[0]); v[17] = hi16(x2[0]); v[18] = lo16(x2[1]); v[19] = hi16(x2[1]);
      float s = 0.f;
#pragma unroll
      for (int k = 0; k < 20; ++k) s += v[k];
      s += dpp<ROR1>(s); s += dpp<ROR2>(s); s += dpp<ROR4>(s); s += dpp<ROR8>(s); s = tr16(s, s);
      const float mean = s * (1.f / D);
      float q = 0.f;
#pragma unroll
      for (int k = 0; k < 20; ++k) { const float d = v[k] - mean; q = fmaf(d, d, q); }
      q += dpp<ROR1>(q); q += dpp<ROR2>(q); q += dpp<ROR4>(q); q += dpp<ROR8>(q); q = tr16(q, q);
      const float mb = rbf(mean), sd = rbf(sqrtf(rbf(rbf(q * (1.f / D)) + 1e-5f)));
      LAS float* xn = (LAS float*)(L + L_XN) + (a * 2 + xb) * 96 + xl;
#pragma unroll
      for (int i = 0; i < 3; ++i) xn[32 * i] = rbf(rbf(bf2f(xs[i]) - mb) / sd);
    }
    asm volatile("s_waitcnt lgkmcnt(0)" :: "v"(pre) : "memory");
    f32x2 p2[16];
#pragma unroll
    for (int k = 0; k < 16; ++k) p2[k] = f32x2{0.f, 0.f};
    { const LAS float* xn = (const LAS float*)(L + L_XN) + a * 192 + r;
#pragma unroll
      for (int u = 0; u < 10; ++u) { const float xa = xn[8 * u], xc = xn[96 + 8 * u];
#pragma unroll
        for (int k = 0; k < 4; ++k) {
          const f32x2 fa = {lo16(wa[u][k]), hi16(wa[u][k])}, fb = {lo16(wb[u][k]), hi16(wb[u][k])};
          p2[k] = __builtin_elementwise_fma(f32x2{xa, xa}, fa, p2[k]); p2[4 + k] = __builtin_elementwise_fma(f32x2{xa, xa}, fb, p2[4 + k]);
          p2[8 + k] = __builtin_elementwise_fma(f32x2{xc, xc}, fa, p2[8 + k]); p2[12 + k] = __builtin_elementwise_fma(f32x2{xc, xc}, fb, p2[12 + k]);
        } } }
    float pa[32];
#pragma unroll
    for (int k = 0; k < 16; ++k) { pa[2 * k] = p2[k][0]; pa[2 * k + 1] = p2[k][1]; }
#pragma unroll
    for (int k = 0; k < 16; ++k) pa[k] = tr32(pa[k], pa[k + 16]);
#pragma unroll
    for (int k = 0; k < 8; ++k) pa[k] = tr16(pa[k], pa[k + 8]);
#pragma unroll
    for (int k = 0; k < 4; ++k) pa[k] = tr8(pa[k], pa[k + 4], r & 1);
    STAMP(2);
    { gu64* qx = (gu64*)(ws + WS_QX) + bid * 512 + (lane >> 5) * 256 + (2 * a + ((lane >> 4) & 1)) * 64 + (lane & 15);
#pragma unroll
      for (int t = 0; t < 4; ++t) put(qx + t * 16, tag, pa[t]); }
    const int col = (lane & 7) * 8 + ((lane >> 3) & 1) * 4 + (lane >> 4);
    if (a == 0) {
      unsigned gv[24]; int go[24];
#pragma unroll
      for (int i = 0; i < 24; ++i) go[i] = (int)WS_QX + ((h * 8 + i / 3) * 512 + b * 256 + (i % 3) * 64 + lane) * 8;
      gather<0>(gv, go, wsr, tag, ts);
#pragma unroll
      for (int u = 0; u < 3; ++u) { float q = 0.f;
#pragma unroll
        for (int p = 0; p < 8; ++p) q += asf(gv[p * 3 + u]);
        *(LAS u16*)(L + L_Q + (u * 64 + col) * 2) = f2bf(q); }
    }
    STAMP(3);
    barrier();
    const unsigned tB1 = now() - ts;
    __builtin_amdgcn_s_setprio(0);
    if (a == 0) {
      const bool live = (lane & 7) < 6;
      const float bias = bf2f(b1[48 * h + (live ? col : 0)]);
      unsigned gv[16]; int go[16];
#pragma unroll
      for (int i = 0; i < 16; ++i) go[i] = live ? (int)WS_QX + ((h * 8) * 512 + i * 256 + 192 + lane) * 8 : OOB;
      gather<16>(gv, go, wsr, tag, ts);
      float hs[2] = {0.f, 0.f};
#pragma unroll
      for (int p = 0; p < 16; ++p) hs[p & 1] += asf(gv[p]);
      if (live) {
#pragma unroll
        for (int u = 0; u < 2; ++u) { float t = rbf(rbf(hs[u]) + bias); t = t > 0.f ? t : 0.f; *(LAS u16*)(L + L_HA + (u * 48 + col) * 2) = f2bf(t * t); } }
    }
    else {
      float u0 = 0.f;
      for (int rd = 0; rd < 8; ++rd) {
        const unsigned Tk = PT0 + rd * PDT;
        gu64* cl = (gu64*)(ws + WS_CT) + rd * 544;
        while (now() - ts < Tk && lds_ld(ctr) < NU - 24) __builtin_amdgcn_s_sleep(8);
        const unsigned um = lds_ld(ctr);
        if (lane == 0) __hip_atomic_store(cl + blockIdx.x, ((u64)tag << 32) | um, RLX_AGENT);
        if (um >= NU - 24) break;
        while (now() - ts < Tk + PLAG) __builtin_amdgcn_s_sleep(8);
        float s = 0.f; int n = 0;
#pragma unroll
        for (int k = 0; k < (PCL + 63) / 64; ++k) { const u64 v = get(cl + (blockIdx.x & ~(PCL - 1)) + (lane & (PCL - 1)) + 64 * k); const bool ok = (unsigned)(v >> 32) == tag;
          s += ok ? (float)(unsigned)v : 0.f; n += __builtin_popcountll(__ballot(ok)); }
        s += dpp<ROR1>(s); s += dpp<ROR2>(s); s += dpp<ROR4>(s); s += dpp<ROR8>(s); s = tr16(s, s); s = tr32(s, s);
        const float ub = s / (float)n;
        const float tau = rd ? (float)(rd * PDT) / (ub - u0) * (1.f - PEPS * 0.001f) : (float)(PT0 - POFF) / (ub + 6.25f);
        if (rd == 0) u0 = ub;
        const float tend = (float)Tk + ((float)NU - ub) * tau, tnow = (float)(int)(now() - ts), ti = (tend - tnow) / ((float)NU - (float)lds_ld(ctr));
        if (lane == 0 && n > 0 && ti > 0.f && ti < 1000.f) __hip_atomic_store(pace, ((u64)asu(ti) << 32) | asu(tend - (float)NU * ti), RLX_WG);
      }
    }
    int vw[5], v2[4];
#pragma unroll
    for (int i = 0; i < 5; ++i) { const int id = i * 64 + lane, row = id / 10; vw[i] = row * (D * 2) + (id - row * 10) * 16; }
#pragma unroll
    for (int i = 0; i < 4; ++i) { const int id = (4 * a + i) * 64 + lane, row = id / 10; v2[i] = id < 480 ? row * (D * 2) + (id - row * 10) * 16 : OOB; }
    const __amdgpu_buffer_rsrc_t ro = __builtin_amdgcn_make_buffer_rsrc((void*)wo, 0, H * DH * D * 2, RSRC3), r2 = __builtin_amdgcn_make_buffer_rsrc((void*)w2, 0, F * D * 2, RSRC3);
    const int so = (DH * h + 96 * a) * (D * 2) + 160 * j, s2 = 48 * h * (D * 2) + 160 * j;
    SCHED();
    if (PRETOUCH) {
      const unsigned t = __builtin_amdgcn_raw_buffer_load_b32(wsr, a ? (lane < 20 ? (int)WS_RX + (j * 32 + h) * 2560 + lane * 128 : OOB) : (int)WS_PX + (h * 8 + (lane >> 3)) * 2048 + (lane & 7) * 128, 0, AUX_SC1);
      const unsigned t2 = __builtin_amdgcn_raw_buffer_load_b32(wsr, a || (lane & 7) >= 5 ? OOB : (int)WS_PX + (h * 8 + (lane >> 3)) * 2048 + (8 + (lane & 7)) * 128, 0, AUX_SC1);
      asm volatile("" :: "v"(t), "v"(t2)); }
    while (lds_ld(ctr) < NU - WTRIG && now() - ts < SPIN_TICKS) __builtin_amdgcn_s_sleep(2);
    const unsigned tEnd = now() - ts;
    v4u wr[19];
#pragma unroll
    for (int i = 0; i < 19; ++i) {
      wr[i] = i < 15 ? __builtin_amdgcn_raw_buffer_load_b128(ro, vw[i % 5], so + (i / 5) * 32 * (D * 2), AUX_NT) : __builtin_amdgcn_raw_buffer_load_b128(r2, v2[i < 15 ? 0 : i - 15], s2, AUX_NT);
      SCHED(); asm volatile("s_waitcnt vmcnt(" STR(WPACE) ")" ::: "memory"); }
    SCHED();
    STAMP(5);
    LAS unsigned char* wi = L + L_WO + lane * 16;
#pragma unroll
    for (int i = 0; i < 15; ++i) *(LAS v4u*)(wi + (15 * a + i) * 1024) = wr[i];
#pragma unroll
    for (int i = 0; i < 4; ++i) if (i < 3 || v2[3] != OOB) *(LAS v4u*)(wi + (30 + 4 * a + i) * 1024) = wr[15 + i];
    STAMP(6);
    barrier();
    if (a == 1) {
      unsigned gv[8]; int go[8];
#pragma unroll
      for (int i = 0; i < 8; ++i) go[i] = (lane & 15) < 10 ? (int)WS_RX + (j * 32 + h) * 2560 + i * 320 + (lane >> 4) * 80 + (lane & 15) * 8 : OOB;
      __builtin_amdgcn_s_sleep(PD2);
      gather8<GD2, GG>(gv, go, wsr, tag, ts);
      float s = 0.f;
#pragma unroll
      for (int i = 0; i < 8; ++i) s += asf(gv[i]);
      s = tr16(s, s); s = tr32(s, s);
      const float t = rbf(rbf(dpp<0x105>(s)) + bf2f(b2v));
      const float gate = rbf(t / (1.f + __expf(-t)));
      if (lane < 5) out[oidx] = f2bf(bf2f(xres) + rbf(rbf(s) * gate));
      if (bid == 0 && lane == 0) __hip_atomic_store((GAS unsigned*)(ws + WS_EPOCH), tag, RLX_AGENT);
      STAMP(10);
      if (blockIdx.x == 0) {
        const unsigned el = now() - ts; const u64 c1 = __builtin_amdgcn_s_memtime();
        unsigned add = 0;
        if (DIAG == 1) add = el;
        if (DIAG == 2) add = tB1;
        if (DIAG == 3) add = (unsigned)((c1 - c0) * 100ull / el);
        if (DIAG == 4) add = tEnd;
        if (DIAG == 5) add = tX * 10;
        if (DIAG == 7) add = (ts - (unsigned)tfirst) * 10;
        if (DIAG == 8) add = (tArg - (unsigned)tfirst) * 10;
        while (now() - (DIAG == 6 ? (unsigned)tfirst : ts) < 10000u + add) __builtin_amdgcn_s_sleep(1);
      }
    }
  }
  if (CODEWARM) asm volatile(".Lfd_end:");
}

extern "C" void kernel_launch(void* const* d_in, const int* in_sizes, int n_in, void* d_out, int out_size, void* d_ws, size_t ws_size, hipStream_t stream) {
  fused_decode<<<256, NTHR, 0, stream>>>((const u16*)d_in[0], (const u16*)d_in[1], (const u16*)d_in[2], (const u16*)d_in[3], (const u16*)d_in[4],
                                         (const u16*)d_in[6], (const u16*)d_in[7], (const u16*)d_in[8], (const u16*)d_in[9], (u16*)d_out, (unsigned char*)d_ws);
}
```

```cpp
#define DIAG 0
#include <hip/hip_runtime.h>
#include <hip/hip_bf16.h>
#include <stdint.h>

#define NS 6
#define P0 5
#define PACE 1
#define XCDGROUP 0
#define WPACE 7
#define PEPS 5
#define POFF 300
#define PM0 56
#define PDM 48
#define GD 1
#define GG 5
#define WTRIG 0
#define PRETOUCH 1
#define QPRE 0
#define PCL 64
#define PD0 6
#define PD1 6
#define PD2 16
#define GD2 GD
#define PWIN 0
#define PT0 1300
#define PDT 900
#define PLAG 130
#define CODEWARM 1
#define TLBWARM 0
#define STAMPS 0

#define GAS __attribute__((address_space(1)))
#define LAS __attribute__((address_space(3)))
#define RLX_AGENT __ATOMIC_RELAXED, __HIP_MEMORY_SCOPE_AGENT
#define RLX_WG __ATOMIC_RELAXED, __HIP_MEMORY_SCOPE_WORKGROUP
typedef unsigned v4u __attribute__((ext_vector_type(4)));
typedef unsigned v2u __attribute__((ext_vector_type(2)));
typedef short v4s __attribute__((ext_vector_type(4)));
typedef short v8s __attribute__((ext_vector_type(8)));
typedef __bf16 v8bf __attribute__((ext_vector_type(8)));
typedef float f32x4 __attribute__((ext_vector_type(4)));
typedef float f32x2 __attribute__((ext_vector_type(2)));
typedef __bf16 bf16x2 __attribute__((ext_vector_type(2)));
typedef unsigned short u16;
typedef unsigned long long u64;
typedef GAS u64 gu64;

constexpr int NB = 2, D = 640, H = 32, SK = 8192, DH = 192, F = 1536;
constexpr int NWAVE = NS + 2, NTHR = NWAVE * 64;
constexpr int NU = 256;
constexpr int KVBYTES = NB * H * SK * DH * 2;
constexpr int AUX_NT = 2;
#define KAUX 2
#define VAUX 2
constexpr int AUX_kr = KAUX, AUX_vr = VAUX;
constexpr int RSRC3 = 0x00020000;
constexpr size_t WS_EPOCH = 0;
constexpr size_t WS_QX = 4096;
constexpr size_t WS_PX = WS_QX + 256 * 512 * 8;
constexpr size_t WS_RX = WS_PX + 256 * 256 * 8;
constexpr size_t WS_CT = WS_RX + 8 * 32 * 320 * 8;
constexpr size_t WS_ST = WS_CT + 8 * 544 * 8;
constexpr int L_XN = 0;
constexpr int L_Q = L_XN + 4 * 96 * 4;
constexpr int L_HA = L_Q + 384;
constexpr int L_OA = L_HA + 192;
constexpr int L_CT = L_OA + 768;
constexpr int L_MG = L_CT + 32;
constexpr int L_WO = L_MG + NS * 800;
constexpr int L_TL = L_WO + 240 * 160;
constexpr int L_END = L_TL + (STAMPS ? 1024 : 0);
static_assert(L_WO % 16 == 0 && L_OA % 16 == 0 && L_HA % 16 == 0 && L_Q % 16 == 0 && L_CT % 16 == 0, "alignment");
constexpr unsigned SPIN_TICKS = 2000000u;

__device__ __forceinline__ float bf2f(u16 b) { return __builtin_bit_cast(float, (unsigned)b << 16); }
__device__ __forceinline__ u16 f2bf(float f) { return __builtin_bit_cast(u16, (__bf16)f); }
__device__ __forceinline__ float rbf(float f) { return bf2f(f2bf(f)); }
__device__ __forceinline__ float lo16(unsigned v) { return __builtin_bit_cast(float, v << 16); }
__device__ __forceinline__ float hi16(unsigned v) { return __builtin_bit_cast(float, v & 0xffff0000u); }
__device__ __forceinline__ float asf(unsigned v) { return __builtin_bit_cast(float, v); }
__device__ __forceinline__ unsigned asu(float v) { return __builtin_bit_cast(unsigned, v); }
__device__ __forceinline__ float dot2(unsigned k, unsigned q, float acc) {
  return __builtin_amdgcn_fdot2_f32_bf16(__builtin_bit_cast(bf16x2, k), __builtin_bit_cast(bf16x2, q), acc, false);
}
template <int CTRL> __device__ __forceinline__ float dpp(float x) {
  return __builtin_bit_cast(float, __builtin_amdgcn_mov_dpp(__builtin_bit_cast(int, x), CTRL, 0xf, 0xf, true));
}
constexpr int XOR1 = 0xB1, XOR2 = 0x4E, XOR7 = 0x141, ROR8 = 0x128, ROR4 = 0x124, ROR2 = 0x122, ROR1 = 0x121;
__device__ __forceinline__ float tr32(float x, float y) {
  auto s = __builtin_amdgcn_permlane32_swap(asu(x), asu(y), false, false); return asf(s[0]) + asf(s[1]);
}
__device__ __forceinline__ float tr16(float x, float y) {
  auto s = __builtin_amdgcn_permlane16_swap(asu(x), asu(y), false, false); return asf(s[0]) + asf(s[1]);
}
__device__ __forceinline__ float tr8(float x, float y, bool up) {
  const float keep = up ? y : x, send = up ? x : y; return keep + dpp<ROR8>(send);
}
__device__ __forceinline__ float max16(float x) { auto s = __builtin_amdgcn_permlane16_swap(asu(x), asu(x), false, false); return fmaxf(asf(s[0]), asf(s[1])); }
__device__ __forceinline__ float max32(float x) { auto s = __builtin_amdgcn_permlane32_swap(asu(x), asu(x), false, false); return fmaxf(asf(s[0]), asf(s[1])); }
__device__ __forceinline__ unsigned now() { return (unsigned)__builtin_amdgcn_s_memrealtime(); }
__device__ __forceinline__ void barrier() { asm volatile("s_waitcnt lgkmcnt(0)\n\ts_barrier" ::: "memory"); }
__device__ __forceinline__ void put(gu64* g, unsigned tag, float v) { __hip_atomic_store(g, ((u64)tag << 32) | asu(v), RLX_AGENT); }
__device__ __forceinline__ u64 get(const gu64* g) { return __hip_atomic_load(g, RLX_AGENT); }
__device__ __forceinline__ unsigned lds_ld(LAS unsigned* p) { return __builtin_amdgcn_readfirstlane(__hip_atomic_load(p, RLX_WG)); }
constexpr int OOB = 0x7ffffff0, AUX_SC1 = 16;
template <int SLEEP, int N> __device__ __forceinline__ void gather(unsigned (&val)[N], int (&off)[N], __amdgpu_buffer_rsrc_t rs, unsigned tag, unsigned ts) {
#pragma unroll
  for (int i = 0; i < N; ++i) val[i] = 0u;
  for (;;) {
    v2u in[N];
    asm volatile("" ::: "memory");
#pragma unroll
    for (int i = 0; i < N; ++i) in[i] = __builtin_amdgcn_raw_buffer_load_b64(rs, off[i], 0, AUX_SC1);
    int left = OOB;
#pragma unroll
    for (int i = 0; i < N; ++i) { const bool got = in[i][1] == tag; val[i] = got ? in[i][0] : val[i]; off[i] = got ? OOB : off[i]; left = min(left, off[i]); }
    if (__all(left == OOB) || now() - ts > SPIN_TICKS) break;
    if (SLEEP) __builtin_amdgcn_s_sleep(SLEEP);
  }
}
template <int DEPTH, int GAP> __device__ __forceinline__ void gather8(unsigned (&val)[8], int (&off)[8], __amdgpu_buffer_rsrc_t rs, unsigned tag, unsigned ts) {
#pragma unroll
  for (int i = 0; i < 8; ++i) val[i] = 0u;
  unsigned t0, t1; u64 m; const int oob = OOB;
  if constexpr (DEPTH == 1) {
    asm volatile(
      "buffer_load_dwordx2 v[208:209], %8, %[rs], 0 offen sc1\n\t"
      "buffer_load_dwordx2 v[210:211], %9, %[rs], 0 offen sc1\n\t"
      "buffer_load_dwordx2 v[212:213], %10, %[rs], 0 offen sc1\n\t"
      "buffer_load_dwordx2 v[214:215], %11, %[rs], 0 offen sc1\n\t"
      "buffer_load_dwordx2 v[216:217], %12, %[rs], 0 offen sc1\n\t"
      "buffer_load_dwordx2 v[218:219], %13, %[rs], 0 offen sc1\n\t"
      "buffer_load_dwordx2 v[220:221], %14, %[rs], 0 offen sc1\n\t"
      "buffer_load_dwordx2 v[222:223], %15, %[rs], 0 offen sc1\n\t"
      ".Lgloop%=:\n\t"
      "s_waitcnt vmcnt(0)\n\t"
      "v_cmp_eq_u32_e32 vcc, %[tag], v209\n\tv_cmp_eq_u32_e64 %[m], %[tag], v211\n\ts_nop 0\n\t"
      "v_cndmask_b32_e32 %8, %8, %[oob], vcc\n\tv_cndmask_b32_e64 %9, %9, %[oob], %[m]\n\tv_cndmask_b32_e32 %0, %0, v208, vcc\n\tv_cndmask_b32_e64 %1, %1, v210, %[m]\n\t"
      "v_cmp_eq_u32_e32 vcc, %[tag], v213\n\tv_cmp_eq_u32_e64 %[m], %[tag], v215\n\ts_nop 0\n\t"
      "v_cndmask_b32_e32 %10, %10, %[oob], vcc\n\tv_cndmask_b32_e64 %11, %11, %[oob], %[m]\n\tv_cndmask_b32_e32 %2, %2, v212, vcc\n\tv_cndmask_b32_e64 %3, %3, v214, %[m]\n\t"
      "v_cmp_eq_u32_e32 vcc, %[tag], v217\n\tv_cmp_eq_u32_e64 %[m], %[tag], v219\n\ts_nop 0\n\t"
      "v_cndmask_b32_e32 %12, %12, %[oob], vcc\n\tv_cndmask_b32_e64 %13, %13, %[oob], %[m]\n\tv_cndmask_b32_e32 %4, %4, v216, vcc\n\tv_cndmask_b32_e64 %5, %5, v218, %[m]\n\t"
      "v_cmp_eq_u32_e32 vcc, %[tag], v221\n\tv_cmp_eq_u32_e64 %[m], %[tag], v223\n\ts_nop 0\n\t"
      "v_cndmask_b32_e32 %14, %14, %[oob], vcc\n\tv_cndmask_b32_e64 %15, %15, %[oob], %[m]\n\tv_cndmask_b32_e32 %6, %6, v220, vcc\n\tv_cndmask_b32_e64 %7, %7, v222, %[m]\n\t"
      "v_min3_u32 %[t0], %8, %9, %10\n\tv_min3_u32 %[t1], %11, %12, %13\n\tv_min3_u32 %[t0], %[t0], %14, %15\n\tv_min_u32_e32 %[t0], %[t0], %[t1]\n\t"
      "v_cmp_ne_u32_e32 vcc, %[t0], %[oob]\n\ts_nop 1\n\ts_cbranch_vccz .Lgdone%=\n\t"
      "buffer_load_dwordx2 v[208:209], %8, %[rs], 0 offen sc1\n\t"
      "buffer_load_dwordx2 v[210:211], %9, %[rs], 0 offen sc1\n\t"
      "buffer_load_dwordx2 v[212:213], %10, %[rs], 0 offen sc1\n\t"
      "buffer_load_dwordx2 v[214:215], %11, %[rs], 0 offen sc1\n\t"
      "buffer_load_dwordx2 v[216:217], %12, %[rs], 0 offen sc1\n\t"
      "buffer_load_dwordx2 v[218:219], %13, %[rs], 0 offen sc1\n\t"
      "buffer_load_dwordx2 v[220:221], %14, %[rs], 0 offen sc1\n\t"
      "buffer_load_dwordx2 v[222:223], %15, %[rs], 0 offen sc1\n\t"
      "s_memrealtime vcc\n\ts_waitcnt lgkmcnt(0)\n\ts_sub_u32 vcc_lo, vcc_lo, %[ts]\n\ts_cmp_lt_u32 vcc_lo, %[lim]\n\ts_cbranch_scc1 .Lgloop%=\n"
      ".Lgdone%=:"
      : "+v"(val[0]), "+v"(val[1]), "+v"(val[2]), "+v"(val[3]), "+v"(val[4]), "+v"(val[5]), "+v"(val[6]), "+v"(val[7]),
        "+v"(off[0]), "+v"(off[1]), "+v"(off[2]), "+v"(off[3]), "+v"(off[4]), "+v"(off[5]), "+v"(off[6]), "+v"(off[7]),
        [t0] "=&v"(t0), [t1] "=&v"(t1), [m] "=&s"(m)
      : [rs] "s"(rs), [tag] "s"(tag), [oob] "v"(oob), [ts] "s"(ts), [lim] "s"(SPIN_TICKS), [gap] "n"(GAP)
      : "memory", "vcc", "scc", "v208", "v209", "v210", "v211", "v212", "v213", "v214", "v215", "v216", "v217", "v218", "v219", "v220", "v221", "v222", "v223");
  } else if constexpr (DEPTH == 2) {
    asm volatile(
      "buffer_load_dwordx2 v[208:209], %8, %[rs], 0 offen sc1\n\t"
      "buffer_load_dwordx2 v[210:211], %9, %[rs], 0 offen sc1\n\t"
      "buffer_load_dwordx2 v[212:213], %10, %[rs], 0 offen sc1\n\t"
      "buffer_load_dwordx2 v[214:215], %11, %[rs], 0 offen sc1\n\t"
      "buffer_load_dwordx2 v[216:217], %12, %[rs], 0 offen sc1\n\t"
      "buffer_load_dwordx2 v[218:219], %13, %[rs], 0 offen sc1\n\t"
      "buffer_load_dwordx2 v[220:221], %14, %[rs], 0 offen sc1\n\t"
      "buffer_load_dwordx2 v[222:223], %15, %[rs], 0 offen sc1\n\t"
      "s_sleep %[gap]\n\t"
      "buffer_load_dwordx2 v[224:225], %8, %[rs], 0 offen sc1\n\t"
      "buffer_load_dwordx2 v[226:227], %9, %[rs], 0 offen sc1\n\t"
      "buffer_load_dwordx2 v[228:229], %10, %[rs], 0 offen sc1\n\t"
      "buffer_load_dwordx2 v[230:231], %11, %[rs], 0 offen sc1\n\t"
      "buffer_load_dwordx2 v[232:233], %12, %[rs], 0 offen sc1\n\t"
      "buffer_load_dwordx2 v[234:235], %13, %[rs], 0 offen sc1\n\t"
      "buffer_load_dwordx2 v[236:237], %14, %[rs], 0 offen sc1\n\t"
      "buffer_load_dwordx2 v[238:239], %15, %[rs], 0 offen sc1\n\t"
      ".Lgloop%=:\n\t"
      "s_waitcnt vmcnt(8)\n\t"
      "v_cmp_eq_u32_e32 vcc, %[tag], v209\n\tv_cmp_eq_u32_e64 %[m], %[tag], v211\n\ts_nop 0\n\t"
      "v_cndmask_b32_e32 %8, %8, %[oob], vcc\n\tv_cndmask_b32_e64 %9, %9, %[oob], %[m]\n\tv_cndmask_b32_e32 %0, %0, v208, vcc\n\tv_cndmask_b32_e64 %1, %1, v210, %[m]\n\t"
      "v_cmp_eq_u32_e32 vcc, %[tag], v213\n\tv_cmp_eq_u32_e64 %[m], %[tag], v215\n\ts_nop 0\n\t"
      "v_cndmask_b32_e32 %10, %10, %[oob], vcc\n\tv_cndmask_b32_e64 %11, %11, %[oob], %[m]\n\tv_cndmask_b32_e32 %2, %2, v212, vcc\n\tv_cndmask_b32_e64 %3, %3, v214, %[m]\n\t"
      "v_cmp_eq_u32_e32 vcc, %[tag], v217\n\tv_cmp_eq_u32_e64 %[m], %[tag], v219\n\ts_nop 0\n\t"
      "v_cndmask_b32_e32 %12, %12, %[oob], vcc\n\tv_cndmask_b32_e64 %13, %13, %[oob], %[m]\n\tv_cndmask_b32_e32 %4, %4, v216, vcc\n\tv_cndmask_b32_e64 %5, %5, v218, %[m]\n\t"
      "v_cmp_eq_u32_e32 vcc, %[tag], v221\n\tv_cmp_eq_u32_e64 %[m], %[tag], v223\n\ts_nop 0\n\t"
      "v_cndmask_b32_e32 %14, %14, %[oob], vcc\n\tv_cndmask_b32_e64 %15, %15, %[oob], %[m]\n\tv_cndmask_b32_e32 %6, %6, v220, vcc\n\tv_cndmask_b32_e64 %7, %7, v222, %[m]\n\t"
      "v_min3_u32 %[t0], %8, %9, %10\n\tv_min3_u32 %[t1], %11, %12, %13\n\tv_min3_u32 %[t0], %[t0], %14, %15\n\tv_min_u32_e32 %[t0], %[t0], %[t1]\n\t"
      "v_cmp_ne_u32_e32 vcc, %[t0], %[oob]\n\ts_nop 1\n\ts_cbranch_vccz .Lgdone%=\n\t"
      "buffer_load_dwordx2 v[208:209], %8, %[rs], 0 offen sc1\n\t"
      "buffer_load_dwordx2 v[210:211], %9, %[rs], 0 offen sc1\n\t"
      "buffer_load_dwordx2 v[212:213], %10, %[rs], 0 offen sc1\n\t"
      "buffer_load_dwordx2 v[214:215], %11, %[rs], 0 offen sc1\n\t"
      "buffer_load_dwordx2 v[216:217], %12, %[rs], 0 offen sc1\n\t"
      "buffer_load_dwordx2 v[218:219], %13, %[rs], 0 offen sc1\n\t"
      "buffer_load_dwordx2 v[220:221], %14, %[rs], 0 offen sc1\n\t"
      "buffer_load_dwordx2 v[222:223], %15, %[rs], 0 offen sc1\n\t"
      "s_waitcnt vmcnt(8)\n\t"
      "v_cmp_eq_u32_e32 vcc, %[tag], v225\n\tv_cmp_eq_u32_e64 %[m], %[tag], v227\n\ts_nop 0\n\t"
      "v_cndmask_b32_e32 %8, %8, %[oob], vcc\n\tv_cndmask_b32_e64 %9, %9, %[oob], %[m]\n\tv_cndmask_b32_e32 %0, %0, v224, vcc\n\tv_cndmask_b32_e64 %1, %1, v226, %[m]\n\t"
      "v_cmp_eq_u32_e32 vcc, %[tag], v229\n\tv_cmp_eq_u32_e64 %[m], %[tag], v231\n\ts_nop 0\n\t"
      "v_cndmask_b32_e32 %10, %10, %[oob], vcc\n\tv_cndmask_b32_e64 %11, %11, %[oob], %[m]\n\tv_cndmask_b32_e32 %2, %2, v228, vcc\n\tv_cndmask_b32_e64 %3, %3, v230, %[m]\n\t"
      "v_cmp_eq_u32_e32 vcc, %[tag], v233\n\tv_cmp_eq_u32_e64 %[m], %[tag], v235\n\ts_nop 0\n\t"
      "v_cndmask_b32_e32 %12, %12, %[oob], vcc\n\tv_cndmask_b32_e64 %13, %13, %[oob], %[m]\n\tv_cndmask_b32_e32 %4, %4, v232, vcc\n\tv_cndmask_b32_e64 %5, %5, v234, %[m]\n\t"
      "v_cmp_eq_u32_e32 vcc, %[tag], v237\n\tv_cmp_eq_u32_e64 %[m], %[tag], v239\n\ts_nop 0\n\t"
      "v_cndmask_b32_e32 %14, %14, %[oob], vcc\n\tv_cndmask_b32_e64 %15, %15, %[oob], %[m]\n\tv_cndmask_b32_e32 %6, %6, v236, vcc\n\tv_cndmask_b32_e64 %7, %7, v238, %[m]\n\t"
      "v_min3_u32 %[t0], %8, %9, %10\n\tv_min3_u32 %[t1], %11, %12, %13\n\tv_min3_u32 %[t0], %[t0], %14, %15\n\tv_min_u32_e32 %[t0], %[t0], %[t1]\n\t"
      "v_cmp_ne_u32_e32 vcc, %[t0], %[oob]\n\ts_nop 1\n\ts_cbranch_vccz .Lgdone%=\n\t"
      "buffer_load_dwordx2 v[224:225], %8, %[rs], 0 offen sc1\n\t"
      "buffer_load_dwordx2 v[226:227], %9, %[rs], 0 offen sc1\n\t"
      "buffer_load_dwordx2 v[228:229], %10, %[rs], 0 offen sc1\n\t"
      "buffer_load_dwordx2 v[230:231], %11, %[rs], 0 offen sc1\n\t"
      "buffer_load_dwordx2 v[232:233], %12, %[rs], 0 offen sc1\n\t"
      "buffer_load_dwordx2 v[234:235], %13, %[rs], 0 offen sc1\n\t"
      "buffer_load_dwordx2 v[236:237], %14, %[rs], 0 offen sc1\n\t"
      "buffer_load_dwordx2 v[238:239], %15, %[rs], 0 offen sc1\n\t"
      "s_memrealtime vcc\n\ts_waitcnt lgkmcnt(0)\n\ts_sub_u32 vcc_lo, vcc_lo, %[ts]\n\ts_cmp_lt_u32 vcc_lo, %[lim]\n\ts_cbranch_scc1 .Lgloop%=\n"
      ".Lgdone%=:"
      : "+v"(val[0]), "+v"(val[1]), "+v"(val[2]), "+v"(val[3]), "+v"(val[4]), "+v"(val[5]), "+v"(val[6]), "+v"(val[7]),
        "+v"(off[0]), "+v"(off[1]), "+v"(off[2]), "+v"(off[3]), "+v"(off[4]), "+v"(off[5]), "+v"(off[6]), "+v"(off[7]),
        [t0] "=&v"(t0), [t1] "=&v"(t1), [m] "=&s"(m)
      : [rs] "s"(rs), [tag] "s"(tag), [oob] "v"(oob), [ts] "s"(ts), [lim] "s"(SPIN_TICKS), [gap] "n"(GAP)
      : "memory", "vcc", "scc", "v208", "v209", "v210", "v211", "v212", "v213", "v214", "v215", "v216", "v217", "v218", "v219", "v220", "v221", "v222", "v223", "v224", "v225", "v226", "v227", "v228", "v229", "v230", "v231", "v232", "v233", "v234", "v235", "v236", "v237", "v238", "v239");
  } else {
    asm volatile(
      "buffer_load_dwordx2 v[208:209], %8, %[rs], 0 offen sc1\n\t"
      "buffer_load_dwordx2 v[210:211], %9, %[rs], 0 offen sc1\n\t"
      "buffer_load_dwordx2 v[212:213], %10, %[rs], 0 offen sc1\n\t"
      "buffer_load_dwordx2 v[214:215], %11, %[rs], 0 offen sc1\n\t"
      "buffer_load_dwordx2 v[216:217], %12, %[rs], 0 offen sc1\n\t"
      "buffer_load_dwordx2 v[218:219], %13, %[rs], 0 offen sc1\n\t"
      "buffer_load_dwordx2 v[220:221], %14, %[rs], 0 offen sc1\n\t"
      "buffer_load_dwordx2 v[222:223], %15, %[rs], 0 offen sc1\n\t"
      "s_sleep %[gap]\n\t"
      "buffer_load_dwordx2 v[224:225], %8, %[rs], 0 offen sc1\n\t"
      "buffer_load_dwordx2 v[226:227], %9, %[rs], 0 offen sc1\n\t"
      "buffer_load_dwordx2 v[228:229], %10, %[rs], 0 offen sc1\n\t"
      "buffer_load_dwordx2 v[230:231], %11, %[rs], 0 offen sc1\n\t"
      "buffer_load_dwordx2 v[232:233], %12, %[rs], 0 offen sc1\n\t"
      "buffer_load_dwordx2 v[234:235], %13, %[rs], 0 offen sc1\n\t"
      "buffer_load_dwordx2 v[236:237], %14, %[rs], 0 offen sc1\n\t"
      "buffer_load_dwordx2 v[238:239], %15, %[rs], 0 offen sc1\n\t"
      "s_sleep %[gap]\n\t"
      "buffer_load_dwordx2 v[240:241], %8, %[rs], 0 offen sc1\n\t"
      "buffer_load_dwordx2 v[242:243], %9, %[rs], 0 offen sc1\n\t"
      "buffer_load_dwordx2 v[244:245], %10, %[rs], 0 offen sc1\n\t"
      "buffer_load_dwordx2 v[246:247], %11, %[rs], 0 offen sc1\n\t"
      "buffer_load_dwordx2 v[248:249], %12, %[rs], 0 offen sc1\n\t"
      "buffer_load_dwordx2 v[250:251], %13, %[rs], 0 offen sc1\n\t"
      "buffer_load_dwordx2 v[252:253], %14, %[rs], 0 offen sc1\n\t"
      "buffer_load_dwordx2 v[254:255], %15, %[rs], 0 offen sc1\n\t"
      ".Lgloop%=:\n\t"
      "s_waitcnt vmcnt(16)\n\t"
      "v_cmp_eq_u32_e32 vcc, %[tag], v209\n\tv_cmp_eq_u32_e64 %[m], %[tag], v211\n\ts_nop 0\n\t"
      "v_cndmask_b32_e32 %8, %8, %[oob], vcc\n\tv_cndmask_b32_e64 %9, %9, %[oob], %[m]\n\tv_cndmask_b32_e32 %0, %0, v208, vcc\n\tv_cndmask_b32_e64 %1, %1, v210, %[m]\n\t"
      "v_cmp_eq_u32_e32 vcc, %[tag], v213\n\tv_cmp_eq_u32_e64 %[m], %[tag], v215\n\ts_nop 0\n\t"
      "v_cndmask_b32_e32 %10, %10, %[oob], vcc\n\tv_cndmask_b32_e64 %11, %11, %[oob], %[m]\n\tv_cndmask_b32_e32 %2, %2, v212, vcc\n\tv_cndmask_b32_e64 %3, %3, v214, %[m]\n\t"
      "v_cmp_eq_u32_e32 vcc, %[tag], v217\n\tv_cmp_eq_u32_e64 %[m], %[tag], v219\n\ts_nop 0\n\t"
      "v_cndmask_b32_e32 %12, %12, %[oob], vcc\n\tv_cndmask_b32_e64 %13, %13, %[oob], %[m]\n\tv_cndmask_b32_e32 %4, %4, v216, vcc\n\tv_cndmask_b32_e64 %5, %5, v218, %[m]\n\t"
      "v_cmp_eq_u32_e32 vcc, %[tag], v221\n\tv_cmp_eq_u32_e64 %[m], %[tag], v223\n\ts_nop 0\n\t"
      "v_cndmask_b32_e32 %14, %14, %[oob], vcc\n\tv_cndmask_b32_e64 %15, %15, %[oob], %[m]\n\tv_cndmask_b32_e32 %6, %6, v220, vcc\n\tv_cndmask_b32_e64 %7, %7, v222, %[m]\n\t"
      "v_min3_u32 %[t0], %8, %9, %10\n\tv_min3_u32 %[t1], %11, %12, %13\n\tv_min3_u32 %[t0], %[t0], %14, %15\n\tv_min_u32_e32 %[t0], %[t0], %[t1]\n\t"
      "v_cmp_ne_u32_e32 vcc, %[t0], %[oob]\n\ts_nop 1\n\ts_cbranch_vccz .Lgdone%=\n\t"
      "buffer_load_dwordx2 v[208:209], %8, %[rs], 0 offen sc1\n\t"
      "buffer_load_dwordx2 v[210:211], %9, %[rs], 0 offen sc1\n\t"
      "buffer_load_dwordx2 v[212:213], %10, %[rs], 0 offen sc1\n\t"
      "buffer_load_dwordx2 v[214:215], %11, %[rs], 0 offen sc1\n\t"
      "buffer_load_dwordx2 v[216:217], %12, %[rs], 0 offen sc1\n\t"
      "buffer_load_dwordx2 v[218:219], %13, %[rs], 0 offen sc1\n\t"
      "buffer_load_dwordx2 v[220:221], %14, %[rs], 0 offen sc1\n\t"
      "buffer_load_dwordx2 v[222:223], %15, %[rs], 0 offen sc1\n\t"
      "s_waitcnt vmcnt(16)\n\t"
      "v_cmp_eq_u32_e32 vcc, %[tag], v225\n\tv_cmp_eq_u32_e64 %[m], %[tag], v227\n\ts_nop 0\n\t"
      "v_cndmask_b32_e32 %8, %8, %[oob], vcc\n\tv_cndmask_b32_e64 %9, %9, %[oob], %[m]\n\tv_cndmask_b32_e32 %0, %0, v224, vcc\n\tv_cndmask_b32_e64 %1, %1, v226, %[m]\n\t"
      "v_cmp_eq_u32_e32 vcc, %[tag], v229\n\tv_cmp_eq_u32_e64 %[m], %[tag], v231\n\ts_nop 0\n\t"
      "v_cndmask_b32_e32 %10, %10, %[oob], vcc\n\tv_cndmask_b32_e64 %11, %11, %[oob], %[m]\n\tv_cndmask_b32_e32 %2, %2, v228, vcc\n\tv_cndmask_b32_e64 %3, %3, v230, %[m]\n\t"
      "v_cmp_eq_u32_e32 vcc, %[tag], v233\n\tv_cmp_eq_u32_e64 %[m], %[tag], v235\n\ts_nop 0\n\t"
      "v_cndmask_b32_e32 %12, %12, %[oob], vcc\n\tv_cndmask_b32_e64 %13, %13, %[oob], %[m]\n\tv_cndmask_b32_e32 %4, %4, v232, vcc\n\tv_cndmask_b32_e64 %5, %5, v234, %[m]\n\t"
      "v_cmp_eq_u32_e32 vcc, %[tag], v237\n\tv_cmp_eq_u32_e64 %[m], %[tag], v239\n\ts_nop 0\n\t"
      "v_cndmask_b32_e32 %14, %14, %[oob], vcc\n\tv_cndmask_b32_e64 %15, %15, %[oob], %[m]\n\tv_cndmask_b32_e32 %6, %6, v236, vcc\n\tv_cndmask_b32_e64 %7, %7, v238, %[m]\n\t"
      "v_min3_u32 %[t0], %8, %9, %10\n\tv_min3_u32 %[t1], %11, %12, %13\n\tv_min3_u32 %[t0], %[t0], %14, %15\n\tv_min_u32_e32 %[t0], %[t0], %[t1]\n\t"
      "v_cmp_ne_u32_e32 vcc, %[t0], %[oob]\n\ts_nop 1\n\ts_cbranch_vccz .Lgdone%=\n\t"
      "buffer_load_dwordx2 v[224:225], %8, %[rs], 0 offen sc1\n\t"
      "buffer_load_dwordx2 v[226:227], %9, %[rs], 0 offen sc1\n\t"
      "buffer_load_dwordx2 v[228:229], %10, %[rs], 0 offen sc1\n\t"
      "buffer_load_dwordx2 v[230:231], %11, %[rs], 0 offen sc1\n\t"
      "buffer_load_dwordx2 v[232:233], %12, %[rs], 0 offen sc1\n\t"
      "buffer_load_dwordx2 v[234:235], %13, %[rs], 0 offen sc1\n\t"
      "buffer_load_dwordx2 v[236:237], %14, %[rs], 0 offen sc1\n\t"
      "buffer_load_dwordx2 v[238:239], %15, %[rs], 0 offen sc1\n\t"
      "s_waitcnt vmcnt(16)\n\t"
      "v_cmp_eq_u32_e32 vcc, %[tag], v241\n\tv_cmp_eq_u32_e64 %[m], %[tag], v243\n\ts_nop 0\n\t"
      "v_cndmask_b32_e32 %8, %8, %[oob], vcc\n\tv_cndmask_b32_e64 %9, %9, %[oob], %[m]\n\tv_cndmask_b32_e32 %0, %0, v240, vcc\n\tv_cndmask_b32_e64 %1, %1, v242, %[m]\n\t"
      "v_cmp_eq_u32_e32 vcc, %[tag], v245\n\tv_cmp_eq_u32_e64 %[m], %[tag], v247\n\ts_nop 0\n\t"
      "v_cndmask_b32_e32 %10, %10, %[oob], vcc\n\tv_cndmask_b32_e64 %11, %11, %[oob], %[m]\n\tv_cndmask_b32_e32 %2, %2, v244, vcc\n\tv_cndmask_b32_e64 %3, %3, v246, %[m]\n\t"
      "v_cmp_eq_u32_e32 vcc, %[tag], v249\n\tv_cmp_eq_u32_e64 %[m], %[tag], v251\n\ts_nop 0\n\t"
      "v_cndmask_b32_e32 %12, %12, %[oob], vcc\n\tv_cndmask_b32_e64 %13, %13, %[oob], %[m]\n\tv_cndmask_b32_e32 %4, %4, v248, vcc\n\tv_cndmask_b32_e64 %5, %5, v250, %[m]\n\t"
      "v_cmp_eq_u32_e32 vcc, %[tag], v253\n\tv_cmp_eq_u32_e64 %[m], %[tag], v255\n\ts_nop 0\n\t"
      "v_cndmask_b32_e32 %14, %14, %[oob], vcc\n\tv_cndmask_b32_e64 %15, %15, %[oob], %[m]\n\tv_cndmask_b32_e32 %6, %6, v252, vcc\n\tv_cndmask_b32_e64 %7, %7, v254, %[m]\n\t"
      "v_min3_u32 %[t0], %8, %9, %10\n\tv_min3_u32 %[t1], %11, %12, %13\n\tv_min3_u32 %[t0], %[t0], %14, %15\n\tv_min_u32_e32 %[t0], %[t0], %[t1]\n\t"
      "v_cmp_ne_u32_e32 vcc, %[t0], %[oob]\n\ts_nop 1\n\ts_cbranch_vccz .Lgdone%=\n\t"
      "buffer_load_dwordx2 v[240:241], %8, %[rs], 0 offen sc1\n\t"
      "buffer_load_dwordx2 v[242:243], %9, %[rs], 0 offen sc1\n\t"
      "buffer_load_dwordx2 v[244:245], %10, %[rs], 0 offen sc1\n\t"
      "buffer_load_dwordx2 v[246:247], %11, %[rs], 0 offen sc1\n\t"
      "buffer_load_dwordx2 v[248:249], %12, %[rs], 0 offen sc1\n\t"
      "buffer_load_dwordx2 v[250:251], %13, %[rs], 0 offen sc1\n\t"
      "buffer_load_dwordx2 v[252:253], %14, %[rs], 0 offen sc1\n\t"
      "buffer_load_dwordx2 v[254:255], %15, %[rs], 0 offen sc1\n\t"
      "s_memrealtime vcc\n\ts_waitcnt lgkmcnt(0)\n\ts_sub_u32 vcc_lo, vcc_lo, %[ts]\n\ts_cmp_lt_u32 vcc_lo, %[lim]\n\ts_cbranch_scc1 .Lgloop%=\n"
      ".Lgdone%=:"
      : "+v"(val[0]), "+v"(val[1]), "+v"(val[2]), "+v"(val[3]), "+v"(val[4]), "+v"(val[5]), "+v"(val[6]), "+v"(val[7]),
        "+v"(off[0]), "+v"(off[1]), "+v"(off[2]), "+v"(off[3]), "+v"(off[4]), "+v"(off[5]), "+v"(off[6]), "+v"(off[7]),
        [t0] "=&v"(t0), [t1] "=&v"(t1), [m] "=&s"(m)
      : [rs] "s"(rs), [tag] "s"(tag), [oob] "v"(oob), [ts] "s"(ts), [lim] "s"(SPIN_TICKS), [gap] "n"(GAP)
      : "memory", "vcc", "scc", "v208", "v209", "v210", "v211", "v212", "v213", "v214", "v215", "v216", "v217", "v218", "v219", "v220", "v221", "v222", "v223", "v224", "v225", "v226", "v227", "v228", "v229", "v230", "v231", "v232", "v233", "v234", "v235", "v236", "v237", "v238", "v239", "v240", "v241", "v242", "v243", "v244", "v245", "v246", "v247", "v248", "v249", "v250", "v251", "v252", "v253", "v254", "v255");
  }
}
#define PW 2
#define STR2(x) #x
#define STR(x) STR2(x)
#define PWAIT() asm volatile("s_waitcnt vmcnt(" STR(PW) ")" ::: "memory")
#define SCHED() do { asm volatile("" ::: "memory"); __builtin_amdgcn_sched_barrier(0); } while (0)
#define STAMP(i) do {} while (0)
#define UT(u) do {} while (0)

__global__ void __launch_bounds__(NTHR, 2)
fused_decode(const u16* x, const u16* kc, const u16* vc, const u16* wq, const u16* wo,
             const u16* w1, const u16* b1, const u16* w2, const u16* b2, u16* out, unsigned char* ws) {
  __shared__ __attribute__((aligned(16))) unsigned char lds_raw[L_END];
  LAS unsigned char* L = (LAS unsigned char*)lds_raw;
  const int bid = blockIdx.x;
  const int h = bid >> 3, j = bid & 7, b = j >> 2, sp = j & 3;
  const int tid = threadIdx.x, lane = tid & 63, w = __builtin_amdgcn_readfirstlane(tid >> 6), r = lane >> 3, c = lane & 7;
  unsigned warm = 0;
  if (CODEWARM && w == 0) {
    u64 pc; unsigned len;
    asm volatile("s_getpc_b64 %0\n.Lfd_here:\n\ts_mov_b32 %1, .Lfd_end-.Lfd_here" : "=s"(pc), "=s"(len));
    const unsigned off = min((unsigned)(((blockIdx.x >> 3) & 3) * 8192 + lane * 128), len - 4u) & ~3u;
    warm = *(const GAS unsigned*)((pc & ~127ull) + off);
  }
  SCHED();
  LAS unsigned* ctr = (LAS unsigned*)(L + L_CT);
  LAS u64* pace = (LAS u64*)(L + L_CT + 8);
  LAS unsigned* done = (LAS unsigned*)(L + L_CT + 16);
  const unsigned ts = now();
  const u64 c0 = __builtin_amdgcn_s_memtime();
  STAMP(0);
  unsigned tag;
  constexpr float C2 = 0.07216878364870322f * 1.4426950408889634f;

  const __amdgpu_buffer_rsrc_t wsr = __builtin_amdgcn_make_buffer_rsrc((void*)ws, 0, (int)WS_ST, RSRC3);

  if (w < NS) {
    if (tid == 0) { *ctr = NS * P0; *pace = 0ull; *done = 0u; }
    const int rbase = ((b * H + h) * SK + sp * 8) * (DH * 2);
    constexpr int USTR = 4 * 8 * DH * 2;
    const int voff = r * (DH * 2) + c * 16;
    v4u Kb[P0][3], Vb[P0][3];
    const __amdgpu_buffer_rsrc_t kr = __builtin_amdgcn_make_buffer_rsrc((void*)kc, 0, KVBYTES, RSRC3), vr = __builtin_amdgcn_make_buffer_rsrc((void*)vc, 0, KVBYTES, RSRC3);
#define LD(dst, rs, t) { const int so_ = rbase + (t) * USTR; \
    _Pragma("unroll") for (int u = 0; u < 3; ++u) dst[u] = __builtin_amdgcn_raw_buffer_load_b128(rs, voff + u * 128, so_, AUX_##rs); }
#pragma unroll
    for (int i = 0; i < P0; ++i) {
      LD(Kb[i], kr, w + i * NS) UT(w + i * NS); SCHED(); PWAIT(); LD(Vb[i], vr, w + i * NS) SCHED(); if (i + 1 < P0) PWAIT(); }
    SCHED();
    STAMP(1);
    barrier();
    STAMP(2);
    asm volatile("" :: "v"(warm));
    tag = lds_ld(ctr + 1);
    v4u qv[3];
#pragma unroll
    for (int u = 0; u < 3; ++u) qv[u] = *(LAS v4u*)(L + L_Q + u * 128 + c * 16);
    float m = -INFINITY, l = 0.f; f32x2 ac2[12];
#pragma unroll
    for (int k = 0; k < 12; ++k) ac2[k] = f32x2{0.f, 0.f};
#define QK(Kx, s) { float d0 = 0.f, d1 = 0.f; \
      _Pragma("unroll") for (int u = 0; u < 3; ++u) { d0 = dot2(Kx[u][0], qv[u][0], d0); d1 = dot2(Kx[u][1], qv[u][1], d1); d0 = dot2(Kx[u][2], qv[u][2], d0); d1 = dot2(Kx[u][3], qv[u][3], d1); } \
      s = d0 + d1; asm volatile("" : "+v"(s)); }
#define PV(Vx, s) { s += dpp<XOR1>(s); s += dpp<XOR2>(s); s += dpp<XOR7>(s); s *= C2; \
      const float mn = fmaxf(m, s), al = __builtin_amdgcn_exp2f(m - mn), p = __builtin_amdgcn_exp2f(s - mn); m = mn; l = fmaf(l, al, p); \
      _Pragma("unroll") for (int u = 0; u < 3; ++u) { SCHED(); _Pragma("unroll") for (int k = 0; k < 4; ++k) \
          ac2[u * 4 + k] = __builtin_elementwise_fma(f32x2{p, p}, f32x2{lo16(Vx[u][k]), hi16(Vx[u][k])}, ac2[u * 4 + k] * al); \
        asm volatile("" : "+v"(ac2[u * 4]), "+v"(ac2[u * 4 + 1]), "+v"(ac2[u * 4 + 2]), "+v"(ac2[u * 4 + 3])); } }
    const unsigned ctra = L_CT + (unsigned)(uintptr_t)L, one = 1u;
#define COUNT() asm volatile("s_mov_b64 exec, 1\n\tds_add_u32 %0, %1\n\ts_mov_b64 exec, -1" :: "v"(ctra), "v"(one) : "memory")
    float s;
    int nxt = w + NS * P0;
#define STEP() { QK(Kb[0], s) SCHED(); LD(Kb[0], kr, nxt) COUNT(); UT(nxt); SCHED(); PV(Vb[0], s) SCHED(); LD(Vb[0], vr, nxt) SCHED(); nxt += NS; }
    static_assert(NS * (P0 + P0 / 2 + 1) <= NU, "the first steps load real units");
    STEP()
#pragma unroll
    for (int i = 1; i < P0; ++i) { QK(Kb[i], s) PV(Vb[i], s) SCHED(); if (i % 2 == 0) STEP() }
    STAMP(3);
    while (nxt < NU) {
      QK(Kb[0], s)
#pragma unroll 1
      for (int spn = 0; spn < 256; ++spn) {
        const u64 pv = __hip_atomic_load(pace, RLX_WG);
        const float t0 = asf(__builtin_amdgcn_readfirstlane((unsigned)pv)), tau = asf(__builtin_amdgcn_readfirstlane((unsigned)(pv >> 32)));
        if (fmaf((float)nxt, tau, t0) <= (float)(int)(now() - ts)) break;
        __builtin_amdgcn_s_sleep(2);
      }
      SCHED(); LD(Kb[0], kr, nxt) COUNT(); UT(nxt); SCHED();
      PV(Vb[0], s)
      SCHED(); LD(Vb[0], vr, nxt) SCHED();
      nxt += NS;
    }
    QK(Kb[0], s)
    PV(Vb[0], s)
    STAMP(4);
    float M = fmaxf(m, dpp<ROR8>(m)); M = max16(M); M = max32(M);
    const float sc = __builtin_amdgcn_exp2f(m - M);
    l *= sc; l += dpp<ROR8>(l); l = tr16(l, l); l = tr32(l, l);
    float acc[24];
#pragma unroll
    for (int k = 0; k < 12; ++k) { const f32x2 t = ac2[k] * sc; acc[2 * k] = t[0]; acc[2 * k + 1] = t[1]; }
#pragma unroll
    for (int k = 0; k < 12; ++k) acc[k] = tr32(acc[k], acc[k + 12]);
#pragma unroll
    for (int k = 0; k < 6; ++k) acc[k] = tr16(acc[k], acc[k + 6]);
#pragma unroll
    for (int k = 0; k < 3; ++k) acc[k] = tr8(acc[k], acc[k + 3], r & 1);
    LAS float* mg = (LAS float*)(L + L_MG) + w * 200;
#pragma unroll
    for (int k = 0; k < 3; ++k) mg[k * 64 + lane] = acc[k];
    if (lane == 0) { mg[192] = M; mg[193] = l; }
    asm volatile("s_waitcnt lgkmcnt(0)\n\ts_mov_b64 exec, 1\n\tds_add_u32 %0, %1 offset:16\n\ts_mov_b64 exec, -1" :: "v"(ctra), "v"(one) : "memory");
    while (lds_ld(done) < NS && now() - ts < SPIN_TICKS) {}
    asm volatile("" ::: "memory");
    if (w >= 3) __builtin_amdgcn_s_sleep(PD0);
    else {
      STAMP(5);
      const LAS float* mg0 = (const LAS float*)(L + L_MG);
      float MM = -INFINITY;
#pragma unroll
      for (int i = 0; i < NS; ++i) MM = fmaxf(MM, mg0[i * 200 + 192]);
      float Ls = 0.f, o = 0.f;
#pragma unroll
      for (int i = 0; i < NS; ++i) { const float e = __builtin_amdgcn_exp2f(mg0[i * 200 + 192] - MM); Ls = fmaf(e, mg0[i * 200 + 193], Ls); o = fmaf(e, mg0[i * 200 + tid], o); }
      gu64* px = (gu64*)(ws + WS_PX) + bid * 256;
      put(px + tid, tag, o / Ls);
      if (tid == 0) put(px + 192, tag, MM + __builtin_amdgcn_logf(Ls));
    }
    STAMP(6);
    static_assert(NS == 6, "waves 0..2 | 3..5 gather batch row 0 | 1");
    const int bb = w >= 3, pos = tid - bb * 192;
    const int vo1 = (int)WS_PX + (h * 8 + bb * 4) * 2048;
    const int gq = lane >> 4, vb = gq & 1, which = gq >> 1, oi = vb * 80 + 16 * w + (lane & 15), hp = oi / 5;
    gu64* rx = (gu64*)(ws + WS_RX) + ((j * 32 + hp) * 32 + h) * 10 + which * 5 + (oi - hp * 5);
    const int di = (lane >> 5) * 12 + ((lane >> 4) & 1) * 6 + ((lane >> 3) & 1) * 3 + (w - 3 * bb);
    LAS u16* oa = (LAS u16*)(L + L_OA + (bb * DH + (di >> 3) * 64 + c * 8 + (di & 7)) * 2);
    unsigned gv[8]; int go[8];
#pragma unroll
    for (int i = 0; i < 8; ++i) go[i] = vo1 + (i & 3) * 2048 + (i < 4 ? pos : 192) * 8;
    __builtin_amdgcn_s_sleep(PD1);
    gather8<GD, GG>(gv, go, wsr, tag, ts);
    { const float e0 = asf(gv[4]), e1 = asf(gv[5]), e2 = asf(gv[6]), e3 = asf(gv[7]);
      const float em = fmaxf(fmaxf(e0, e1), fmaxf(e2, e3));
      const float w0 = __builtin_amdgcn_exp2f(e0 - em), w1v = __builtin_amdgcn_exp2f(e1 - em), w2v = __builtin_amdgcn_exp2f(e2 - em), w3 = __builtin_amdgcn_exp2f(e3 - em);
      *oa = f2bf((w0 * asf(gv[0]) + w1v * asf(gv[1]) + w2v * asf(gv[2]) + w3 * asf(gv[3])) / (w0 + w1v + w2v + w3));
    }
    STAMP(8);
    barrier();
    STAMP(7);
    if (w < 5) {
      const LAS unsigned char* base = L + L_WO + (((lane & 15) >> 2) + 8 * (lane >> 4)) * 160 + w * 32 + (lane & 3) * 8;
      const LAS unsigned char* ao = L + L_OA + (lane & 1) * (DH * 2) + (lane >> 4) * 16;
      const LAS unsigned char* ah = L + L_HA + (lane & 1) * 96;
#define TRB(off) __builtin_amdgcn_ds_read_tr16_b64_v4i16((LAS v4s*)(base + (off)))
#define BFRAG(k) __builtin_bit_cast(v8bf, __builtin_shufflevector(TRB((k) * 32 * 160), TRB((k) * 32 * 160 + 4 * 160), 0, 1, 2, 3, 4, 5, 6, 7))
      const f32x4 Z = {0.f, 0.f, 0.f, 0.f};
      f32x4 S = Z;
#pragma unroll
      for (int k = 0; k < 6; ++k) S = __builtin_amdgcn_mfma_f32_16x16x32_bf16(*(const LAS v8bf*)(ao + k * 64), BFRAG(k), S, 0, 0, 0);
      f32x4 Ga = __builtin_amdgcn_mfma_f32_16x16x32_bf16(*(const LAS v8bf*)(ah + (lane >> 4) * 16), BFRAG(6), Z, 0, 0, 0);
      f32x4 Gb = __builtin_amdgcn_mfma_f32_16x16x16bf16_1k(*(const LAS v4s*)(ah + 64 + (lane >> 4) * 8), TRB(7 * 32 * 160 - 4 * (lane >> 4) * 160), Z, 0, 0, 0);
      asm volatile("" : "+v"(S), "+v"(Ga), "+v"(Gb));
      put(rx, tag, which ? (vb ? Ga[1] + Gb[1] : Ga[0] + Gb[0]) : (vb ? S[1] : S[0]));
      STAMP(9);
    }
  } else {
    __builtin_amdgcn_s_setprio(3);
    const int a = w - NS;
    const unsigned epoch = __hip_atomic_load((GAS unsigned*)(ws + WS_EPOCH), RLX_AGENT);
    const unsigned pre = QPRE < 0 ? 0u : __builtin_amdgcn_raw_buffer_load_b32(wsr, lane < 16 ? (int)WS_QX + (bid * 512 + (lane >> 3) * 256 + a * 128) * 8 + (lane & 7) * 128 : (a && lane < 24  && PACE ? (int)WS_CT + ((lane - 16) * 544 + blockIdx.x) * 8 : OOB), 0, QPRE < 0 ? 0 : QPRE);
    const int xb = lane >> 5, xl = lane & 31;
    const __amdgpu_buffer_rsrc_t xr = __builtin_amdgcn_make_buffer_rsrc((void*)x, 0, NB * D * 2, RSRC3);
    const int xo = xb * (D * 2) + xl * 40;
    const v4u x0 = __builtin_amdgcn_raw_buffer_load_b128(xr, xo, 0, 0), x1 = __builtin_amdgcn_raw_buffer_load_b128(xr, xo + 16, 0, 0);
    const v2u x2 = __builtin_amdgcn_raw_buffer_load_b64(xr, xo + 32, 0, 0);
    u16 xs[3];
#pragma unroll
    for (int i = 0; i < 3; ++i) xs[i] = __builtin_amdgcn_raw_buffer_load_b16(xr, xl + 32 * i < 80 ? xb * (D * 2) + (80 * j + xl + 32 * i) * 2 : OOB, 0, 0);
    const int oi = 5 * h + (lane < 5 ? lane : 0), ocol = 80 * j + oi % 80, oidx = (oi / 80) * D + ocol;
    const u16 xres = x[oidx], b2v = b2[ocol];
    v4u wa[10], wb[10];
    { const __amdgpu_buffer_rsrc_t ra = __builtin_amdgcn_make_buffer_rsrc((void*)wq, 0, D * H * DH * 2, RSRC3);
      const __amdgpu_buffer_rsrc_t rb = __builtin_amdgcn_make_buffer_rsrc((void*)(a ? w1 : wq), 0, a ? D * F * 2 : D * H * DH * 2, RSRC3);
      const int sa = H * DH * 2, sb = a ? F * 2 : H * DH * 2;
      const int va = r * sa + c * 16, vb = (a && c >= 6) ? OOB : r * sb + c * 16;
      const int oa = 80 * j * sa + h * (DH * 2) + a * 256, ob = 80 * j * sb + (a ? h * 96 : h * (DH * 2) + 128);
#pragma unroll
      for (int u = 0; u < 10; ++u) { wa[u] = __builtin_amdgcn_raw_buffer_load_b128(ra, va, oa + u * 8 * sa, AUX_NT); wb[u] = __builtin_amdgcn_raw_buffer_load_b128(rb, vb, ob + u * 8 * sb, AUX_NT); }
    }
    SCHED();
    STAMP(1);
    tag = (unsigned)__builtin_amdgcn_readfirstlane(epoch) * 0x9E3779B1u + 0x7F4A7C15u;
    tag = tag ? tag : 1u;
    const unsigned tX = now() - ts;
    if (a == 0 && lane == 0) ctr[1] = tag;
    {
      float v[20];
#pragma unroll
      for (int k = 0; k < 4; ++k) { v[2 * k] = lo16(x0[k]); v[2 * k + 1] = hi16(x0[k]); v[8 + 2 * k] = lo16(x1[k]); v[9 + 2 * k] = hi16(x1[k]); }
      v[16] = lo16(x2[0]); v[17] = hi16(x2[0]); v[18] = lo16(x2[1]); v[19] = hi16(x2[1]);
      float s = 0.f;
#pragma unroll
      for (int k = 0; k < 20; ++k) s += v[k];
      s += dpp<ROR1>(s); s += dpp<ROR2>(s); s += dpp<ROR4>(s); s += dpp<ROR8>(s); s = tr16(s, s);
      const float mean = s * (1.f / D);
      float q = 0.f;
#pragma unroll
      for (int k = 0; k < 20; ++k) { const float d = v[k] - mean; q = fmaf(d, d, q); }
      q += dpp<ROR1>(q); q += dpp<ROR2>(q); q += dpp<ROR4>(q); q += dpp<ROR8>(q); q = tr16(q, q);
      const float mb = rbf(mean), sd = rbf(sqrtf(rbf(rbf(q * (1.f / D)) + 1e-5f)));
      LAS float* xn = (LAS float*)(L + L_XN) + (a * 2 + xb) * 96 + xl;
#pragma unroll
      for (int i = 0; i < 3; ++i) xn[32 * i] = rbf(rbf(bf2f(xs[i]) - mb) / sd);
    }
    asm volatile("s_waitcnt lgkmcnt(0)" :: "v"(pre) : "memory");
    f32x2 p2[16];
#pragma unroll
    for (int k = 0; k < 16; ++k) p2[k] = f32x2{0.f, 0.f};
    { const LAS float* xn = (const LAS float*)(L + L_XN) + a * 192 + r;
#pragma unroll
      for (int u = 0; u < 10; ++u) { const float xa = xn[8 * u], xc = xn[96 + 8 * u];
#pragma unroll
        for (int k = 0; k < 4; ++k) {
          const f32x2 fa = {lo16(wa[u][k]), hi16(wa[u][k])}, fb = {lo16(wb[u][k]), hi16(wb[u][k])};
          p2[k] = __builtin_elementwise_fma(f32x2{xa, xa}, fa, p2[k]); p2[4 + k] = __builtin_elementwise_fma(f32x2{xa, xa}, fb, p2[4 + k]);
          p2[8 + k] = __builtin_elementwise_fma(f32x2{xc, xc}, fa, p2[8 + k]); p2[12 + k] = __builtin_elementwise_fma(f32x2{xc, xc}, fb, p2[12 + k]);
        } } }
    float pa[32];
#pragma unroll
    for (int k = 0; k < 16; ++k) { pa[2 * k] = p2[k][0]; pa[2 * k + 1] = p2[k][1]; }
#pragma unroll
    for (int k = 0; k < 16; ++k) pa[k] = tr32(pa[k], pa[k + 16]);
#pragma unroll
    for (int k = 0; k < 8; ++k) pa[k] = tr16(pa[k], pa[k + 8]);
#pragma unroll
    for (int k = 0; k < 4; ++k) pa[k] = tr8(pa[k], pa[k + 4], r & 1);
    STAMP(2);
    { gu64* qx = (gu64*)(ws + WS_QX) + bid * 512 + (lane >> 5) * 256 + (2 * a + ((lane >> 4) & 1)) * 64 + (lane & 15);
#pragma unroll
      for (int t = 0; t < 4; ++t) put(qx + t * 16, tag, pa[t]); }
    const int col = (lane & 7) * 8 + ((lane >> 3) & 1) * 4 + (lane >> 4);
    if (a == 0) {
      unsigned gv[24]; int go[24];
#pragma unroll
      for (int i = 0; i < 24; ++i) go[i] = (int)WS_QX + ((h * 8 + i / 3) * 512 + b * 256 + (i % 3) * 64 + lane) * 8;
      gather<0>(gv, go, wsr, tag, ts);
#pragma unroll
      for (int u = 0; u < 3; ++u) { float q = 0.f;
#pragma unroll
        for (int p = 0; p < 8; ++p) q += asf(gv[p * 3 + u]);
        *(LAS u16*)(L + L_Q + (u * 64 + col) * 2) = f2bf(q); }
    }
    STAMP(3);
    barrier();
    const unsigned tB1 = now() - ts;
    __builtin_amdgcn_s_setprio(0);
    if (a == 0) {
      const bool live = (lane & 7) < 6;
      const float bias = bf2f(b1[48 * h + (live ? col : 0)]);
      unsigned gv[16]; int go[16];
#pragma unroll
      for (int i = 0; i < 16; ++i) go[i] = live ? (int)WS_QX + ((h * 8) * 512 + i * 256 + 192 + lane) * 8 : OOB;
      gather<16>(gv, go, wsr, tag, ts);
      float hs[2] = {0.f, 0.f};
#pragma unroll
      for (int p = 0; p < 16; ++p) hs[p & 1] += asf(gv[p]);
      if (live) {
#pragma unroll
        for (int u = 0; u < 2; ++u) { float t = rbf(rbf(hs[u]) + bias); t = t > 0.f ? t : 0.f; *(LAS u16*)(L + L_HA + (u * 48 + col) * 2) = f2bf(t * t); } }
    }
    else {
      float u0 = 0.f;
      for (int rd = 0; rd < 8; ++rd) {
        const unsigned Tk = PT0 + rd * PDT;
        gu64* cl = (gu64*)(ws + WS_CT) + rd * 544;
        while (now() - ts < Tk && lds_ld(ctr) < NU - 24) __builtin_amdgcn_s_sleep(8);
        const unsigned um = lds_ld(ctr);
        if (lane == 0) __hip_atomic_store(cl + blockIdx.x, ((u64)tag << 32) | um, RLX_AGENT);
        if (um >= NU - 24) break;
        while (now() - ts < Tk + PLAG) __builtin_amdgcn_s_sleep(8);
        float s = 0.f; int n = 0;
#pragma unroll
        for (int k = 0; k < (PCL + 63) / 64; ++k) { const u64 v = get(cl + (blockIdx.x & ~(PCL - 1)) + (lane & (PCL - 1)) + 64 * k); const bool ok = (unsigned)(v >> 32) == tag;
          s += ok ? (float)(unsigned)v : 0.f; n += __builtin_popcountll(__ballot(ok)); }
        s += dpp<ROR1>(s); s += dpp<ROR2>(s); s += dpp<ROR4>(s); s += dpp<ROR8>(s); s = tr16(s, s); s = tr32(s, s);
        const float ub = s / (float)n;
        const float tau = rd ? (float)(rd * PDT) / (ub - u0) * (1.f - PEPS * 0.001f) : (float)(PT0 - POFF) / (ub + 6.25f);
        if (rd == 0) u0 = ub;
        const float tend = (float)Tk + ((float)NU - ub) * tau, tnow = (float)(int)(now() - ts), ti = (tend - tnow) / ((float)NU - (float)lds_ld(ctr));
        if (lane == 0 && n > 0 && ti > 0.f && ti < 1000.f) __hip_atomic_store(pace, ((u64)asu(ti) << 32) | asu(tend - (float)NU * ti), RLX_WG);
      }
    }
    int vw[5], v2[4];
#pragma unroll
    for (int i = 0; i < 5; ++i) { const int id = i * 64 + lane, row = id / 10; vw[i] = row * (D * 2) + (id - row * 10) * 16; }
#pragma unroll
    for (int i = 0; i < 4; ++i) { const int id = (4 * a + i) * 64 + lane, row = id / 10; v2[i] = id < 480 ? row * (D * 2) + (id - row * 10) * 16 : OOB; }
    const __amdgpu_buffer_rsrc_t ro = __builtin_amdgcn_make_buffer_rsrc((void*)wo, 0, H * DH * D * 2, RSRC3), r2 = __builtin_amdgcn_make_buffer_rsrc((void*)w2, 0, F * D * 2, RSRC3);
    const int so = (DH * h + 96 * a) * (D * 2) + 160 * j, s2 = 48 * h * (D * 2) + 160 * j;
    SCHED();
    if (PRETOUCH) {
      const unsigned t = __builtin_amdgcn_raw_buffer_load_b32(wsr, a ? (lane < 20 ? (int)WS_RX + (j * 32 + h) * 2560 + lane * 128 : OOB) : (int)WS_PX + (h * 8 + (lane >> 3)) * 2048 + (lane & 7) * 128, 0, AUX_SC1);
      const unsigned t2 = __builtin_amdgcn_raw_buffer_load_b32(wsr, a || (lane & 7) >= 5 ? OOB : (int)WS_PX + (h * 8 + (lane >> 3)) * 2048 + (8 + (lane & 7)) * 128, 0, AUX_SC1);
      asm volatile("" :: "v"(t), "v"(t2)); }
    while (lds_ld(ctr) < NU - WTRIG && now() - ts < SPIN_TICKS) __builtin_amdgcn_s_sleep(2);
    const unsigned tEnd = now() - ts;
    v4u wr[19];
#pragma unroll
    for (int i = 0; i < 19; ++i) {
      wr[i] = i < 15 ? __builtin_amdgcn_raw_buffer_load_b128(ro, vw[i % 5], so + (i / 5) * 32 * (D * 2), AUX_NT) : __builtin_amdgcn_raw_buffer_load_b128(r2, v2[i < 15 ? 0 : i - 15], s2, AUX_NT);
      SCHED(); asm volatile("s_waitcnt vmcnt(" STR(WPACE) ")" ::: "memory"); }
    SCHED();
    STAMP(5);
    LAS unsigned char* wi = L + L_WO + lane * 16;
#pragma unroll
    for (int i = 0; i < 15; ++i) *(LAS v4u*)(wi + (15 * a + i) * 1024) = wr[i];
#pragma unroll
    for (int i = 0; i < 4; ++i) if (i < 3 || v2[3] != OOB) *(LAS v4u*)(wi + (30 + 4 * a + i) * 1024) = wr[15 + i];
    STAMP(6);
    barrier();
    if (a == 1) {
      unsigned gv[8]; int go[8];
#pragma unroll
      for (int i = 0; i < 8; ++i) go[i] = (lane & 15) < 10 ? (int)WS_RX + (j * 32 + h) * 2560 + i * 320 + (lane >> 4) * 80 + (lane & 15) * 8 : OOB;
      __builtin_amdgcn_s_sleep(PD2);
      gather8<GD2, GG>(gv, go, wsr, tag, ts);
      float s = 0.f;
#pragma unroll
      for (int i = 0; i < 8; ++i) s += asf(gv[i]);
      s = tr16(s, s); s = tr32(s, s);
      const float t = rbf(rbf(dpp<0x105>(s)) + bf2f(b2v));
      const float gate = rbf(t / (1.f + __expf(-t)));
      if (lane < 5) out[oidx] = f2bf(bf2f(xres) + rbf(rbf(s) * gate));
      if (bid == 0 && lane == 0) __hip_atomic_store((GAS unsigned*)(ws + WS_EPOCH), tag, RLX_AGENT);
      STAMP(10);
      if (blockIdx.x == 0) {
        const unsigned el = now() - ts; const u64 c1 = __builtin_amdgcn_s_memtime();
        unsigned add = 0;
        if (DIAG == 1) add = el;
        if (DIAG == 2) add = tB1;
        if (DIAG == 3) add = (unsigned)((c1 - c0) * 100ull / el);
        if (DIAG == 4) add = tEnd;
        if (DIAG == 5) add = tX * 10;
        while (now() - ts < 10000u + add) __builtin_amdgcn_s_sleep(1);
      }
    }
  }
  if (CODEWARM) asm volatile(".Lfd_end:");
}

__global__ void __launch_bounds__(512) k_p(int* p) { __shared__ int l[11520]; asm volatile("" :: "v"(l) : "v223"); }
extern "C" void kernel_launch(void* const* d_in, const int* in_sizes, int n_in, void* d_out, int out_size, void* d_ws, size_t ws_size, hipStream_t stream) {
  fused_decode<<<256, NTHR, 0, stream>>>((const u16*)d_in[0], (const u16*)d_in[1], (const u16*)d_in[2], (const u16*)d_in[3], (const u16*)d_in[4],
                                         (const u16*)d_in[6], (const u16*)d_in[7], (const u16*)d_in[8], (const u16*)d_in[9], (u16*)d_out, (unsigned char*)d_ws);
  k_p<<<256, 512, 0, stream>>>((int*)d_ws);
}
```

```cpp
#include <hip/hip_runtime.h>
#include <hip/hip_bf16.h>
#include <stdint.h>

#define NS 6
#define P0 5
#define PACE 1
#define XCDGROUP 1
#define WPACE 7
#define PEPS 15
#define POFF 300
#define PM0 56
#define PDM 48
#define GD 1
#define GG 5
#define WTRIG 0
#define PRETOUCH 1
#define QPRE 0
#define PCL 256
#define PD0 6
#define PD1 6
#define PD2 12
#define GD2 GD
#define PWIN 0
#define PT0 1300
#define PDT 900
#define PLAG 130
#define PREWALK 1
#define PLEAD 30
#define QD 470
#define TG1 200
#define TG2 -100000
#define PA 3
#define PSTOP 40
#define PB 0.f
#define PKAP 0
#define PINIT 207
#define PDF PDT
#define PTH 5
#define PDN 5
#define PUP 0
#define ROT 5
#define ROTO 0
#define CONTIG 0
#define CODEWARM 1
#define TLBWARM 0
#define STAMPS 0
#define HG 1

#define GAS __attribute__((address_space(1)))
#define LAS __attribute__((address_space(3)))
#define RLX_AGENT __ATOMIC_RELAXED, __HIP_MEMORY_SCOPE_AGENT
#define RLX_WG __ATOMIC_RELAXED, __HIP_MEMORY_SCOPE_WORKGROUP
typedef unsigned v4u __attribute__((ext_vector_type(4)));
typedef unsigned v2u __attribute__((ext_vector_type(2)));
typedef short v4s __attribute__((ext_vector_type(4)));
typedef short v8s __attribute__((ext_vector_type(8)));
typedef __bf16 v8bf __attribute__((ext_vector_type(8)));
typedef float f32x4 __attribute__((ext_vector_type(4)));
typedef float f32x2 __attribute__((ext_vector_type(2)));
typedef __bf16 bf16x2 __attribute__((ext_vector_type(2)));
typedef unsigned short u16;
typedef unsigned long long u64;
typedef GAS u64 gu64;

constexpr int NB = 2, D = 640, H = 32, SK = 8192, DH = 192, F = 1536;
constexpr int NWAVE = NS + 2, NTHR = NWAVE * 64;
constexpr int NU = 256;
constexpr int KVBYTES = NB * H * SK * DH * 2;
constexpr int AUX_NT = 2;
#define KAUX 2
#define VAUX 2
constexpr int AUX_kr = KAUX, AUX_vr = VAUX;
constexpr int RSRC3 = 0x00020000;
constexpr size_t WS_EPOCH = 0;
constexpr size_t WS_QX = 4096;
constexpr size_t WS_PX = WS_QX + 256 * 512 * 8;
constexpr size_t WS_RX = WS_PX + 256 * 256 * 8;
constexpr size_t WS_CT = WS_RX + 8 * 32 * 320 * 8;
constexpr size_t WS_ST = WS_CT + 8 * 544 * 8;
constexpr int L_XN = 0;
constexpr int L_Q = L_XN + 4 * 96 * 4;
constexpr int L_HA = L_Q + 384;
constexpr int L_OA = L_HA + 192;
constexpr int L_CT = L_OA + 768;
constexpr int L_MG = L_CT + 32;
constexpr int L_WO = L_MG + NS * 800;
constexpr int L_TL = L_WO + 240 * 160;
constexpr int L_END = L_TL + (STAMPS ? 1024 + 512 : 0);
static_assert(L_WO % 16 == 0 && L_OA % 16 == 0 && L_HA % 16 == 0 && L_Q % 16 == 0 && L_CT % 16 == 0, "alignment");
constexpr unsigned SPIN_TICKS = 2000000u;

__device__ __forceinline__ float bf2f(u16 b) { return __builtin_bit_cast(float, (unsigned)b << 16); }
__device__ __forceinline__ u16 f2bf(float f) { return __builtin_bit_cast(u16, (__bf16)f); }
__device__ __forceinline__ float rbf(float f) { return bf2f(f2bf(f)); }
__device__ __forceinline__ float lo16(unsigned v) { return __builtin_bit_cast(float, v << 16); }
__device__ __forceinline__ float hi16(unsigned v) { return __builtin_bit_cast(float, v & 0xffff0000u); }
__device__ __forceinline__ float asf(unsigned v) { return __builtin_bit_cast(float, v); }
__device__ __forceinline__ unsigned asu(float v) { return __builtin_bit_cast(unsigned, v); }
__device__ __forceinline__ float dot2(unsigned k, unsigned q, float acc) {
  return __builtin_amdgcn_fdot2_f32_bf16(__builtin_bit_cast(bf16x2, k), __builtin_bit_cast(bf16x2, q), acc, false);
}
template <int CTRL> __device__ __forceinline__ float dpp(float x) {
  return __builtin_bit_cast(float, __builtin_amdgcn_mov_dpp(__builtin_bit_cast(int, x), CTRL, 0xf, 0xf, true));
}
constexpr int XOR1 = 0xB1, XOR2 = 0x4E, XOR7 = 0x141, ROR8 = 0x128, ROR4 = 0x124, ROR2 = 0x122, ROR1 = 0x121;
__device__ __forceinline__ float tr32(float x, float y) {
  auto s = __builtin_amdgcn_permlane32_swap(asu(x), asu(y), false, false); return asf(s[0]) + asf(s[1]);
}
__device__ __forceinline__ float tr16(float x, float y) {
  auto s = __builtin_amdgcn_permlane16_swap(asu(x), asu(y), false, false); return asf(s[0]) + asf(s[1]);
}
__device__ __forceinline__ float tr8(float x, float y, bool up) {
  const float keep = up ? y : x, send = up ? x : y; return keep + dpp<ROR8>(send);
}
__device__ __forceinline__ float max16(float x) { auto s = __builtin_amdgcn_permlane16_swap(asu(x), asu(x), false, false); return fmaxf(asf(s[0]), asf(s[1])); }
__device__ __forceinline__ float max32(float x) { auto s = __builtin_amdgcn_permlane32_swap(asu(x), asu(x), false, false); return fmaxf(asf(s[0]), asf(s[1])); }
__device__ __forceinline__ unsigned now() { return (unsigned)__builtin_amdgcn_s_memrealtime(); }
__device__ __forceinline__ void barrier() { asm volatile("s_waitcnt lgkmcnt(0)\n\ts_barrier" ::: "memory"); }
__device__ __forceinline__ void put(gu64* g, unsigned tag, float v) { __hip_atomic_store(g, ((u64)tag << 32) | asu(v), RLX_AGENT); }
__device__ __forceinline__ u64 get(const gu64* g) { return __hip_atomic_load(g, RLX_AGENT); }
__device__ __forceinline__ unsigned lds_ld(LAS unsigned* p) { return __builtin_amdgcn_readfirstlane(__hip_atomic_load(p, RLX_WG)); }
constexpr int OOB = 0x7ffffff0, AUX_SC1 = 16;
template <int SLEEP, int N> __device__ __forceinline__ void gather(unsigned (&val)[N], int (&off)[N], __amdgpu_buffer_rsrc_t rs, unsigned tag, unsigned ts) {
#pragma unroll
  for (int i = 0; i < N; ++i) val[i] = 0u;
  for (;;) {
    v2u in[N];
    asm volatile("" ::: "memory");
#pragma unroll
    for (int i = 0; i < N; ++i) in[i] = __builtin_amdgcn_raw_buffer_load_b64(rs, off[i], 0, AUX_SC1);
    int left = OOB;
#pragma unroll
    for (int i = 0; i < N; ++i) { const bool got = in[i][1] == tag; val[i] = got ? in[i][0] : val[i]; off[i] = got ? OOB : off[i]; left = min(left, off[i]); }
    if (__all(left == OOB) || now() - ts > SPIN_TICKS) break;
    if (SLEEP) __builtin_amdgcn_s_sleep(SLEEP);
  }
}
template <int DEPTH, int GAP> __device__ __forceinline__ void gather8(unsigned (&val)[8], int (&off)[8], __amdgpu_buffer_rsrc_t rs, unsigned tag, unsigned ts) {
#pragma unroll
  for (int i = 0; i < 8; ++i) val[i] = 0u;
  unsigned t0, t1; u64 m; const int oob = OOB;
  if constexpr (DEPTH == 1) {
    asm volatile(
      "buffer_load_dwordx2 v[208:209], %8, %[rs], 0 offen sc1\n\t"
      "buffer_load_dwordx2 v[210:211], %9, %[rs], 0 offen sc1\n\t"
      "buffer_load_dwordx2 v[212:213], %10, %[rs], 0 offen sc1\n\t"
      "buffer_load_dwordx2 v[214:215], %11, %[rs], 0 offen sc1\n\t"
      "buffer_load_dwordx2 v[216:217], %12, %[rs], 0 offen sc1\n\t"
      "buffer_load_dwordx2 v[218:219], %13, %[rs], 0 offen sc1\n\t"
      "buffer_load_dwordx2 v[220:221], %14, %[rs], 0 offen sc1\n\t"
      "buffer_load_dwordx2 v[222:223], %15, %[rs], 0 offen sc1\n\t"
      ".Lgloop%=:\n\t"
      "s_waitcnt vmcnt(0)\n\t"
      "v_cmp_eq_u32_e32 vcc, %[tag], v209\n\tv_cmp_eq_u32_e64 %[m], %[tag], v211\n\ts_nop 0\n\t"
      "v_cndmask_b32_e32 %8, %8, %[oob], vcc\n\tv_cndmask_b32_e64 %9, %9, %[oob], %[m]\n\tv_cndmask_b32_e32 %0, %0, v208, vcc\n\tv_cndmask_b32_e64 %1, %1, v210, %[m]\n\t"
      "v_cmp_eq_u32_e32 vcc, %[tag], v213\n\tv_cmp_eq_u32_e64 %[m], %[tag], v215\n\ts_nop 0\n\t"
      "v_cndmask_b32_e32 %10, %10, %[oob], vcc\n\tv_cndmask_b32_e64 %11, %11, %[oob], %[m]\n\tv_cndmask_b32_e32 %2, %2, v212, vcc\n\tv_cndmask_b32_e64 %3, %3, v214, %[m]\n\t"
      "v_cmp_eq_u32_e32 vcc, %[tag], v217\n\tv_cmp_eq_u32_e64 %[m], %[tag], v219\n\ts_nop 0\n\t"
      "v_cndmask_b32_e32 %12, %12, %[oob], vcc\n\tv_cndmask_b32_e64 %13, %13, %[oob], %[m]\n\tv_cndmask_b32_e32 %4, %4, v216, vcc\n\tv_cndmask_b32_e64 %5, %5, v218, %[m]\n\t"
      "v_cmp_eq_u32_e32 vcc, %[tag], v221\n\tv_cmp_eq_u32_e64 %[m], %[tag], v223\n\ts_nop 0\n\t"
      "v_cndmask_b32_e32 %14, %14, %[oob], vcc\n\tv_cndmask_b32_e64 %15, %15, %[oob], %[m]\n\tv_cndmask_b32_e32 %6, %6, v220, vcc\n\tv_cndmask_b32_e64 %7, %7, v222, %[m]\n\t"
      "v_min3_u32 %[t0], %8, %9, %10\n\tv_min3_u32 %[t1], %11, %12, %13\n\tv_min3_u32 %[t0], %[t0], %14, %15\n\tv_min_u32_e32 %[t0], %[t0], %[t1]\n\t"
      "v_cmp_ne_u32_e32 vcc, %[t0], %[oob]\n\ts_nop 1\n\ts_cbranch_vccz .Lgdone%=\n\t"
      "buffer_load_dwordx2 v[208:209], %8, %[rs], 0 offen sc1\n\t"
      "buffer_load_dwordx2 v[210:211], %9, %[rs], 0 offen sc1\n\t"
      "buffer_load_dwordx2 v[212:213], %10, %[rs], 0 offen sc1\n\t"
      "buffer_load_dwordx2 v[214:215], %11, %[rs], 0 offen sc1\n\t"
      "buffer_load_dwordx2 v[216:217], %12, %[rs], 0 offen sc1\n\t"
      "buffer_load_dwordx2 v[218:219], %13, %[rs], 0 offen sc1\n\t"
      "buffer_load_dwordx2 v[220:221], %14, %[rs], 0 offen sc1\n\t"
      "buffer_load_dwordx2 v[222:223], %15, %[rs], 0 offen sc1\n\t"
      "s_memrealtime vcc\n\ts_waitcnt lgkmcnt(0)\n\ts_sub_u32 vcc_lo, vcc_lo, %[ts]\n\ts_cmp_lt_u32 vcc_lo, %[lim]\n\ts_cbranch_scc1 .Lgloop%=\n"
      ".Lgdone%=:"
      : "+v"(val[0]), "+v"(val[1]), "+v"(val[2]), "+v"(val[3]), "+v"(val[4]), "+v"(val[5]), "+v"(val[6]), "+v"(val[7]),
        "+v"(off[0]), "+v"(off[1]), "+v"(off[2]), "+v"(off[3]), "+v"(off[4]), "+v"(off[5]), "+v"(off[6]), "+v"(off[7]),
        [t0] "=&v"(t0), [t1] "=&v"(t1), [m] "=&s"(m)
      : [rs] "s"(rs), [tag] "s"(tag), [oob] "v"(oob), [ts] "s"(ts), [lim] "s"(SPIN_TICKS), [gap] "n"(GAP)
      : "memory", "vcc", "scc", "v208", "v209", "v210", "v211", "v212", "v213", "v214", "v215", "v216", "v217", "v218", "v219", "v220", "v221", "v222", "v223");
  } else if constexpr (DEPTH == 2) {
    asm volatile(
      "buffer_load_dwordx2 v[208:209], %8, %[rs], 0 offen sc1\n\t"
      "buffer_load_dwordx2 v[210:211], %9, %[rs], 0 offen sc1\n\t"
      "buffer_load_dwordx2 v[212:213], %10, %[rs], 0 offen sc1\n\t"
      "buffer_load_dwordx2 v[214:215], %11, %[rs], 0 offen sc1\n\t"
      "buffer_load_dwordx2 v[216:217], %12, %[rs], 0 offen sc1\n\t"
      "buffer_load_dwordx2 v[218:219], %13, %[rs], 0 offen sc1\n\t"
      "buffer_load_dwordx2 v[220:221], %14, %[rs], 0 offen sc1\n\t"
      "buffer_load_dwordx2 v[222:223], %15, %[rs], 0 offen sc1\n\t"
      "s_sleep %[gap]\n\t"
      "buffer_load_dwordx2 v[224:225], %8, %[rs], 0 offen sc1\n\t"
      "buffer_load_dwordx2 v[226:227], %9, %[rs], 0 offen sc1\n\t"
      "buffer_load_dwordx2 v[228:229], %10, %[rs], 0 offen sc1\n\t"
      "buffer_load_dwordx2 v[230:231], %11, %[rs], 0 offen sc1\n\t"
      "buffer_load_dwordx2 v[232:233], %12, %[rs], 0 offen sc1\n\t"
      "buffer_load_dwordx2 v[234:235], %13, %[rs], 0 offen sc1\n\t"
      "buffer_load_dwordx2 v[236:237], %14, %[rs], 0 offen sc1\n\t"
      "buffer_load_dwordx2 v[238:239], %15, %[rs], 0 offen sc1\n\t"
      ".Lgloop%=:\n\t"
      "s_waitcnt vmcnt(8)\n\t"
      "v_cmp_eq_u32_e32 vcc, %[tag], v209\n\tv_cmp_eq_u32_e64 %[m], %[tag], v211\n\ts_nop 0\n\t"
      "v_cndmask_b32_e32 %8, %8, %[oob], vcc\n\tv_cndmask_b32_e64 %9, %9, %[oob], %[m]\n\tv_cndmask_b32_e32 %0, %0, v208, vcc\n\tv_cndmask_b32_e64 %1, %1, v210, %[m]\n\t"
      "v_cmp_eq_u32_e32 vcc, %[tag], v213\n\tv_cmp_eq_u32_e64 %[m], %[tag], v215\n\ts_nop 0\n\t"
      "v_cndmask_b32_e32 %10, %10, %[oob], vcc\n\tv_cndmask_b32_e64 %11, %11, %[oob], %[m]\n\tv_cndmask_b32_e32 %2, %2, v212, vcc\n\tv_cndmask_b32_e64 %3, %3, v214, %[m]\n\t"
      "v_cmp_eq_u32_e32 vcc, %[tag], v217\n\tv_cmp_eq_u32_e64 %[m], %[tag], v219\n\ts_nop 0\n\t"
      "v_cndmask_b32_e32 %12, %12, %[oob], vcc\n\tv_cndmask_b32_e64 %13, %13, %[oob], %[m]\n\tv_cndmask_b32_e32 %4, %4, v216, vcc\n\tv_cndmask_b32_e64 %5, %5, v218, %[m]\n\t"
      "v_cmp_eq_u32_e32 vcc, %[tag], v221\n\tv_cmp_eq_u32_e64 %[m], %[tag], v223\n\ts_nop 0\n\t"
      "v_cndmask_b32_e32 %14, %14, %[oob], vcc\n\tv_cndmask_b32_e64 %15, %15, %[oob], %[m]\n\tv_cndmask_b32_e32 %6, %6, v220, vcc\n\tv_cndmask_b32_e64 %7, %7, v222, %[m]\n\t"
      "v_min3_u32 %[t0], %8, %9, %10\n\tv_min3_u32 %[t1], %11, %12, %13\n\tv_min3_u32 %[t0], %[t0], %14, %15\n\tv_min_u32_e32 %[t0], %[t0], %[t1]\n\t"
      "v_cmp_ne_u32_e32 vcc, %[t0], %[oob]\n\ts_nop 1\n\ts_cbranch_vccz .Lgdone%=\n\t"
      "buffer_load_dwordx2 v[208:209], %8, %[rs], 0 offen sc1\n\t"
      "buffer_load_dwordx2 v[210:211], %9, %[rs], 0 offen sc1\n\t"
      "buffer_load_dwordx2 v[212:213], %10, %[rs], 0 offen sc1\n\t"
      "buffer_load_dwordx2 v[214:215], %11, %[rs], 0 offen sc1\n\t"
      "buffer_load_dwordx2 v[216:217], %12, %[rs], 0 offen sc1\n\t"
      "buffer_load_dwordx2 v[218:219], %13, %[rs], 0 offen sc1\n\t"
      "buffer_load_dwordx2 v[220:221], %14, %[rs], 0 offen sc1\n\t"
      "buffer_load_dwordx2 v[222:223], %15, %[rs], 0 offen sc1\n\t"
      "s_waitcnt vmcnt(8)\n\t"
      "v_cmp_eq_u32_e32 vcc, %[tag], v225\n\tv_cmp_eq_u32_e64 %[m], %[tag], v227\n\ts_nop 0\n\t"
      "v_cndmask_b32_e32 %8, %8, %[oob], vcc\n\tv_cndmask_b32_e64 %9, %9, %[oob], %[m]\n\tv_cndmask_b32_e32 %0, %0, v224, vcc\n\tv_cndmask_b32_e64 %1, %1, v226, %[m]\n\t"
      "v_cmp_eq_u32_e32 vcc, %[tag], v229\n\tv_cmp_eq_u32_e64 %[m], %[tag], v231\n\ts_nop 0\n\t"
      "v_cndmask_b32_e32 %10, %10, %[oob], vcc\n\tv_cndmask_b32_e64 %11, %11, %[oob], %[m]\n\tv_cndmask_b32_e32 %2, %2, v228, vcc\n\tv_cndmask_b32_e64 %3, %3, v230, %[m]\n\t"
      "v_cmp_eq_u32_e32 vcc, %[tag], v233\n\tv_cmp_eq_u32_e64 %[m], %[tag], v235\n\ts_nop 0\n\t"
      "v_cndmask_b32_e32 %12, %12, %[oob], vcc\n\tv_cndmask_b32_e64 %13, %13, %[oob], %[m]\n\tv_cndmask_b32_e32 %4, %4, v232, vcc\n\tv_cndmask_b32_e64 %5, %5, v234, %[m]\n\t"
      "v_cmp_eq_u32_e32 vcc, %[tag], v237\n\tv_cmp_eq_u32_e64 %[m], %[tag], v239\n\ts_nop 0\n\t"
      "v_cndmask_b32_e32 %14, %14, %[oob], vcc\n\tv_cndmask_b32_e64 %15, %15, %[oob], %[m]\n\tv_cndmask_b32_e32 %6, %6, v236, vcc\n\tv_cndmask_b32_e64 %7, %7, v238, %[m]\n\t"
      "v_min3_u32 %[t0], %8, %9, %10\n\tv_min3_u32 %[t1], %11, %12, %13\n\tv_min3_u32 %[t0], %[t0], %14, %15\n\tv_min_u32_e32 %[t0], %[t0], %[t1]\n\t"
      "v_cmp_ne_u32_e32 vcc, %[t0], %[oob]\n\ts_nop 1\n\ts_cbranch_vccz .Lgdone%=\n\t"
      "buffer_load_dwordx2 v[224:225], %8, %[rs], 0 offen sc1\n\t"
      "buffer_load_dwordx2 v[226:227], %9, %[rs], 0 offen sc1\n\t"
      "buffer_load_dwordx2 v[228:229], %10, %[rs], 0 offen sc1\n\t"
      "buffer_load_dwordx2 v[230:231], %11, %[rs], 0 offen sc1\n\t"
      "buffer_load_dwordx2 v[232:233], %12, %[rs], 0 offen sc1\n\t"
      "buffer_load_dwordx2 v[234:235], %13, %[rs], 0 offen sc1\n\t"
      "buffer_load_dwordx2 v[236:237], %14, %[rs], 0 offen sc1\n\t"
      "buffer_load_dwordx2 v[238:239], %15, %[rs], 0 offen sc1\n\t"
      "s_memrealtime vcc\n\ts_waitcnt lgkmcnt(0)\n\ts_sub_u32 vcc_lo, vcc_lo, %[ts]\n\ts_cmp_lt_u32 vcc_lo, %[lim]\n\ts_cbranch_scc1 .Lgloop%=\n"
      ".Lgdone%=:"
      : "+v"(val[0]), "+v"(val[1]), "+v"(val[2]), "+v"(val[3]), "+v"(val[4]), "+v"(val[5]), "+v"(val[6]), "+v"(val[7]),
        "+v"(off[0]), "+v"(off[1]), "+v"(off[2]), "+v"(off[3]), "+v"(off[4]), "+v"(off[5]), "+v"(off[6]), "+v"(off[7]),
        [t0] "=&v"(t0), [t1] "=&v"(t1), [m] "=&s"(m)
      : [rs] "s"(rs), [tag] "s"(tag), [oob] "v"(oob), [ts] "s"(ts), [lim] "s"(SPIN_TICKS), [gap] "n"(GAP)
      : "memory", "vcc", "scc", "v208", "v209", "v210", "v211", "v212", "v213", "v214", "v215", "v216", "v217", "v218", "v219", "v220", "v221", "v222", "v223", "v224", "v225", "v226", "v227", "v228", "v229", "v230", "v231", "v232", "v233", "v234", "v235", "v236", "v237", "v238", "v239");
  } else {
    asm volatile(
      "buffer_load_dwordx2 v[208:209], %8, %[rs], 0 offen sc1\n\t"
      "buffer_load_dwordx2 v[210:211], %9, %[rs], 0 offen sc1\n\t"
      "buffer_load_dwordx2 v[212:213], %10, %[rs], 0 offen sc1\n\t"
      "buffer_load_dwordx2 v[214:215], %11, %[rs], 0 offen sc1\n\t"
      "buffer_load_dwordx2 v[216:217], %12, %[rs], 0 offen sc1\n\t"
      "buffer_load_dwordx2 v[218:219], %13, %[rs], 0 offen sc1\n\t"
      "buffer_load_dwordx2 v[220:221], %14, %[rs], 0 offen sc1\n\t"
      "buffer_load_dwordx2 v[222:223], %15, %[rs], 0 offen sc1\n\t"
      "s_sleep %[gap]\n\t"
      "buffer_load_dwordx2 v[224:225], %8, %[rs], 0 offen sc1\n\t"
      "buffer_load_dwordx2 v[226:227], %9, %[rs], 0 offen sc1\n\t"
      "buffer_load_dwordx2 v[228:229], %10, %[rs], 0 offen sc1\n\t"
      "buffer_load_dwordx2 v[230:231], %11, %[rs], 0 offen sc1\n\t"
      "buffer_load_dwordx2 v[232:233], %12, %[rs], 0 offen sc1\n\t"
      "buffer_load_dwordx2 v[234:235], %13, %[rs], 0 offen sc1\n\t"
      "buffer_load_dwordx2 v[236:237], %14, %[rs], 0 offen sc1\n\t"
      "buffer_load_dwordx2 v[238:239], %15, %[rs], 0 offen sc1\n\t"
      "s_sleep %[gap]\n\t"
      "buffer_load_dwordx2 v[240:241], %8, %[rs], 0 offen sc1\n\t"
      "buffer_load_dwordx2 v[242:243], %9, %[rs], 0 offen sc1\n\t"
      "buffer_load_dwordx2 v[244:245], %10, %[rs], 0 offen sc1\n\t"
      "buffer_load_dwordx2 v[246:247], %11, %[rs], 0 offen sc1\n\t"
      "buffer_load_dwordx2 v[248:249], %12, %[rs], 0 offen sc1\n\t"
      "buffer_load_dwordx2 v[250:251], %13, %[rs], 0 offen sc1\n\t"
      "buffer_load_dwordx2 v[252:253], %14, %[rs], 0 offen sc1\n\t"
      "buffer_load_dwordx2 v[254:255], %15, %[rs], 0 offen sc1\n\t"
      ".Lgloop%=:\n\t"
      "s_waitcnt vmcnt(16)\n\t"
      "v_cmp_eq_u32_e32 vcc, %[tag], v209\n\tv_cmp_eq_u32_e64 %[m], %[tag], v211\n\ts_nop 0\n\t"
      "v_cndmask_b32_e32 %8, %8, %[oob], vcc\n\tv_cndmask_b32_e64 %9, %9, %[oob], %[m]\n\tv_cndmask_b32_e32 %0, %0, v208, vcc\n\tv_cndmask_b32_e64 %1, %1, v210, %[m]\n\t"
      "v_cmp_eq_u32_e32 vcc, %[tag], v213\n\tv_cmp_eq_u32_e64 %[m], %[tag], v215\n\ts_nop 0\n\t"
      "v_cndmask_b32_e32 %10, %10, %[oob], vcc\n\tv_cndmask_b32_e64 %11, %11, %[oob], %[m]\n\tv_cndmask_b32_e32 %2, %2, v212, vcc\n\tv_cndmask_b32_e64 %3, %3, v214, %[m]\n\t"
      "v_cmp_eq_u32_e32 vcc, %[tag], v217\n\tv_cmp_eq_u32_e64 %[m], %[tag], v219\n\ts_nop 0\n\t"
      "v_cndmask_b32_e32 %12, %12, %[oob], vcc\n\tv_cndmask_b32_e64 %13, %13, %[oob], %[m]\n\tv_cndmask_b32_e32 %4, %4, v216, vcc\n\tv_cndmask_b32_e64 %5, %5, v218, %[m]\n\t"
      "v_cmp_eq_u32_e32 vcc, %[tag], v221\n\tv_cmp_eq_u32_e64 %[m], %[tag], v223\n\ts_nop 0\n\t"
      "v_cndmask_b32_e32 %14, %14, %[oob], vcc\n\tv_cndmask_b32_e64 %15, %15, %[oob], %[m]\n\tv_cndmask_b32_e32 %6, %6, v220, vcc\n\tv_cndmask_b32_e64 %7, %7, v222, %[m]\n\t"
      "v_min3_u32 %[t0], %8, %9, %10\n\tv_min3_u32 %[t1], %11, %12, %13\n\tv_min3_u32 %[t0], %[t0], %14, %15\n\tv_min_u32_e32 %[t0], %[t0], %[t1]\n\t"
      "v_cmp_ne_u32_e32 vcc, %[t0], %[oob]\n\ts_nop 1\n\ts_cbranch_vccz .Lgdone%=\n\t"
      "buffer_load_dwordx2 v[208:209], %8, %[rs], 0 offen sc1\n\t"
      "buffer_load_dwordx2 v[210:211], %9, %[rs], 0 offen sc1\n\t"
      "buffer_load_dwordx2 v[212:213], %10, %[rs], 0 offen sc1\n\t"
      "buffer_load_dwordx2 v[214:215], %11, %[rs], 0 offen sc1\n\t"
      "buffer_load_dwordx2 v[216:217], %12, %[rs], 0 offen sc1\n\t"
      "buffer_load_dwordx2 v[218:219], %13, %[rs], 0 offen sc1\n\t"
      "buffer_load_dwordx2 v[220:221], %14, %[rs], 0 offen sc1\n\t"
      "buffer_load_dwordx2 v[222:223], %15, %[rs], 0 offen sc1\n\t"
      "s_waitcnt vmcnt(16)\n\t"
      "v_cmp_eq_u32_e32 vcc, %[tag], v225\n\tv_cmp_eq_u32_e64 %[m], %[tag], v227\n\ts_nop 0\n\t"
      "v_cndmask_b32_e32 %8, %8, %[oob], vcc\n\tv_cndmask_b32_e64 %9, %9, %[oob], %[m]\n\tv_cndmask_b32_e32 %0, %0, v224, vcc\n\tv_cndmask_b32_e64 %1, %1, v226, %[m]\n\t"
      "v_cmp_eq_u32_e32 vcc, %[tag], v229\n\tv_cmp_eq_u32_e64 %[m], %[tag], v231\n\ts_nop 0\n\t"
      "v_cndmask_b32_e32 %10, %10, %[oob], vcc\n\tv_cndmask_b32_e64 %11, %11, %[oob], %[m]\n\tv_cndmask_b32_e32 %2, %2, v228, vcc\n\tv_cndmask_b32_e64 %3, %3, v230, %[m]\n\t"
      "v_cmp_eq_u32_e32 vcc, %[tag], v233\n\tv_cmp_eq_u32_e64 %[m], %[tag], v235\n\ts_nop 0\n\t"
      "v_cndmask_b32_e32 %12, %12, %[oob], vcc\n\tv_cndmask_b32_e64 %13, %13, %[oob], %[m]\n\tv_cndmask_b32_e32 %4, %4, v232, vcc\n\tv_cndmask_b32_e64 %5, %5, v234, %[m]\n\t"
      "v_cmp_eq_u32_e32 vcc, %[tag], v237\n\tv_cmp_eq_u32_e64 %[m], %[tag], v239\n\ts_nop 0\n\t"
      "v_cndmask_b32_e32 %14, %14, %[oob], vcc\n\tv_cndmask_b32_e64 %15, %15, %[oob], %[m]\n\tv_cndmask_b32_e32 %6, %6, v236, vcc\n\tv_cndmask_b32_e64 %7, %7, v238, %[m]\n\t"
      "v_min3_u32 %[t0], %8, %9, %10\n\tv_min3_u32 %[t1], %11, %12, %13\n\tv_min3_u32 %[t0], %[t0], %14, %15\n\tv_min_u32_e32 %[t0], %[t0], %[t1]\n\t"
      "v_cmp_ne_u32_e32 vcc, %[t0], %[oob]\n\ts_nop 1\n\ts_cbranch_vccz .Lgdone%=\n\t"
      "buffer_load_dwordx2 v[224:225], %8, %[rs], 0 offen sc1\n\t"
      "buffer_load_dwordx2 v[226:227], %9, %[rs], 0 offen sc1\n\t"
      "buffer_load_dwordx2 v[228:229], %10, %[rs], 0 offen sc1\n\t"
      "buffer_load_dwordx2 v[230:231], %11, %[rs], 0 offen sc1\n\t"
      "buffer_load_dwordx2 v[232:233], %12, %[rs], 0 offen sc1\n\t"
      "buffer_load_dwordx2 v[234:235], %13, %[rs], 0 offen sc1\n\t"
      "buffer_load_dwordx2 v[236:237], %14, %[rs], 0 offen sc1\n\t"
      "buffer_load_dwordx2 v[238:239], %15, %[rs], 0 offen sc1\n\t"
      "s_waitcnt vmcnt(16)\n\t"
      "v_cmp_eq_u32_e32 vcc, %[tag], v241\n\tv_cmp_eq_u32_e64 %[m], %[tag], v243\n\ts_nop 0\n\t"
      "v_cndmask_b32_e32 %8, %8, %[oob], vcc\n\tv_cndmask_b32_e64 %9, %9, %[oob], %[m]\n\tv_cndmask_b32_e32 %0, %0, v240, vcc\n\tv_cndmask_b32_e64 %1, %1, v242, %[m]\n\t"
      "v_cmp_eq_u32_e32 vcc, %[tag], v245\n\tv_cmp_eq_u32_e64 %[m], %[tag], v247\n\ts_nop 0\n\t"
      "v_cndmask_b32_e32 %10, %10, %[oob], vcc\n\tv_cndmask_b32_e64 %11, %11, %[oob], %[m]\n\tv_cndmask_b32_e32 %2, %2, v244, vcc\n\tv_cndmask_b32_e64 %3, %3, v246, %[m]\n\t"
      "v_cmp_eq_u32_e32 vcc, %[tag], v249\n\tv_cmp_eq_u32_e64 %[m], %[tag], v251\n\ts_nop 0\n\t"
      "v_cndmask_b32_e32 %12, %12, %[oob], vcc\n\tv_cndmask_b32_e64 %13, %13, %[oob], %[m]\n\tv_cndmask_b32_e32 %4, %4, v248, vcc\n\tv_cndmask_b32_e64 %5, %5, v250, %[m]\n\t"
      "v_cmp_eq_u32_e32 vcc, %[tag], v253\n\tv_cmp_eq_u32_e64 %[m], %[tag], v255\n\ts_nop 0\n\t"
      "v_cndmask_b32_e32 %14, %14, %[oob], vcc\n\tv_cndmask_b32_e64 %15, %15, %[oob], %[m]\n\tv_cndmask_b32_e32 %6, %6, v252, vcc\n\tv_cndmask_b32_e64 %7, %7, v254, %[m]\n\t"
      "v_min3_u32 %[t0], %8, %9, %10\n\tv_min3_u32 %[t1], %11, %12, %13\n\tv_min3_u32 %[t0], %[t0], %14, %15\n\tv_min_u32_e32 %[t0], %[t0], %[t1]\n\t"
      "v_cmp_ne_u32_e32 vcc, %[t0], %[oob]\n\ts_nop 1\n\ts_cbranch_vccz .Lgdone%=\n\t"
      "buffer_load_dwordx2 v[240:241], %8, %[rs], 0 offen sc1\n\t"
      "buffer_load_dwordx2 v[242:243], %9, %[rs], 0 offen sc1\n\t"
      "buffer_load_dwordx2 v[244:245], %10, %[rs], 0 offen sc1\n\t"
      "buffer_load_dwordx2 v[246:247], %11, %[rs], 0 offen sc1\n\t"
      "buffer_load_dwordx2 v[248:249], %12, %[rs], 0 offen sc1\n\t"
      "buffer_load_dwordx2 v[250:251], %13, %[rs], 0 offen sc1\n\t"
      "buffer_load_dwordx2 v[252:253], %14, %[rs], 0 offen sc1\n\t"
      "buffer_load_dwordx2 v[254:255], %15, %[rs], 0 offen sc1\n\t"
      "s_memrealtime vcc\n\ts_waitcnt lgkmcnt(0)\n\ts_sub_u32 vcc_lo, vcc_lo, %[ts]\n\ts_cmp_lt_u32 vcc_lo, %[lim]\n\ts_cbranch_scc1 .Lgloop%=\n"
      ".Lgdone%=:"
      : "+v"(val[0]), "+v"(val[1]), "+v"(val[2]), "+v"(val[3]), "+v"(val[4]), "+v"(val[5]), "+v"(val[6]), "+v"(val[7]),
        "+v"(off[0]), "+v"(off[1]), "+v"(off[2]), "+v"(off[3]), "+v"(off[4]), "+v"(off[5]), "+v"(off[6]), "+v"(off[7]),
        [t0] "=&v"(t0), [t1] "=&v"(t1), [m] "=&s"(m)
      : [rs] "s"(rs), [tag] "s"(tag), [oob] "v"(oob), [ts] "s"(ts), [lim] "s"(SPIN_TICKS), [gap] "n"(GAP)
      : "memory", "vcc", "scc", "v208", "v209", "v210", "v211", "v212", "v213", "v214", "v215", "v216", "v217", "v218", "v219", "v220", "v221", "v222", "v223", "v224", "v225", "v226", "v227", "v228", "v229", "v230", "v231", "v232", "v233", "v234", "v235", "v236", "v237", "v238", "v239", "v240", "v241", "v242", "v243", "v244", "v245", "v246", "v247", "v248", "v249", "v250", "v251", "v252", "v253", "v254", "v255");
  }
}
#define PW 2
#define STR2(x) #x
#define STR(x) STR2(x)
#define PWAIT() asm volatile("s_waitcnt vmcnt(" STR(PW) ")" ::: "memory")
#define SCHED() do { asm volatile("" ::: "memory"); __builtin_amdgcn_sched_barrier(0); } while (0)
#define STAMP(i) do {} while (0)
#define STAMP_DUMP() do {} while (0)
#define UT(u) do {} while (0)

__global__ void __launch_bounds__(NTHR, 2)
fused_decode(const u16* x, const u16* kc, const u16* vc, const u16* wq, const u16* wo,
             const u16* w1, const u16* b1, const u16* w2, const u16* b2, u16* out, unsigned char* ws) {
  __shared__ __attribute__((aligned(16))) unsigned char lds_raw[L_END];
  LAS unsigned char* L = (LAS unsigned char*)lds_raw;
  const int bid = (blockIdx.x & 7) * 32 + (blockIdx.x >> 3);
  const int h = bid >> 3, j = bid & 7, b = j >> 2, sp = j & 3;
  const int tid = threadIdx.x, lane = tid & 63, w = __builtin_amdgcn_readfirstlane(tid >> 6), r = lane >> 3, c = lane & 7;
  unsigned warm = 0;
  if (CODEWARM && w == 0) {
    u64 pc; unsigned len;
    asm volatile("s_getpc_b64 %0\n.Lfd_here:\n\ts_mov_b32 %1, .Lfd_end-.Lfd_here" : "=s"(pc), "=s"(len));
    const unsigned off = min((unsigned)(((blockIdx.x >> 3) & 3) * 8192 + lane * 128), len - 4u) & ~3u;
    warm = *(const GAS unsigned*)((pc & ~127ull) + off);
  }
  SCHED();
  LAS unsigned* ctr = (LAS unsigned*)(L + L_CT);
  LAS u64* pace = (LAS u64*)(L + L_CT + 8);
  LAS unsigned* done = (LAS unsigned*)(L + L_CT + 16);
  const unsigned ts = now();
  STAMP(0);
  unsigned tag;
  constexpr float C2 = 0.07216878364870322f * 1.4426950408889634f;

  const int rot = (CONTIG ? (((b * H + h) * 4 + sp) * ROT) : (b * H + h) * ROT + (ROTO)) & (NU - 1);
  const __amdgpu_buffer_rsrc_t wsr = __builtin_amdgcn_make_buffer_rsrc((void*)ws, 0, (int)WS_ST, RSRC3);

  if (w < NS) {
    if (tid == 0) { *ctr = NS * P0; *pace = 0ull; *done = 0u; done[1] = 0u; }
    const int oi = 5 * h + (lane < 5 ? lane : 0), ocol = 80 * j + oi % 80, oidx = (oi / 80) * D + ocol;
    u16 xres = 0, b2v = 0;
    const int rbase = ((b * H + h) * SK + sp * (CONTIG ? SK / 4 : 8)) * (DH * 2);
    constexpr int USTR = (CONTIG ? 1 : 4) * 8 * DH * 2;
    const int voff = r * (DH * 2) + c * 16;
    v4u Kb[P0][3], Vb[P0][3];
    const __amdgpu_buffer_rsrc_t kr = __builtin_amdgcn_make_buffer_rsrc((void*)kc, 0, KVBYTES, RSRC3), vr = __builtin_amdgcn_make_buffer_rsrc((void*)vc, 0, KVBYTES, RSRC3);
#define LD(dst, rs, t) { const int so_ = rbase + (((t) + rot) & (NU - 1)) * USTR; \
    _Pragma("unroll") for (int u = 0; u < 3; ++u) dst[u] = __builtin_amdgcn_raw_buffer_load_b128(rs, voff + u * 128, so_, AUX_##rs); }
#pragma unroll
    for (int i = 0; i < P0; ++i) {
      LD(Kb[i], kr, w + i * NS) UT(w + i * NS); SCHED(); PWAIT(); LD(Vb[i], vr, w + i * NS) SCHED(); if (i + 1 < P0) PWAIT(); }
    SCHED();
    STAMP(1);
    barrier();
    STAMP(2);
    asm volatile("" :: "v"(warm));
    if (w == 5) { xres = x[oidx]; b2v = b2[ocol]; }
    tag = lds_ld(ctr + 1);
    v4u qv[3];
#pragma unroll
    for (int u = 0; u < 3; ++u) qv[u] = *(LAS v4u*)(L + L_Q + u * 128 + c * 16);
    float m = -INFINITY, l = 0.f; f32x2 ac2[12];
#pragma unroll
    for (int k = 0; k < 12; ++k) ac2[k] = f32x2{0.f, 0.f};
#define QK(Kx, s) { float d0 = 0.f, d1 = 0.f; \
      _Pragma("unroll") for (int u = 0; u < 3; ++u) { d0 = dot2(Kx[u][0], qv[u][0], d0); d1 = dot2(Kx[u][1], qv[u][1], d1); d0 = dot2(Kx[u][2], qv[u][2], d0); d1 = dot2(Kx[u][3], qv[u][3], d1); } \
      s = d0 + d1; asm volatile("" : "+v"(s)); }
#define PV(Vx, s) { s += dpp<XOR1>(s); s += dpp<XOR2>(s); s += dpp<XOR7>(s); s *= C2; \
      const float mn = fmaxf(m, s), al = __builtin_amdgcn_exp2f(m - mn), p = __builtin_amdgcn_exp2f(s - mn); m = mn; l = fmaf(l, al, p); \
      _Pragma("unroll") for (int u = 0; u < 3; ++u) { SCHED(); _Pragma("unroll") for (int k = 0; k < 4; ++k) \
          ac2[u * 4 + k] = __builtin_elementwise_fma(f32x2{p, p}, f32x2{lo16(Vx[u][k]), hi16(Vx[u][k])}, ac2[u * 4 + k] * al); \
        asm volatile("" : "+v"(ac2[u * 4]), "+v"(ac2[u * 4 + 1]), "+v"(ac2[u * 4 + 2]), "+v"(ac2[u * 4 + 3])); } }
    const unsigned ctra = L_CT + (unsigned)(uintptr_t)L, one = 1u;
#define COUNT() asm volatile("s_mov_b64 exec, 1\n\tds_add_u32 %0, %1\n\ts_mov_b64 exec, -1" :: "v"(ctra), "v"(one) : "memory")
    float s;
    int nxt = w + NS * P0;
#define STEP() { QK(Kb[0], s) SCHED(); LD(Kb[0], kr, nxt) UT(nxt); SCHED(); PV(Vb[0], s) SCHED(); LD(Vb[0], vr, nxt) COUNT(); SCHED(); nxt += NS; }
    static_assert(NS * (P0 + P0 / 2 + 1) <= NU, "the first steps load real units");
    STEP()
#pragma unroll
    for (int i = 1; i < P0; ++i) { QK(Kb[i], s) PV(Vb[i], s) SCHED(); if (i % 2 == 0) STEP() }
    STAMP(3);
    while (nxt < NU) {
      QK(Kb[0], s)
#pragma unroll 1
      for (int spn = 0; spn < 256; ++spn) {
        const u64 pv = __hip_atomic_load(pace, RLX_WG);
        const float t0 = asf(__builtin_amdgcn_readfirstlane((unsigned)pv)), tau = asf(__builtin_amdgcn_readfirstlane((unsigned)(pv >> 32)));
        if (fmaf((float)nxt, tau, t0) <= (float)(int)(now() - ts)) break;
        __builtin_amdgcn_s_sleep(2);
      }
      SCHED(); LD(Kb[0], kr, nxt) UT(nxt); SCHED();
      PV(Vb[0], s)
      SCHED(); LD(Vb[0], vr, nxt) COUNT(); SCHED();
      nxt += NS;
    }
    QK(Kb[0], s)
    PV(Vb[0], s)
    STAMP(4);
    float M = fmaxf(m, dpp<ROR8>(m)); M = max16(M); M = max32(M);
    const float sc = __builtin_amdgcn_exp2f(m - M);
    l *= sc; l += dpp<ROR8>(l); l = tr16(l, l); l = tr32(l, l);
    float acc[24];
#pragma unroll
    for (int k = 0; k < 12; ++k) { const f32x2 t = ac2[k] * sc; acc[2 * k] = t[0]; acc[2 * k + 1] = t[1]; }
#pragma unroll
    for (int k = 0; k < 12; ++k) acc[k] = tr32(acc[k], acc[k + 12]);
#pragma unroll
    for (int k = 0; k < 6; ++k) acc[k] = tr16(acc[k], acc[k + 6]);
#pragma unroll
    for (int k = 0; k < 3; ++k) acc[k] = tr8(acc[k], acc[k + 3], r & 1);
    LAS float* mg = (LAS float*)(L + L_MG) + w * 200;
#pragma unroll
    for (int k = 0; k < 3; ++k) mg[k * 64 + lane] = acc[k];
    if (lane == 0) { mg[192] = M; mg[193] = l; }
    asm volatile("s_waitcnt lgkmcnt(0)\n\ts_mov_b64 exec, 1\n\tds_add_u32 %0, %1 offset:16\n\ts_mov_b64 exec, -1" :: "v"(ctra), "v"(one) : "memory");
    while (lds_ld(done) < NS && now() - ts < SPIN_TICKS) {}
    asm volatile("" ::: "memory");
    if (w >= 3) __builtin_amdgcn_s_sleep(PD0);
    else {
      STAMP(5);
      const LAS float* mg0 = (const LAS float*)(L + L_MG);
      float MM = -INFINITY;
#pragma unroll
      for (int i = 0; i < NS; ++i) MM = fmaxf(MM, mg0[i * 200 + 192]);
      float Ls = 0.f, o = 0.f;
#pragma unroll
      for (int i = 0; i < NS; ++i) { const float e = __builtin_amdgcn_exp2f(mg0[i * 200 + 192] - MM); Ls = fmaf(e, mg0[i * 200 + 193], Ls); o = fmaf(e, mg0[i * 200 + tid], o); }
      gu64* px = (gu64*)(ws + WS_PX) + bid * 256;
      put(px + tid, tag, o * __builtin_amdgcn_rcpf(Ls));
      if (tid == 0) put(px + 192, tag, MM + __builtin_amdgcn_logf(Ls));
    }
    STAMP(6);
    static_assert(NS == 6, "waves 0..2 | 3..5 gather batch row 0 | 1");
    const int bb = w >= 3, pos = tid - bb * 192;
    const int vo1 = (int)WS_PX + (h * 8 + bb * 4) * 2048;
    const int di = (lane >> 5) * 12 + ((lane >> 4) & 1) * 6 + ((lane >> 3) & 1) * 3 + (w - 3 * bb);
    LAS u16* oa = (LAS u16*)(L + L_OA + (bb * DH + (di >> 3) * 64 + c * 8 + (di & 7)) * 2);
    unsigned gv[8]; int go[8];
#pragma unroll
    for (int i = 0; i < 8; ++i) go[i] = vo1 + (i & 3) * 2048 + (i < 4 ? pos : 192) * 8;
    const u64 pv = __hip_atomic_load(pace, RLX_WG);
    const int tend = (int)fmaf((float)NU, asf(__builtin_amdgcn_readfirstlane((unsigned)(pv >> 32))), asf(__builtin_amdgcn_readfirstlane((unsigned)pv)));
    __builtin_amdgcn_s_sleep(PD1);
    { const unsigned tb = now(); while ((int)(now() - ts) < tend + TG1 && now() - tb < 300u) __builtin_amdgcn_s_sleep(1); }
    gather8<GD, GG>(gv, go, wsr, tag, ts);
    { const float e0 = asf(gv[4]), e1 = asf(gv[5]), e2 = asf(gv[6]), e3 = asf(gv[7]);
      const float em = fmaxf(fmaxf(e0, e1), fmaxf(e2, e3));
      const float w0 = __builtin_amdgcn_exp2f(e0 - em), w1v = __builtin_amdgcn_exp2f(e1 - em), w2v = __builtin_amdgcn_exp2f(e2 - em), w3 = __builtin_amdgcn_exp2f(e3 - em);
      *oa = f2bf((w0 * asf(gv[0]) + w1v * asf(gv[1]) + w2v * asf(gv[2]) + w3 * asf(gv[3])) * __builtin_amdgcn_rcpf(w0 + w1v + w2v + w3));
    }
    STAMP(8);
    barrier();
    STAMP(7);
    if (w == 5) {
#pragma unroll
      for (int i = 0; i < 8; ++i) go[i] = (lane & 15) < 10 ? (int)WS_RX + (j * 32 + h) * 2560 + i * 320 + (lane >> 4) * 80 + (lane & 15) * 8 : OOB;
      if (GD > 1) asm volatile("s_waitcnt vmcnt(0)" ::: "memory");
      __builtin_amdgcn_s_sleep(PD2);
      { const unsigned tb = now(); while ((int)(now() - ts) < tend + TG2 && now() - tb < 300u) __builtin_amdgcn_s_sleep(1); }
      gather8<GD2, GG>(gv, go, wsr, tag, ts);
      float s = 0.f;
#pragma unroll
      for (int i = 0; i < 8; ++i) s += asf(gv[i]);
      s = tr16(s, s); s = tr32(s, s);
      const float t = rbf(rbf(dpp<0x105>(s)) + bf2f(b2v));
      const float gate = rbf(t * __builtin_amdgcn_rcpf(1.f + __expf(-t)));
      if (lane < 5) out[oidx] = f2bf(bf2f(xres) + rbf(rbf(s) * gate));
      if (bid == 0 && lane == 0) __hip_atomic_store((GAS unsigned*)(ws + WS_EPOCH), tag, RLX_AGENT);
      STAMP(10);
    }
  } else {
    __builtin_amdgcn_s_setprio(3);
    const int a = w - NS;
    const unsigned epoch = __hip_atomic_load((GAS unsigned*)(ws + WS_EPOCH), RLX_AGENT);
    const unsigned pre = QPRE < 0 ? 0u : __builtin_amdgcn_raw_buffer_load_b32(wsr, lane < 16 ? (int)WS_QX + (bid * 512 + (lane >> 3) * 256 + a * 128) * 8 + (lane & 7) * 128 : (a && lane < 24  && PACE ? (int)WS_CT + ((lane - 16) * 544 + blockIdx.x) * 8 : OOB), 0, QPRE < 0 ? 0 : QPRE);
    const int xb = lane >> 5, xl = lane & 31;
    const __amdgpu_buffer_rsrc_t xr = __builtin_amdgcn_make_buffer_rsrc((void*)x, 0, NB * D * 2, RSRC3);
    const int xo = xb * (D * 2) + xl * 40;
    const v4u x0 = __builtin_amdgcn_raw_buffer_load_b128(xr, xo, 0, 0), x1 = __builtin_amdgcn_raw_buffer_load_b128(xr, xo + 16, 0, 0);
    const v2u x2 = __builtin_amdgcn_raw_buffer_load_b64(xr, xo + 32, 0, 0);
    u16 xs[3];
#pragma unroll
    for (int i = 0; i < 3; ++i) xs[i] = __builtin_amdgcn_raw_buffer_load_b16(xr, xl + 32 * i < 80 ? xb * (D * 2) + (80 * j + xl + 32 * i) * 2 : OOB, 0, 0);
    v4u wa[10], wb[10];
    { const __amdgpu_buffer_rsrc_t ra = __builtin_amdgcn_make_buffer_rsrc((void*)wq, 0, D * H * DH * 2, RSRC3);
      const __amdgpu_buffer_rsrc_t rb = __builtin_amdgcn_make_buffer_rsrc((void*)(a ? w1 : wq), 0, a ? D * F * 2 : D * H * DH * 2, RSRC3);
      const int sa = H * DH * 2, sb = a ? F * 2 : H * DH * 2;
      const int va = r * sa + c * 16, vb = (a && c >= 6) ? OOB : r * sb + c * 16;
      const int oa = 80 * j * sa + h * (DH * 2) + a * 256, ob = 80 * j * sb + (a ? h * 96 : h * (DH * 2) + 128);
#pragma unroll
      for (int u = 0; u < 10; ++u) { wa[u] = __builtin_amdgcn_raw_buffer_load_b128(ra, va, oa + u * 8 * sa, AUX_NT); wb[u] = __builtin_amdgcn_raw_buffer_load_b128(rb, vb, ob + u * 8 * sb, AUX_NT); }
    }
    SCHED();
    STAMP(1);
    tag = (unsigned)__builtin_amdgcn_readfirstlane(epoch) * 0x9E3779B1u + 0x7F4A7C15u;
    tag = tag ? tag : 1u;
    if (a == 0 && lane == 0) ctr[1] = tag;
    {
      float v[20];
#pragma unroll
      for (int k = 0; k < 4; ++k) { v[2 * k] = lo16(x0[k]); v[2 * k + 1] = hi16(x0[k]); v[8 + 2 * k] = lo16(x1[k]); v[9 + 2 * k] = hi16(x1[k]); }
      v[16] = lo16(x2[0]); v[17] = hi16(x2[0]); v[18] = lo16(x2[1]); v[19] = hi16(x2[1]);
      float s = 0.f;
#pragma unroll
      for (int k = 0; k < 20; ++k) s += v[k];
      s += dpp<ROR1>(s); s += dpp<ROR2>(s); s += dpp<ROR4>(s); s += dpp<ROR8>(s); s = tr16(s, s);
      const float mean = s * (1.f / D);
      float q = 0.f;
#pragma unroll
      for (int k = 0; k < 20; ++k) { const float d = v[k] - mean; q = fmaf(d, d, q); }
      q += dpp<ROR1>(q); q += dpp<ROR2>(q); q += dpp<ROR4>(q); q += dpp<ROR8>(q); q = tr16(q, q);
      const float mb = rbf(mean), sd = rbf(sqrtf(rbf(rbf(q * (1.f / D)) + 1e-5f)));
      LAS float* xn = (LAS float*)(L + L_XN) + (a * 2 + xb) * 96 + xl;
#pragma unroll
      for (int i = 0; i < 3; ++i) xn[32 * i] = rbf(rbf(bf2f(xs[i]) - mb) / sd);
    }
    asm volatile("s_waitcnt lgkmcnt(0)" :: "v"(pre) : "memory");
    f32x2 p2[16];
#pragma unroll
    for (int k = 0; k < 16; ++k) p2[k] = f32x2{0.f, 0.f};
    { const LAS float* xn = (const LAS float*)(L + L_XN) + a * 192 + r;
#pragma unroll
      for (int u = 0; u < 10; ++u) { const float xa = xn[8 * u], xc = xn[96 + 8 * u];
#pragma unroll
        for (int k = 0; k < 4; ++k) {
          const f32x2 fa = {lo16(wa[u][k]), hi16(wa[u][k])}, fb = {lo16(wb[u][k]), hi16(wb[u][k])};
          p2[k] = __builtin_elementwise_fma(f32x2{xa, xa}, fa, p2[k]); p2[4 + k] = __builtin_elementwise_fma(f32x2{xa, xa}, fb, p2[4 + k]);
          p2[8 + k] = __builtin_elementwise_fma(f32x2{xc, xc}, fa, p2[8 + k]); p2[12 + k] = __builtin_elementwise_fma(f32x2{xc, xc}, fb, p2[12 + k]);
        } } }
    float pa[32];
#pragma unroll
    for (int k = 0; k < 16; ++k) { pa[2 * k] = p2[k][0]; pa[2 * k + 1] = p2[k][1]; }
#pragma unroll
    for (int k = 0; k < 16; ++k) pa[k] = tr32(pa[k], pa[k + 16]);
#pragma unroll
    for (int k = 0; k < 8; ++k) pa[k] = tr16(pa[k], pa[k + 8]);
#pragma unroll
    for (int k = 0; k < 4; ++k) pa[k] = tr8(pa[k], pa[k + 4], r & 1);
    STAMP(2);
    { gu64* qx = (gu64*)(ws + WS_QX) + bid * 512 + (lane >> 5) * 256 + (2 * a + ((lane >> 4) & 1)) * 64 + (lane & 15);
#pragma unroll
      for (int t = 0; t < 4; ++t) put(qx + t * 16, tag, pa[t]); }
    const int col = (lane & 7) * 8 + ((lane >> 3) & 1) * 4 + (lane >> 4);
    if (a == 0) {
      unsigned gv[24]; int go[24];
#pragma unroll
      for (int i = 0; i < 24; ++i) go[i] = (int)WS_QX + ((h * 8 + i / 3) * 512 + b * 256 + (i % 3) * 64 + lane) * 8;
      while (now() - ts < QD) __builtin_amdgcn_s_sleep(2);
      gather<0>(gv, go, wsr, tag, ts);
#pragma unroll
      for (int u = 0; u < 3; ++u) { float q = 0.f;
#pragma unroll
        for (int p = 0; p < 8; ++p) q += asf(gv[p * 3 + u]);
        *(LAS u16*)(L + L_Q + (u * 64 + col) * 2) = f2bf(q); }
    }
    STAMP(3);
    barrier();
    __builtin_amdgcn_s_setprio(0);
    if (a == 0) {
      const bool live = (lane & 7) < 6;
      const float bias = bf2f(b1[48 * h + (live ? col : 0)]);
      unsigned gv[16]; int go[16];
#pragma unroll
      for (int i = 0; i < 16; ++i) go[i] = live ? (int)WS_QX + ((h * 8) * 512 + i * 256 + 192 + lane) * 8 : OOB;
      gather<16>(gv, go, wsr, tag, ts);
      float hs[2] = {0.f, 0.f};
#pragma unroll
      for (int p = 0; p < 16; ++p) hs[p & 1] += asf(gv[p]);
      if (live) {
#pragma unroll
        for (int u = 0; u < 2; ++u) { float t = rbf(rbf(hs[u]) + bias); t = t > 0.f ? t : 0.f; *(LAS u16*)(L + L_HA + (u * 48 + col) * 2) = f2bf(t * t); } }
#define TOUCH(p) do { unsigned t_; asm volatile("s_load_dword %0, %1, 0x0\n\ts_waitcnt lgkmcnt(0)" : "=s"(t_) : "s"((const void*)(p)) : "memory"); } while (0)
      const size_t kvo = (size_t)(b * H + h) * (SK * DH * 2);
      const int lead = PLEAD + ((blockIdx.x >> 3) & 31);
#pragma unroll 1
      for (int k = 1; k < 12; ++k) {
        while ((int)lds_ld(ctr) < k * 64 / 3 - lead && now() - ts < SPIN_TICKS) __builtin_amdgcn_s_sleep(16);
        const int po = ((rot + k * 64 / 3) & (NU - 1)) * (CONTIG ? 3072 : 12288) + (CONTIG ? sp * 786432 : 0);
        TOUCH((const unsigned char*)kc + kvo + po); TOUCH((const unsigned char*)vc + kvo + po);
        if (k == 8) { TOUCH(wo + DH * h * D); TOUCH(wo + (DH * h + DH - 2) * D); TOUCH(w2 + 48 * h * D); TOUCH(w2 + (48 * h + 46) * D); TOUCH(out); TOUCH(ws + WS_RX + j * 32 * 2560); TOUCH(ws + WS_CT - 64); }
      }
    }
    else {
      float lt = PINIT * 0.1f, l0 = (float)(int)(now() - ts) - (float)(NS * P0 + NS) * lt, up = 0.f, tp = 0.f;
      if (lane == 0) __hip_atomic_store(pace, ((u64)asu(lt) << 32) | asu(l0), RLX_WG);
      for (int rd = 0; rd < 8; ++rd) {
        const unsigned Tk = PT0 + rd * PDT;
        gu64* cl = (gu64*)(ws + WS_CT) + rd * 544;
        while (now() - ts < Tk && lds_ld(ctr) < NU - PSTOP) __builtin_amdgcn_s_sleep(8);
        const unsigned um = lds_ld(ctr);
        if (um >= NU - PSTOP) break;
        if (lane == 0) __hip_atomic_store(cl + blockIdx.x, ((u64)tag << 32) | (ts << 16) | um, RLX_AGENT);
        while (now() - ts < Tk + PLAG) __builtin_amdgcn_s_sleep(8);
        float su = 0.f, st = 0.f; int n = 0;
#pragma unroll
        for (int k = 0; k < (PCL + 63) / 64; ++k) { const u64 v = get(cl + (blockIdx.x & ~(PCL - 1)) + (lane & (PCL - 1)) + 64 * k); const bool ok = (unsigned)(v >> 32) == tag;
          su += ok ? (float)((unsigned)v & 0xffffu) : 0.f; st += ok ? (float)((int)((unsigned)v - (ts << 16)) >> 16) : 0.f; n += __builtin_popcountll(__ballot(ok)); }
        if (n < (PCL < 64 ? 64 : PCL) * 7 / 8) break;
        su += dpp<ROR1>(su); su += dpp<ROR2>(su); su += dpp<ROR4>(su); su += dpp<ROR8>(su); su = tr16(su, su); su = tr32(su, su);
        st += dpp<ROR1>(st); st += dpp<ROR2>(st); st += dpp<ROR4>(st); st += dpp<ROR8>(st); st = tr16(st, st); st = tr32(st, st);
        const float ub = su / (float)n, tm = (float)PDT / (ub - up);
        up = ub;
        if (rd == 0) continue;
        tp = rd == 1 ? tm * (1.f + PEPS * 0.001f) : (tm > tp * (1.f + PTH * 0.001f) ? tm * (1.f + PDN * 0.001f) : tp * (1.f - PUP * 0.001f));
        const float tend = (float)Tk + st / (float)n + ((float)NU - ub) * tp, tnow = (float)(int)(now() - ts);
        const float ti = (tend - tnow) / ((float)NU - fminf((float)(lds_ld(ctr) + PA), (tnow - l0) / lt));
        if (ti > 0.f && ti < 1000.f) { lt = ti; l0 = tend - (float)NU * ti; if (lane == 0) __hip_atomic_store(pace, ((u64)asu(lt) << 32) | asu(l0), RLX_WG); }
      }
    }
    int vw[5], v2[4];
#pragma unroll
    for (int i = 0; i < 5; ++i) { const int id = i * 64 + lane, row = id / 10; vw[i] = row * (D * 2) + (id - row * 10) * 16; }
#pragma unroll
    for (int i = 0; i < 4; ++i) { const int id = (4 * a + i) * 64 + lane, row = id / 10; v2[i] = id < 480 ? row * (D * 2) + (id - row * 10) * 16 : OOB; }
    const __amdgpu_buffer_rsrc_t ro = __builtin_amdgcn_make_buffer_rsrc((void*)wo, 0, H * DH * D * 2, RSRC3), r2 = __builtin_amdgcn_make_buffer_rsrc((void*)w2, 0, F * D * 2, RSRC3);
    const int so = (DH * h + 96 * a) * (D * 2) + 160 * j, s2 = 48 * h * (D * 2) + 160 * j;
    SCHED();
    if (PRETOUCH) {
      const unsigned t = __builtin_amdgcn_raw_buffer_load_b32(wsr, a ? (lane < 20 ? (int)WS_RX + (j * 32 + h) * 2560 + lane * 128 : OOB) : (int)WS_PX + (h * 8 + (lane >> 3)) * 2048 + (lane & 7) * 128, 0, AUX_SC1);
      const unsigned t2 = __builtin_amdgcn_raw_buffer_load_b32(wsr, a || (lane & 7) >= 5 ? OOB : (int)WS_PX + (h * 8 + (lane >> 3)) * 2048 + (8 + (lane & 7)) * 128, 0, AUX_SC1);
      asm volatile("" :: "v"(t), "v"(t2)); }
    while (lds_ld(ctr) < NU - WTRIG && now() - ts < SPIN_TICKS) __builtin_amdgcn_s_sleep(2);
    v4u wr[19];
#pragma unroll
    for (int i = 0; i < 19; ++i) {
      wr[i] = i < 15 ? __builtin_amdgcn_raw_buffer_load_b128(ro, vw[i % 5], so + (i / 5) * 32 * (D * 2), AUX_NT) : __builtin_amdgcn_raw_buffer_load_b128(r2, v2[i < 15 ? 0 : i - 15], s2, AUX_NT);
      SCHED(); asm volatile("s_waitcnt vmcnt(" STR(WPACE) ")" ::: "memory"); }
    SCHED();
    STAMP(5);
    LAS unsigned char* wi = L + L_WO + lane * 16;
#pragma unroll
    for (int i = 0; i < 15; ++i) *(LAS v4u*)(wi + (15 * a + i) * 1024) = wr[i];
#pragma unroll
    for (int i = 0; i < 4; ++i) if (i < 3 || v2[3] != OOB) *(LAS v4u*)(wi + (30 + 4 * a + i) * 1024) = wr[15 + i];
    asm volatile("s_waitcnt lgkmcnt(0)\n\ts_mov_b64 exec, 1\n\tds_add_u32 %0, %1 offset:20\n\ts_mov_b64 exec, -1" :: "v"(L_CT + (unsigned)(uintptr_t)L), "v"(1u) : "memory");
    while (lds_ld(done + 1) < 2 && now() - ts < SPIN_TICKS) {}
    asm volatile("" ::: "memory");
    STAMP(6);
    const LAS unsigned char* base = L + L_WO + (((lane & 15) >> 2) + 8 * (lane >> 4)) * 160 + a * 96 + (lane & 3) * 8;
    const LAS unsigned char* ah = L + L_HA + (lane & 1) * 96;
#define TRB(off) __builtin_amdgcn_ds_read_tr16_b64_v4i16((LAS v4s*)(base + (off)))
#define BFRAG(t, k) __builtin_bit_cast(v8bf, __builtin_shufflevector(TRB((t) * 32 + (k) * 32 * 160), TRB((t) * 32 + (k) * 32 * 160 + 4 * 160), 0, 1, 2, 3, 4, 5, 6, 7))
    const f32x4 Z = {0.f, 0.f, 0.f, 0.f};
    const int gq = lane >> 4, vb = gq & 1, which = gq >> 1;
    v8bf Bf[3][6]; float gsum[3]; gu64* rx[3];
#pragma unroll
    for (int t = 0; t < 3; ++t) {
      const int tt = (t == 2 && a) ? 1 : t;
#pragma unroll
      for (int k = 0; k < 6; ++k) Bf[t][k] = BFRAG(tt, k);
      f32x4 Ga = __builtin_amdgcn_mfma_f32_16x16x32_bf16(*(const LAS v8bf*)(ah + (lane >> 4) * 16), BFRAG(tt, 6), Z, 0, 0, 0);
      f32x4 Gb = __builtin_amdgcn_mfma_f32_16x16x16bf16_1k(*(const LAS v4s*)(ah + 64 + (lane >> 4) * 8), TRB(tt * 32 + 7 * 32 * 160 - 4 * (lane >> 4) * 160), Z, 0, 0, 0);
      asm volatile("" : "+v"(Ga), "+v"(Gb));
      gsum[t] = vb ? Ga[1] + Gb[1] : Ga[0] + Gb[0];
      const int oi = vb * 80 + 16 * (3 * a + tt) + (lane & 15), hp = oi / 5;
      rx[t] = (gu64*)(ws + WS_RX) + ((j * 32 + hp) * 32 + h) * 10 + which * 5 + (oi - hp * 5);
    }
#pragma unroll
    for (int t = 0; t < 3; ++t) asm volatile("" : "+v"(Bf[t][0]), "+v"(Bf[t][1]), "+v"(Bf[t][2]), "+v"(Bf[t][3]), "+v"(Bf[t][4]), "+v"(Bf[t][5]), "+v"(gsum[t]), "+v"(rx[t]));
    barrier();
    STAMP(7);
    const LAS unsigned char* ao = L + L_OA + (lane & 1) * (DH * 2) + (lane >> 4) * 16;
    f32x4 S[3] = {Z, Z, Z};
    v8bf A[6];
#pragma unroll
    for (int k = 0; k < 6; ++k) A[k] = *(const LAS v8bf*)(ao + k * 64);
    SCHED();
#pragma unroll
    for (int k = 0; k < 6; ++k)
#pragma unroll
      for (int t = 0; t < 3; ++t) S[t] = __builtin_amdgcn_mfma_f32_16x16x32_bf16(A[k], Bf[t][k], S[t], 0, 0, 0);
    asm volatile("" : "+v"(S[0]), "+v"(S[1]), "+v"(S[2]));
#pragma unroll
    for (int t = 0; t < 3; ++t) { float s0 = S[t][0], s1 = S[t][1]; asm volatile("" : "+v"(s0), "+v"(s1));
      float v = vb ? s1 : s0; v = which ? gsum[t] : v; asm volatile("" : "+v"(v));
      if (t < 2 || a == 0) put(rx[t], tag, v); }
    STAMP(9);
  }
  STAMP_DUMP();
  if (CODEWARM) asm volatile(".Lfd_end:");
}

extern "C" void kernel_launch(void* const* d_in, const int* in_sizes, int n_in, void* d_out, int out_size, void* d_ws, size_t ws_size, hipStream_t stream) {
  fused_decode<<<256, NTHR, 0, stream>>>((const u16*)d_in[0], (const u16*)d_in[1], (const u16*)d_in[2], (const u16*)d_in[3], (const u16*)d_in[4],
                                         (const u16*)d_in[6], (const u16*)d_in[7], (const u16*)d_in[8], (const u16*)d_in[9], (u16*)d_out, (unsigned char*)d_ws);
}
```

```cpp
#include <hip/hip_runtime.h>
#include <hip/hip_bf16.h>
#include <stdint.h>

#define GAS __attribute__((address_space(1)))
#define LAS __attribute__((address_space(3)))
#define RLX_AGENT __ATOMIC_RELAXED, __HIP_MEMORY_SCOPE_AGENT
#define RLX_WG __ATOMIC_RELAXED, __HIP_MEMORY_SCOPE_WORKGROUP
typedef unsigned v4u __attribute__((ext_vector_type(4)));
typedef unsigned v2u __attribute__((ext_vector_type(2)));
typedef short v4s __attribute__((ext_vector_type(4)));
typedef __bf16 v8bf __attribute__((ext_vector_type(8)));
typedef float f32x4 __attribute__((ext_vector_type(4)));
typedef float f32x2 __attribute__((ext_vector_type(2)));
typedef __bf16 bf16x2 __attribute__((ext_vector_type(2)));
typedef unsigned short u16;
typedef unsigned long long u64;
typedef GAS u64 gu64;

constexpr int NB = 2, D = 640, H = 32, SK = 8192, DH = 192, F = 1536;
constexpr int NS = 6, NTHR = (NS + 2) * 64;
constexpr int NU = 256;
constexpr int P0 = 4;
constexpr int PW = 2;
constexpr int ROT = 5;
constexpr unsigned QD = 440;
constexpr int TG1 = 195;
constexpr int GAP1 = 9;
constexpr int PD2 = 12;
constexpr int WPACE = 7;
constexpr unsigned PT0 = 1300;
constexpr unsigned PDT = 900;
constexpr unsigned PLAG = 130;
constexpr unsigned PSTOP = 40;
constexpr unsigned PA = 2;
constexpr float PINIT = 20.7f;
constexpr float PEPS = 1.008f;
constexpr float PTH = 1.003f;
constexpr float PDN = 1.003f;
constexpr int AUX_NT = 2, AUX_SC1 = 16, RSRC3 = 0x00020000, OOB = 0x7ffffff0;
constexpr unsigned SPIN_TICKS = 2000000u;
constexpr int WS_EPOCH = 0;
constexpr int WS_QX = 4096;
constexpr int WS_PX = WS_QX + 256 * 512 * 8;
constexpr int WS_RX = WS_PX + 256 * 256 * 8;
constexpr int WS_CT = WS_RX + 8 * 32 * 320 * 8;
constexpr int WS_END = WS_CT + 8 * 544 * 8;
constexpr int L_XN = 0;
constexpr int L_Q = L_XN + 4 * 96 * 4;
constexpr int L_HA = L_Q + 384;
constexpr int L_OA = L_HA + 192;
constexpr int L_CT = L_OA + 768;
constexpr int L_MG = L_CT + 32;
constexpr int L_WO = L_MG + NS * 800;
constexpr int L_END = L_WO + 38 * 1024;
static_assert(L_WO % 16 == 0 && L_OA % 16 == 0 && L_HA % 16 == 0 && L_Q % 16 == 0 && L_CT % 16 == 0, "alignment");

__device__ __forceinline__ float asf(unsigned v) { return __builtin_bit_cast(float, v); }
__device__ __forceinline__ unsigned asu(float v) { return __builtin_bit_cast(unsigned, v); }
__device__ __forceinline__ float bf2f(u16 b) { return asf((unsigned)b << 16); }
__device__ __forceinline__ u16 f2bf(float f) { return __builtin_bit_cast(u16, (__bf16)f); }
__device__ __forceinline__ float rbf(float f) { return bf2f(f2bf(f)); }
__device__ __forceinline__ f32x2 unpk(unsigned v) { return f32x2{asf(v << 16), asf(v & 0xffff0000u)}; }
__device__ __forceinline__ float dot2(unsigned k, unsigned q, float acc) {
  return __builtin_amdgcn_fdot2_f32_bf16(__builtin_bit_cast(bf16x2, k), __builtin_bit_cast(bf16x2, q), acc, false);
}
template <int CTRL> __device__ __forceinline__ float dpp(float x) { return asf(__builtin_amdgcn_mov_dpp((int)asu(x), CTRL, 0xf, 0xf, true)); }
constexpr int XOR1 = 0xB1, XOR2 = 0x4E, XOR7 = 0x141, ROR8 = 0x128, ROR4 = 0x124, ROR2 = 0x122, ROR1 = 0x121, SHL5 = 0x105;
__device__ __forceinline__ float tr32(float x, float y) {
  auto s = __builtin_amdgcn_permlane32_swap(asu(x), asu(y), false, false); return asf(s[0]) + asf(s[1]);
}
__device__ __forceinline__ float tr16(float x, float y) {
  auto s = __builtin_amdgcn_permlane16_swap(asu(x), asu(y), false, false); return asf(s[0]) + asf(s[1]);
}
__device__ __forceinline__ float tr8(float x, float y, bool up) {
  const float keep = up ? y : x, send = up ? x : y; return keep + dpp<ROR8>(send);
}
__device__ __forceinline__ float sum16(float s) { s += dpp<ROR1>(s); s += dpp<ROR2>(s); s += dpp<ROR4>(s); return s + dpp<ROR8>(s); }
__device__ __forceinline__ float max16(float x) { auto s = __builtin_amdgcn_permlane16_swap(asu(x), asu(x), false, false); return fmaxf(asf(s[0]), asf(s[1])); }
__device__ __forceinline__ float max32(float x) { auto s = __builtin_amdgcn_permlane32_swap(asu(x), asu(x), false, false); return fmaxf(asf(s[0]), asf(s[1])); }
__device__ __forceinline__ unsigned now() { return (unsigned)__builtin_amdgcn_s_memrealtime(); }
__device__ __forceinline__ void barrier() { asm volatile("s_waitcnt lgkmcnt(0)\n\ts_barrier" ::: "memory"); }
__device__ __forceinline__ void put(gu64* g, unsigned tag, unsigned v) { __hip_atomic_store(g, ((u64)tag << 32) | v, RLX_AGENT); }
__device__ __forceinline__ unsigned lds_ld(LAS unsigned* p) { return __builtin_amdgcn_readfirstlane(__hip_atomic_load(p, RLX_WG)); }
template <bool ORDERED> __device__ __forceinline__ void lds_inc(LAS unsigned* p) {
  if (ORDERED) asm volatile("s_waitcnt lgkmcnt(0)" ::: "memory");
  asm volatile("s_mov_b64 exec, 1\n\tds_add_u32 %0, %1\n\ts_mov_b64 exec, -1" :: "v"((unsigned)(uintptr_t)p), "v"(1u) : "memory");
}
#define SCHED() do { asm volatile("" ::: "memory"); __builtin_amdgcn_sched_barrier(0); } while (0)
#define VMCNT(n) asm volatile("s_waitcnt vmcnt(%0)" :: "n"(n) : "memory")

template <int SLEEP, int N> __device__ __forceinline__ void gather(unsigned (&val)[N], int (&off)[N], __amdgpu_buffer_rsrc_t rs, unsigned tag, unsigned ts) {
#pragma unroll
  for (int i = 0; i < N; ++i) val[i] = 0u;
  for (;;) {
    v2u in[N];
    asm volatile("" ::: "memory");
#pragma unroll
    for (int i = 0; i < N; ++i) in[i] = __builtin_amdgcn_raw_buffer_load_b64(rs, off[i], 0, AUX_SC1);
    int left = OOB;
#pragma unroll
    for (int i = 0; i < N; ++i) { const bool got = in[i][1] == tag; val[i] = got ? in[i][0] : val[i]; off[i] = got ? OOB : off[i]; left = min(left, off[i]); }
    if (__all(left == OOB) || now() - ts > SPIN_TICKS) break;
    if (SLEEP) __builtin_amdgcn_s_sleep(SLEEP);
  }
}
#define G8_LD(a, b, o) "buffer_load_dwordx2 v[" #a ":" #b "], %" #o ", %[rs], 0 offen sc1\n\t"
#define G8_MRG(x0, t0, x1, t1, v0, v1, o0, o1) \
  "v_cmp_eq_u32_e32 vcc, %[tag], v" #t0 "\n\tv_cmp_eq_u32_e64 %[m], %[tag], v" #t1 "\n\ts_nop 0\n\t" \
  "v_cndmask_b32_e32 %" #o0 ", %" #o0 ", %[oob], vcc\n\tv_cndmask_b32_e64 %" #o1 ", %" #o1 ", %[oob], %[m]\n\t" \
  "v_cndmask_b32_e32 %" #v0 ", %" #v0 ", v" #x0 ", vcc\n\tv_cndmask_b32_e64 %" #v1 ", %" #v1 ", v" #x1 ", %[m]\n\t"
#define G8_TEST \
  "v_min3_u32 %[t0], %8, %9, %10\n\tv_min3_u32 %[t1], %11, %12, %13\n\tv_min3_u32 %[t0], %[t0], %14, %15\n\tv_min_u32_e32 %[t0], %[t0], %[t1]\n\t" \
  "v_cmp_ne_u32_e32 vcc, %[t0], %[oob]\n\ts_nop 1\n\ts_cbranch_vccz .Lgdone%=\n\t"
#define G8_AGAIN "s_memrealtime vcc\n\ts_waitcnt lgkmcnt(0)\n\ts_sub_u32 vcc_lo, vcc_lo, %[ts]\n\ts_cmp_lt_u32 vcc_lo, %[lim]\n\ts_cbranch_scc1 .Lgloop%=\n.Lgdone%=:"
#define G8_LOAD_A G8_LD(208, 209, 8) G8_LD(210, 211, 9) G8_LD(212, 213, 10) G8_LD(214, 215, 11) G8_LD(216, 217, 12) G8_LD(218, 219, 13) G8_LD(220, 221, 14) G8_LD(222, 223, 15)
#define G8_LOAD_B G8_LD(224, 225, 8) G8_LD(226, 227, 9) G8_LD(228, 229, 10) G8_LD(230, 231, 11) G8_LD(232, 233, 12) G8_LD(234, 235, 13) G8_LD(236, 237, 14) G8_LD(238, 239, 15)
#define G8_MERGE_A G8_MRG(208, 209, 210, 211, 0, 1, 8, 9) G8_MRG(212, 213, 214, 215, 2, 3, 10, 11) G8_MRG(216, 217, 218, 219, 4, 5, 12, 13) G8_MRG(220, 221, 222, 223, 6, 7, 14, 15) G8_TEST
#define G8_MERGE_B G8_MRG(224, 225, 226, 227, 0, 1, 8, 9) G8_MRG(228, 229, 230, 231, 2, 3, 10, 11) G8_MRG(232, 233, 234, 235, 4, 5, 12, 13) G8_MRG(236, 237, 238, 239, 6, 7, 14, 15) G8_TEST
#define G8_OPERANDS \
  : "+v"(val[0]), "+v"(val[1]), "+v"(val[2]), "+v"(val[3]), "+v"(val[4]), "+v"(val[5]), "+v"(val[6]), "+v"(val[7]), \
    "+v"(off[0]), "+v"(off[1]), "+v"(off[2]), "+v"(off[3]), "+v"(off[4]), "+v"(off[5]), "+v"(off[6]), "+v"(off[7]), \
    [t0] "=&v"(t0), [t1] "=&v"(t1), [m] "=&s"(m) \
  : [rs] "s"(rs), [tag] "s"(tag), [oob] "v"(oob), [ts] "s"(ts), [lim] "s"(SPIN_TICKS), [gap] "n"(GAP) \
  : "memory", "vcc", "scc", "v208", "v209", "v210", "v211", "v212", "v213", "v214", "v215", "v216", "v217", "v218", "v219", "v220", "v221", "v222", "v223", \
    "v224", "v225", "v226", "v227", "v228", "v229", "v230", "v231", "v232", "v233", "v234", "v235", "v236", "v237", "v238", "v239"
template <int GAP> __device__ __forceinline__ void gather8(unsigned (&val)[8], int (&off)[8], __amdgpu_buffer_rsrc_t rs, unsigned tag, unsigned ts) {
#pragma unroll
  for (int i = 0; i < 8; ++i) val[i] = 0u;
  unsigned t0, t1; u64 m; const int oob = OOB;
  if constexpr (GAP == 0)
    asm volatile(G8_LOAD_A ".Lgloop%=:\n\ts_waitcnt vmcnt(0)\n\t" G8_MERGE_A G8_LOAD_A G8_AGAIN G8_OPERANDS);
  else
    asm volatile(G8_LOAD_A "s_sleep %[gap]\n\t" G8_LOAD_B ".Lgloop%=:\n\ts_waitcnt vmcnt(8)\n\t" G8_MERGE_A G8_LOAD_A "s_waitcnt vmcnt(8)\n\t" G8_MERGE_B G8_LOAD_B
                 G8_AGAIN G8_OPERANDS);
}

__global__ void __launch_bounds__(NTHR, 2)
fused_decode(const u16* x, const u16* kc, const u16* vc, const u16* wq, const u16* wo,
             const u16* w1, const u16* b1, const u16* w2, const u16* b2, u16* out, unsigned char* ws) {
  __shared__ __attribute__((aligned(16))) unsigned char lds_raw[L_END];
  LAS unsigned char* L = (LAS unsigned char*)lds_raw;
  const int bid = (blockIdx.x & 7) * 32 + (blockIdx.x >> 3), h = bid >> 3, j = bid & 7, b = j >> 2, sp = j & 3;
  const int tid = threadIdx.x, lane = tid & 63, w = __builtin_amdgcn_readfirstlane(tid >> 6), r = lane >> 3, c = lane & 7;
  LAS unsigned* ctr = (LAS unsigned*)(L + L_CT);
  LAS u64* pace = (LAS u64*)(L + L_CT + 8);
  LAS unsigned* done = (LAS unsigned*)(L + L_CT + 16);
  const unsigned ts = now();
  const int rot = (b * H + h) * ROT;
  const __amdgpu_buffer_rsrc_t wsr = __builtin_amdgcn_make_buffer_rsrc((void*)ws, 0, WS_END, RSRC3);

  if (w < NS) {
    if (tid == 0) { *ctr = NS * P0; *pace = 0ull; done[0] = 0u; done[1] = 0u; }
    const __amdgpu_buffer_rsrc_t kr = __builtin_amdgcn_make_buffer_rsrc((void*)kc, 0, NB * H * SK * DH * 2, RSRC3);
    const __amdgpu_buffer_rsrc_t vr = __builtin_amdgcn_make_buffer_rsrc((void*)vc, 0, NB * H * SK * DH * 2, RSRC3);
    const int rbase = ((b * H + h) * SK + sp * 8) * (DH * 2), voff = r * (DH * 2) + c * 16;
#define LD(dst, rs, t) { const int so_ = rbase + (((t) + rot) & (NU - 1)) * (32 * DH * 2); \
    _Pragma("unroll") for (int u = 0; u < 3; ++u) dst[u] = __builtin_amdgcn_raw_buffer_load_b128(rs, voff + u * 128, so_, AUX_NT); SCHED(); }
    v4u Kb[P0][3], Vb[P0][3];
#pragma unroll
    for (int i = 0; i < P0; ++i) {
      LD(Kb[i], kr, w + i * NS) VMCNT(PW); LD(Vb[i], vr, w + i * NS) if (i + 1 < P0) VMCNT(PW); }
    barrier();
    const int oi = 5 * h + (lane < 5 ? lane : 0), ocol = 80 * j + oi % 80, oidx = (oi / 80) * D + ocol;
    u16 xres = 0, b2v = 0;
    if (w == 5) { xres = x[oidx]; b2v = b2[ocol]; }
    const unsigned tag = lds_ld(ctr + 1);
    v4u qv[3];
#pragma unroll
    for (int u = 0; u < 3; ++u) qv[u] = *(LAS v4u*)(L + L_Q + u * 128 + c * 16);
    constexpr float C2 = 0.07216878364870322f * 1.4426950408889634f;
    float m = -3.0e38f, l = 0.f, s; f32x2 acc[12];
#pragma unroll
    for (int k = 0; k < 12; ++k) acc[k] = f32x2{0.f, 0.f};
#define QK(Kx) { float d0 = 0.f, d1 = 0.f; \
      _Pragma("unroll") for (int u = 0; u < 3; ++u) { d0 = dot2(Kx[u][0], qv[u][0], d0); d1 = dot2(Kx[u][1], qv[u][1], d1); d0 = dot2(Kx[u][2], qv[u][2], d0); d1 = dot2(Kx[u][3], qv[u][3], d1); } \
      s = d0 + d1; asm volatile("" : "+v"(s)); SCHED(); }
#define PV(Vx) { s += dpp<XOR1>(s); s += dpp<XOR2>(s); s += dpp<XOR7>(s); s *= C2; \
      const float mn = fmaxf(m, s), al = __builtin_amdgcn_exp2f(m - mn), p = __builtin_amdgcn_exp2f(s - mn); m = mn; l = fmaf(l, al, p); \
      _Pragma("unroll") for (int u = 0; u < 3; ++u) { SCHED(); \
        _Pragma("unroll") for (int k = 0; k < 4; ++k) acc[u * 4 + k] = __builtin_elementwise_fma(f32x2{p, p}, unpk(Vx[u][k]), acc[u * 4 + k] * al); \
        asm volatile("" : "+v"(acc[u * 4]), "+v"(acc[u * 4 + 1]), "+v"(acc[u * 4 + 2]), "+v"(acc[u * 4 + 3])); } SCHED(); }
#define REFILL() { LD(Kb[0], kr, nxt) PV(Vb[0]) LD(Vb[0], vr, nxt) lds_inc<false>(ctr); nxt += NS; }
    int nxt = w + NS * P0;
    static_assert(NS * (P0 + P0 / 2 + 1) <= NU, "the first refills load real units");
    QK(Kb[0]) REFILL()
#pragma unroll
    for (int i = 1; i < P0; ++i) { QK(Kb[i]) PV(Vb[i]) if (i % 2 == 0) { QK(Kb[0]) REFILL() } }
    while (nxt < NU) {
      QK(Kb[0])
#pragma unroll 1
      for (int spn = 0; spn < 256; ++spn) {
        const u64 pv = __hip_atomic_load(pace, RLX_WG);
        const float t0 = asf(__builtin_amdgcn_readfirstlane((unsigned)pv)), tau = asf(__builtin_amdgcn_readfirstlane((unsigned)(pv >> 32)));
        if (fmaf((float)nxt, tau, t0) <= (float)(int)(now() - ts)) break;
        __builtin_amdgcn_s_sleep(2);
      }
      SCHED();
      REFILL()
    }
    QK(Kb[0]) PV(Vb[0])
    {
      float M = fmaxf(m, dpp<ROR8>(m)); M = max16(M); M = max32(M);
      const float sc = __builtin_amdgcn_exp2f(m - M);
      l *= sc; l += dpp<ROR8>(l); l = tr16(l, l); l = tr32(l, l);
      float o[24];
#pragma unroll
      for (int k = 0; k < 12; ++k) { const f32x2 t = acc[k] * sc; o[2 * k] = t[0]; o[2 * k + 1] = t[1]; }
#pragma unroll
      for (int k = 0; k < 12; ++k) o[k] = tr32(o[k], o[k + 12]);
#pragma unroll
      for (int k = 0; k < 6; ++k) o[k] = tr16(o[k], o[k + 6]);
      LAS float* mg = (LAS float*)(L + L_MG) + w * 200;
#pragma unroll
      for (int k = 0; k < 3; ++k) mg[k * 64 + lane] = tr8(o[k], o[k + 3], r & 1);
      if (lane == 0) { mg[192] = M; mg[193] = l; }
    }
    lds_inc<true>(done);
    for (int spn = 0; lds_ld(done) < NS && spn < (1 << 22); ++spn) {}
    asm volatile("" ::: "memory");
    if (w < 3) {
      const LAS float* mg = (const LAS float*)(L + L_MG);
      float M = mg[192];
#pragma unroll
      for (int i = 1; i < NS; ++i) M = fmaxf(M, mg[i * 200 + 192]);
      float ls = 0.f, o = 0.f;
#pragma unroll
      for (int i = 0; i < NS; ++i) { const float e = __builtin_amdgcn_exp2f(mg[i * 200 + 192] - M); ls = fmaf(e, mg[i * 200 + 193], ls); o = fmaf(e, mg[i * 200 + tid], o); }
      gu64* px = (gu64*)(ws + WS_PX) + bid * 256;
      put(px + tid, tag, asu(o * __builtin_amdgcn_rcpf(ls)));
      if (tid == 0) put(px + 192, tag, asu(M + __builtin_amdgcn_logf(ls)));
    } else __builtin_amdgcn_s_sleep(6);
    static_assert(NS == 6, "six streaming waves");
    const int bb = w >= 3, di = (lane >> 5) * 12 + ((lane >> 4) & 1) * 6 + ((lane >> 3) & 1) * 3 + (w - 3 * bb);
    unsigned gv[8]; int go[8];
#pragma unroll
    for (int i = 0; i < 8; ++i) go[i] = WS_PX + (h * 8 + bb * 4 + (i & 3)) * 2048 + (i < 4 ? tid - bb * 192 : 192) * 8;
    {
      const u64 pv = __hip_atomic_load(pace, RLX_WG);
      const int tend = (int)fmaf((float)NU, asf(__builtin_amdgcn_readfirstlane((unsigned)(pv >> 32))), asf(__builtin_amdgcn_readfirstlane((unsigned)pv)));
      __builtin_amdgcn_s_sleep(6);
      const unsigned tb = now();
      while ((int)(now() - ts) < tend + TG1 && now() - tb < 300u) __builtin_amdgcn_s_sleep(1);
    }
    gather8<GAP1>(gv, go, wsr, tag, ts);
    { const float e0 = asf(gv[4]), e1 = asf(gv[5]), e2 = asf(gv[6]), e3 = asf(gv[7]), em = fmaxf(fmaxf(e0, e1), fmaxf(e2, e3));
      const float p0 = __builtin_amdgcn_exp2f(e0 - em), p1 = __builtin_amdgcn_exp2f(e1 - em), p2 = __builtin_amdgcn_exp2f(e2 - em), p3 = __builtin_amdgcn_exp2f(e3 - em);
      *(LAS u16*)(L + L_OA + (bb * DH + (di >> 3) * 64 + c * 8 + (di & 7)) * 2) =
          f2bf((p0 * asf(gv[0]) + p1 * asf(gv[1]) + p2 * asf(gv[2]) + p3 * asf(gv[3])) * __builtin_amdgcn_rcpf(p0 + p1 + p2 + p3));
    }
    barrier();
    if (w == 5) {
#pragma unroll
      for (int i = 0; i < 8; ++i) go[i] = (lane & 15) < 10 ? WS_RX + (j * 32 + h) * 2560 + i * 320 + (lane >> 4) * 80 + (lane & 15) * 8 : OOB;
      __builtin_amdgcn_s_sleep(PD2);
      gather8<0>(gv, go, wsr, tag, ts);
      float t = 0.f;
#pragma unroll
      for (int i = 0; i < 8; ++i) t += asf(gv[i]);
      t = tr16(t, t); t = tr32(t, t);
      const float g = rbf(rbf(dpp<SHL5>(t)) + bf2f(b2v));
      const float gate = rbf(g * __builtin_amdgcn_rcpf(1.f + __expf(-g)));
      if (lane < 5) out[oidx] = f2bf(bf2f(xres) + rbf(rbf(t) * gate));
      if (blockIdx.x == 0 && lane == 0) __hip_atomic_store((GAS unsigned*)(ws + WS_EPOCH), tag, RLX_AGENT);
    }
  } else {
    __builtin_amdgcn_s_setprio(3);
    const int a = w - NS;
    const unsigned epoch = __hip_atomic_load((GAS unsigned*)(ws + WS_EPOCH), RLX_AGENT);
    const int xb = lane >> 5, xl = lane & 31, xo = xb * (D * 2) + xl * 40;
    const __amdgpu_buffer_rsrc_t xr = __builtin_amdgcn_make_buffer_rsrc((void*)x, 0, NB * D * 2, RSRC3);
    const v4u x0 = __builtin_amdgcn_raw_buffer_load_b128(xr, xo, 0, 0), x1 = __builtin_amdgcn_raw_buffer_load_b128(xr, xo + 16, 0, 0);
    const v2u x2 = __builtin_amdgcn_raw_buffer_load_b64(xr, xo + 32, 0, 0);
    u16 xs[3];
#pragma unroll
    for (int i = 0; i < 3; ++i) xs[i] = __builtin_amdgcn_raw_buffer_load_b16(xr, xl + 32 * i < 80 ? xb * (D * 2) + (80 * j + xl + 32 * i) * 2 : OOB, 0, 0);
    v4u wa[10], wb[10];
    { const __amdgpu_buffer_rsrc_t ra = __builtin_amdgcn_make_buffer_rsrc((void*)wq, 0, D * H * DH * 2, RSRC3);
      const __amdgpu_buffer_rsrc_t rb = __builtin_amdgcn_make_buffer_rsrc((void*)(a ? w1 : wq), 0, a ? D * F * 2 : D * H * DH * 2, RSRC3);
      const int sa = H * DH * 2, sb = a ? F * 2 : H * DH * 2;
      const int va = r * sa + c * 16, vb = (a && c >= 6) ? OOB : r * sb + c * 16;
      const int oa = 80 * j * sa + h * (DH * 2) + a * 256, ob = 80 * j * sb + (a ? h * 96 : h * (DH * 2) + 128);
#pragma unroll
      for (int u = 0; u < 10; ++u) { wa[u] = __builtin_amdgcn_raw_buffer_load_b128(ra, va, oa + u * 8 * sa, AUX_NT); wb[u] = __builtin_amdgcn_raw_buffer_load_b128(rb, vb, ob + u * 8 * sb, AUX_NT); }
    }
    SCHED();
    unsigned tag = (unsigned)__builtin_amdgcn_readfirstlane(epoch) * 0x9E3779B1u + 0x7F4A7C15u;
    tag = tag ? tag : 1u;
    if (a == 0 && lane == 0) ctr[1] = tag;
    {
      f32x2 v[10];
#pragma unroll
      for (int k = 0; k < 4; ++k) { v[k] = unpk(x0[k]); v[4 + k] = unpk(x1[k]); }
      v[8] = unpk(x2[0]); v[9] = unpk(x2[1]);
      float s = 0.f, q = 0.f;
#pragma unroll
      for (int k = 0; k < 10; ++k) s += v[k][0] + v[k][1];
      s = sum16(s); s = tr16(s, s);
      const float mean = s * (1.f / D);
#pragma unroll
      for (int k = 0; k < 10; ++k) { const f32x2 d = v[k] - mean; q = fmaf(d[0], d[0], fmaf(d[1], d[1], q)); }
      q = sum16(q); q = tr16(q, q);
      const float mb = rbf(mean), sd = rbf(sqrtf(rbf(rbf(q * (1.f / D)) + 1e-5f)));
      LAS float* xn = (LAS float*)(L + L_XN) + (a * 2 + xb) * 96 + xl;
#pragma unroll
      for (int i = 0; i < 3; ++i) xn[32 * i] = rbf(rbf(bf2f(xs[i]) - mb) / sd);
    }
    asm volatile("s_waitcnt lgkmcnt(0)" ::: "memory");
    { f32x2 p2[16];
#pragma unroll
      for (int k = 0; k < 16; ++k) p2[k] = f32x2{0.f, 0.f};
      const LAS float* xn = (const LAS float*)(L + L_XN) + a * 192 + r;
#pragma unroll
      for (int u = 0; u < 10; ++u) { const float xa = xn[8 * u], xc = xn[96 + 8 * u];
#pragma unroll
        for (int k = 0; k < 4; ++k) {
          const f32x2 fa = unpk(wa[u][k]), fb = unpk(wb[u][k]);
          p2[k] = __builtin_elementwise_fma(f32x2{xa, xa}, fa, p2[k]); p2[4 + k] = __builtin_elementwise_fma(f32x2{xa, xa}, fb, p2[4 + k]);
          p2[8 + k] = __builtin_elementwise_fma(f32x2{xc, xc}, fa, p2[8 + k]); p2[12 + k] = __builtin_elementwise_fma(f32x2{xc, xc}, fb, p2[12 + k]);
        } }
      float pa[32];
#pragma unroll
      for (int k = 0; k < 16; ++k) { pa[2 * k] = p2[k][0]; pa[2 * k + 1] = p2[k][1]; }
#pragma unroll
      for (int k = 0; k < 16; ++k) pa[k] = tr32(pa[k], pa[k + 16]);
#pragma unroll
      for (int k = 0; k < 8; ++k) pa[k] = tr16(pa[k], pa[k + 8]);
      gu64* qx = (gu64*)(ws + WS_QX) + bid * 512 + (lane >> 5) * 256 + (2 * a + ((lane >> 4) & 1)) * 64 + (lane & 15);
#pragma unroll
      for (int t = 0; t < 4; ++t) put(qx + t * 16, tag, asu(tr8(pa[t], pa[t + 4], r & 1)));
    }
    const int col = (lane & 7) * 8 + ((lane >> 3) & 1) * 4 + (lane >> 4);
    if (a == 0) {
      unsigned gv[24]; int go[24];
#pragma unroll
      for (int i = 0; i < 24; ++i) go[i] = WS_QX + ((h * 8 + i / 3) * 512 + b * 256 + (i % 3) * 64 + lane) * 8;
      while (now() - ts < QD) __builtin_amdgcn_s_sleep(2);
      gather<0>(gv, go, wsr, tag, ts);
#pragma unroll
      for (int u = 0; u < 3; ++u) { float q = 0.f;
#pragma unroll
        for (int p = 0; p < 8; ++p) q += asf(gv[p * 3 + u]);
        *(LAS u16*)(L + L_Q + (u * 64 + col) * 2) = f2bf(q); }
    }
    barrier();
    __builtin_amdgcn_s_setprio(0);
    if (a == 0) {
      const bool live = (lane & 7) < 6;
      const float bias = bf2f(b1[48 * h + (live ? col : 0)]);
      unsigned gv[16]; int go[16];
#pragma unroll
      for (int i = 0; i < 16; ++i) go[i] = live ? WS_QX + ((h * 8) * 512 + i * 256 + 192 + lane) * 8 : OOB;
      gather<16>(gv, go, wsr, tag, ts);
      float hs[2] = {0.f, 0.f};
#pragma unroll
      for (int p = 0; p < 16; ++p) hs[p & 1] += asf(gv[p]);
      if (live) {
#pragma unroll
        for (int u = 0; u < 2; ++u) { float t = rbf(rbf(hs[u]) + bias); t = t > 0.f ? t : 0.f; *(LAS u16*)(L + L_HA + (u * 48 + col) * 2) = f2bf(t * t); } }
#define TOUCH(p) do { unsigned t_; asm volatile("s_load_dword %0, %1, 0x0\n\ts_waitcnt lgkmcnt(0)" : "=s"(t_) : "s"((const void*)(p)) : "memory"); } while (0)
      const size_t kvo = (size_t)(b * H + h) * (SK * DH * 2);
      const int lead = 30 + ((blockIdx.x >> 3) & 31);
#pragma unroll 1
      for (int k = 1; k < 12; ++k) {
        while ((int)lds_ld(ctr) < k * 64 / 3 - lead && now() - ts < SPIN_TICKS) __builtin_amdgcn_s_sleep(16);
        const int po = ((rot + k * 64 / 3) & (NU - 1)) * (32 * DH * 2);
        TOUCH((const unsigned char*)kc + kvo + po); TOUCH((const unsigned char*)vc + kvo + po);
        if (k == 8) { TOUCH(wo + DH * h * D); TOUCH(wo + (DH * h + DH - 2) * D); TOUCH(w2 + 48 * h * D); TOUCH(w2 + (48 * h + 46) * D); }
      }
    } else {
      float lt = PINIT, l0 = (float)(int)(now() - ts) - (float)(NS * P0 + NS) * lt, up = 0.f, tp = 0.f;
      if (lane == 0) __hip_atomic_store(pace, ((u64)asu(lt) << 32) | asu(l0), RLX_WG);
      for (int rd = 0; rd < 8; ++rd) {
        const unsigned Tk = PT0 + rd * PDT;
        gu64* cl = (gu64*)(ws + WS_CT) + rd * 544;
        while (now() - ts < Tk && lds_ld(ctr) < NU - PSTOP) __builtin_amdgcn_s_sleep(8);
        const unsigned um = lds_ld(ctr);
        if (um >= NU - PSTOP) break;
        if (lane == 0) put(cl + blockIdx.x, tag, (ts << 16) | um);
        while (now() - ts < Tk + PLAG) __builtin_amdgcn_s_sleep(8);
        float su = 0.f, st = 0.f; int n = 0;
#pragma unroll
        for (int k = 0; k < 4; ++k) { const u64 v = __hip_atomic_load(cl + lane + 64 * k, RLX_AGENT); const bool ok = (unsigned)(v >> 32) == tag;
          su += ok ? (float)((unsigned)v & 0xffffu) : 0.f; st += ok ? (float)((int)((unsigned)v - (ts << 16)) >> 16) : 0.f; n += __builtin_popcountll(__ballot(ok)); }
        if (n < 224) break;
        su = sum16(su); su = tr16(su, su); su = tr32(su, su); st = sum16(st); st = tr16(st, st); st = tr32(st, st);
        const float ub = su / (float)n, tm = (float)PDT / (ub - up);
        up = ub;
        if (rd == 0) continue;
        tp = rd == 1 ? tm * PEPS : (tm > tp * PTH ? tm * PDN : tp);
        const float tend = (float)Tk + st / (float)n + ((float)NU - ub) * tp, tnow = (float)(int)(now() - ts);
        const float ti = (tend - tnow) / ((float)NU - fminf((float)(lds_ld(ctr) + PA), (tnow - l0) / lt));
        if (ti > 0.f && ti < 1000.f) { lt = ti; l0 = tend - (float)NU * ti; if (lane == 0) __hip_atomic_store(pace, ((u64)asu(lt) << 32) | asu(l0), RLX_WG); }
      }
    }
    int vw[5], v2[4];
#pragma unroll
    for (int i = 0; i < 5; ++i) { const int id = i * 64 + lane, row = id / 10; vw[i] = row * (D * 2) + (id - row * 10) * 16; }
#pragma unroll
    for (int i = 0; i < 4; ++i) { const int id = (4 * a + i) * 64 + lane, row = id / 10; v2[i] = id < 480 ? row * (D * 2) + (id - row * 10) * 16 : OOB; }
    const __amdgpu_buffer_rsrc_t ro = __builtin_amdgcn_make_buffer_rsrc((void*)wo, 0, H * DH * D * 2, RSRC3), r2 = __builtin_amdgcn_make_buffer_rsrc((void*)w2, 0, F * D * 2, RSRC3);
    const int so = (DH * h + 96 * a) * (D * 2) + 160 * j, s2 = 48 * h * (D * 2) + 160 * j;
    const int vb = (lane >> 4) & 1, which = lane >> 5;
    gu64* rx[3];
#pragma unroll
    for (int t = 0; t < 3; ++t) {
      const int oi = vb * 80 + 16 * (3 * a + ((t == 2 && a) ? 1 : t)) + (lane & 15), hp = oi / 5;
      rx[t] = (gu64*)(ws + WS_RX) + ((j * 32 + hp) * 32 + h) * 10 + which * 5 + (oi - hp * 5);
    }
    asm volatile("" : "+v"(vw[0]), "+v"(vw[1]), "+v"(vw[2]), "+v"(vw[3]), "+v"(vw[4]), "+v"(v2[0]), "+v"(v2[1]), "+v"(v2[2]), "+v"(v2[3]), "+v"(rx[0]), "+v"(rx[1]), "+v"(rx[2]));
    while (lds_ld(ctr) < NU && now() - ts < SPIN_TICKS) __builtin_amdgcn_s_sleep(2);
#pragma unroll
    for (int i = 0; i < 19; ++i) {
      if (i < 4) __builtin_amdgcn_raw_ptr_buffer_load_lds(r2, (LAS void*)(L + L_WO + (30 + 4 * a + i) * 1024), 16, v2[i < 4 ? i : 0], s2, 0, AUX_NT);
      else __builtin_amdgcn_raw_ptr_buffer_load_lds(ro, (LAS void*)(L + L_WO + (15 * a + i - 4) * 1024), 16, vw[(i + 1) % 5], so + ((i - 4) / 5) * 32 * (D * 2), 0, AUX_NT);
      SCHED(); VMCNT(WPACE); }
    VMCNT(0);
    lds_inc<true>(done + 1);
    while (lds_ld(done + 1) < 2 && now() - ts < SPIN_TICKS) {}
    asm volatile("" ::: "memory");
    const LAS unsigned char* base = L + L_WO + (((lane & 15) >> 2) + 8 * (lane >> 4)) * 160 + a * 96 + (lane & 3) * 8;
    const LAS unsigned char* ah = L + L_HA + (lane & 1) * 96;
#define TRB(off) __builtin_amdgcn_ds_read_tr16_b64_v4i16((LAS v4s*)(base + (off)))
#define BFRAG(t, k) __builtin_bit_cast(v8bf, __builtin_shufflevector(TRB((t) * 32 + (k) * 32 * 160), TRB((t) * 32 + (k) * 32 * 160 + 4 * 160), 0, 1, 2, 3, 4, 5, 6, 7))
    const f32x4 Z = {0.f, 0.f, 0.f, 0.f};
    v8bf Bf[3][6]; float gsum[3];
#pragma unroll
    for (int t = 0; t < 3; ++t) {
      const int tt = (t == 2 && a) ? 1 : t;
#pragma unroll
      for (int k = 0; k < 6; ++k) Bf[t][k] = BFRAG(tt, k);
      f32x4 Ga = __builtin_amdgcn_mfma_f32_16x16x32_bf16(*(const LAS v8bf*)(ah + (lane >> 4) * 16), BFRAG(tt, 6), Z, 0, 0, 0);
      f32x4 Gb = __builtin_amdgcn_mfma_f32_16x16x16bf16_1k(*(const LAS v4s*)(ah + 64 + (lane >> 4) * 8), TRB(tt * 32 + 7 * 32 * 160 - 4 * (lane >> 4) * 160), Z, 0, 0, 0);
      asm volatile("" : "+v"(Ga), "+v"(Gb));
      gsum[t] = vb ? Ga[1] + Gb[1] : Ga[0] + Gb[0];
    }
#pragma unroll
    for (int t = 0; t < 3; ++t) asm volatile("" : "+v"(Bf[t][0]), "+v"(Bf[t][1]), "+v"(Bf[t][2]), "+v"(Bf[t][3]), "+v"(Bf[t][4]), "+v"(Bf[t][5]), "+v"(gsum[t]));
    barrier();
    const LAS unsigned char* ao = L + L_OA + (lane & 1) * (DH * 2) + (lane >> 4) * 16;
    v8bf A[6];
#pragma unroll
    for (int k = 0; k < 6; ++k) A[k] = *(const LAS v8bf*)(ao + k * 64);
    SCHED();
    f32x4 S[3] = {Z, Z, Z};
#pragma unroll
    for (int k = 0; k < 6; ++k)
#pragma unroll
      for (int t = 0; t < 3; ++t) S[t] = __builtin_amdgcn_mfma_f32_16x16x32_bf16(A[k], Bf[t][k], S[t], 0, 0, 0);
    asm volatile("" : "+v"(S[0]), "+v"(S[1]), "+v"(S[2]));
#pragma unroll
    for (int t = 0; t < 3; ++t) { float s0 = S[t][0], s1 = S[t][1]; asm volatile("" : "+v"(s0), "+v"(s1));
      float v = vb ? s1 : s0; v = which ? gsum[t] : v; asm volatile("" : "+v"(v));
      if (t < 2 || a == 0) put(rx[t], tag, asu(v)); }
  }
}

extern "C" void kernel_launch(void* const* d_in, const int* in_sizes, int n_in, void* d_out, int out_size, void* d_ws, size_t ws_size, hipStream_t stream) {
  fused_decode<<<256, NTHR, 0, stream>>>((const u16*)d_in[0], (const u16*)d_in[1], (const u16*)d_in[2], (const u16*)d_in[3], (const u16*)d_in[4],
                                         (const u16*)d_in[6], (const u16*)d_in[7], (const u16*)d_in[8], (const u16*)d_in[9], (u16*)d_out, (unsigned char*)d_ws);
}
```

```cpp
#include <hip/hip_runtime.h>
#include <hip/hip_bf16.h>
#include <stdint.h>

#define GAS __attribute__((address_space(1)))
#define LAS __attribute__((address_space(3)))
#define RLX_AGENT __ATOMIC_RELAXED, __HIP_MEMORY_SCOPE_AGENT
#define RLX_WG __ATOMIC_RELAXED, __HIP_MEMORY_SCOPE_WORKGROUP
typedef unsigned v4u __attribute__((ext_vector_type(4)));
typedef unsigned v2u __attribute__((ext_vector_type(2)));
typedef short v4s __attribute__((ext_vector_type(4)));
typedef __bf16 v8bf __attribute__((ext_vector_type(8)));
typedef float f32x4 __attribute__((ext_vector_type(4)));
typedef float f32x2 __attribute__((ext_vector_type(2)));
typedef __bf16 bf16x2 __attribute__((ext_vector_type(2)));
typedef unsigned short u16;
typedef unsigned long long u64;
typedef GAS u64 gu64;

constexpr int NB = 2, D = 640, H = 32, SK = 8192, DH = 192, F = 1536;
constexpr int NS = 6, NTHR = (NS + 2) * 64;
constexpr int NU = 256;
constexpr int P0 = 4;
constexpr int PW = 2;
constexpr int ROT = 4;
constexpr unsigned QD = 480;
constexpr unsigned QX = 60;
constexpr int TG1 = 195;
constexpr int GAP1 = 9;
constexpr int PD2 = 12;
constexpr int WPACE = 7;
constexpr unsigned PT0 = 1300;
constexpr unsigned PDT = 900;
constexpr unsigned PLAG = 180;
constexpr unsigned PSTOP = 40;
constexpr unsigned PA = 2;
constexpr float PINIT = 20.7f;
constexpr float PEPS = 1.01f;
constexpr float PTH = 1.003f;
constexpr float PMX = 1.012f;
constexpr unsigned PH = 150;
constexpr int AUX_NT = 2, AUX_SC1 = 16, RSRC3 = 0x00020000, OOB = 0x7ffffff0;
constexpr unsigned SPIN_TICKS = 2000000u;
constexpr int WS_EPOCH = 0;
constexpr int WS_QX = 4096;
constexpr int WS_PX = WS_QX + 256 * 512 * 8;
constexpr int WS_RX = WS_PX + 256 * 256 * 8;
constexpr int WS_CT = WS_RX + 8 * 32 * 320 * 8;
constexpr int WS_END = WS_CT + 8 * 544 * 8;
constexpr int L_XN = 0;
constexpr int L_Q = L_XN + 4 * 96 * 4;
constexpr int L_HA = L_Q + 384;
constexpr int L_OA = L_HA + 192;
constexpr int L_CT = L_OA + 768;
constexpr int L_MG = L_CT + 32;
constexpr int L_WO = L_MG + NS * 800;
constexpr int L_END = L_WO + 38 * 1024;
static_assert(L_WO % 16 == 0 && L_OA % 16 == 0 && L_HA % 16 == 0 && L_Q % 16 == 0 && L_CT % 16 == 0, "alignment");

__device__ __forceinline__ float asf(unsigned v) { return __builtin_bit_cast(float, v); }
__device__ __forceinline__ unsigned asu(float v) { return __builtin_bit_cast(unsigned, v); }
__device__ __forceinline__ float bf2f(u16 b) { return asf((unsigned)b << 16); }
__device__ __forceinline__ u16 f2bf(float f) { return __builtin_bit_cast(u16, (__bf16)f); }
__device__ __forceinline__ float rbf(float f) { return bf2f(f2bf(f)); }
__device__ __forceinline__ f32x2 unpk(unsigned v) { return f32x2{asf(v << 16), asf(v & 0xffff0000u)}; }
__device__ __forceinline__ float dot2(unsigned k, unsigned q, float acc) {
  return __builtin_amdgcn_fdot2_f32_bf16(__builtin_bit_cast(bf16x2, k), __builtin_bit_cast(bf16x2, q), acc, false);
}
template <int CTRL> __device__ __forceinline__ float dpp(float x) { return asf(__builtin_amdgcn_mov_dpp((int)asu(x), CTRL, 0xf, 0xf, true)); }
constexpr int XOR1 = 0xB1, XOR2 = 0x4E, XOR7 = 0x141, ROR8 = 0x128, ROR4 = 0x124, ROR2 = 0x122, ROR1 = 0x121, SHL5 = 0x105;
__device__ __forceinline__ float tr32(float x, float y) {
  auto s = __builtin_amdgcn_permlane32_swap(asu(x), asu(y), false, false); return asf(s[0]) + asf(s[1]);
}
__device__ __forceinline__ float tr16(float x, float y) {
  auto s = __builtin_amdgcn_permlane16_swap(asu(x), asu(y), false, false); return asf(s[0]) + asf(s[1]);
}
__device__ __forceinline__ float tr8(float x, float y, bool up) {
  const float keep = up ? y : x, send = up ? x : y; return keep + dpp<ROR8>(send);
}
__device__ __forceinline__ float sum16(float s) { s += dpp<ROR1>(s); s += dpp<ROR2>(s); s += dpp<ROR4>(s); return s + dpp<ROR8>(s); }
__device__ __forceinline__ float max16(float x) { auto s = __builtin_amdgcn_permlane16_swap(asu(x), asu(x), false, false); return fmaxf(asf(s[0]), asf(s[1])); }
__device__ __forceinline__ float max32(float x) { auto s = __builtin_amdgcn_permlane32_swap(asu(x), asu(x), false, false); return fmaxf(asf(s[0]), asf(s[1])); }
__device__ __forceinline__ unsigned now() { return (unsigned)__builtin_amdgcn_s_memrealtime(); }
__device__ __forceinline__ void barrier() { asm volatile("s_waitcnt lgkmcnt(0)\n\ts_barrier" ::: "memory"); }
__device__ __forceinline__ void put(gu64* g, unsigned tag, unsigned v) { __hip_atomic_store(g, ((u64)tag << 32) | v, RLX_AGENT); }
__device__ __forceinline__ unsigned lds_ld(LAS unsigned* p) { return __builtin_amdgcn_readfirstlane(__hip_atomic_load(p, RLX_WG)); }
template <bool ORDERED> __device__ __forceinline__ void lds_inc(LAS unsigned* p) {
  if (ORDERED) asm volatile("s_waitcnt lgkmcnt(0)" ::: "memory");
  asm volatile("s_mov_b64 exec, 1\n\tds_add_u32 %0, %1\n\ts_mov_b64 exec, -1" :: "v"((unsigned)(uintptr_t)p), "v"(1u) : "memory");
}
#define SCHED() do { asm volatile("" ::: "memory"); __builtin_amdgcn_sched_barrier(0); } while (0)
#define VMCNT(n) asm volatile("s_waitcnt vmcnt(%0)" :: "n"(n) : "memory")

template <int SLEEP, int N> __device__ __forceinline__ void gather(unsigned (&val)[N], int (&off)[N], __amdgpu_buffer_rsrc_t rs, unsigned tag, unsigned ts) {
#pragma unroll
  for (int i = 0; i < N; ++i) val[i] = 0u;
  for (;;) {
    v2u in[N];
    asm volatile("" ::: "memory");
#pragma unroll
    for (int i = 0; i < N; ++i) in[i] = __builtin_amdgcn_raw_buffer_load_b64(rs, off[i], 0, AUX_SC1);
    int left = OOB;
#pragma unroll
    for (int i = 0; i < N; ++i) { const bool got = in[i][1] == tag; val[i] = got ? in[i][0] : val[i]; off[i] = got ? OOB : off[i]; left = min(left, off[i]); }
    if (__all(left == OOB) || now() - ts > SPIN_TICKS) break;
    if (SLEEP) __builtin_amdgcn_s_sleep(SLEEP);
  }
}
#define G8_LD(a, b, o) "buffer_load_dwordx2 v[" #a ":" #b "], %" #o ", %[rs], 0 offen sc1\n\t"
#define G8_MRG(x0, t0, x1, t1, v0, v1, o0, o1) \
  "v_cmp_eq_u32_e32 vcc, %[tag], v" #t0 "\n\tv_cmp_eq_u32_e64 %[m], %[tag], v" #t1 "\n\ts_nop 0\n\t" \
  "v_cndmask_b32_e32 %" #o0 ", %" #o0 ", %[oob], vcc\n\tv_cndmask_b32_e64 %" #o1 ", %" #o1 ", %[oob], %[m]\n\t" \
  "v_cndmask_b32_e32 %" #v0 ", %" #v0 ", v" #x0 ", vcc\n\tv_cndmask_b32_e64 %" #v1 ", %" #v1 ", v" #x1 ", %[m]\n\t"
#define G8_TEST \
  "v_min3_u32 %[t0], %8, %9, %10\n\tv_min3_u32 %[t1], %11, %12, %13\n\tv_min3_u32 %[t0], %[t0], %14, %15\n\tv_min_u32_e32 %[t0], %[t0], %[t1]\n\t" \
  "v_cmp_ne_u32_e32 vcc, %[t0], %[oob]\n\ts_nop 1\n\ts_cbranch_vccz .Lgdone%=\n\t"
#define G8_AGAIN "s_memrealtime vcc\n\ts_waitcnt lgkmcnt(0)\n\ts_sub_u32 vcc_lo, vcc_lo, %[ts]\n\ts_cmp_lt_u32 vcc_lo, %[lim]\n\ts_cbranch_scc1 .Lgloop%=\n.Lgdone%=:"
#define G8_LOAD_A G8_LD(208, 209, 8) G8_LD(210, 211, 9) G8_LD(212, 213, 10) G8_LD(214, 215, 11) G8_LD(216, 217, 12) G8_LD(218, 219, 13) G8_LD(220, 221, 14) G8_LD(222, 223, 15)
#define G8_LOAD_B G8_LD(224, 225, 8) G8_LD(226, 227, 9) G8_LD(228, 229, 10) G8_LD(230, 231, 11) G8_LD(232, 233, 12) G8_LD(234, 235, 13) G8_LD(236, 237, 14) G8_LD(238, 239, 15)
#define G8_MERGE_A G8_MRG(208, 209, 210, 211, 0, 1, 8, 9) G8_MRG(212, 213, 214, 215, 2, 3, 10, 11) G8_MRG(216, 217, 218, 219, 4, 5, 12, 13) G8_MRG(220, 221, 222, 223, 6, 7, 14, 15) G8_TEST
#define G8_MERGE_B G8_MRG(224, 225, 226, 227, 0, 1, 8, 9) G8_MRG(228, 229, 230, 231, 2, 3, 10, 11) G8_MRG(232, 233, 234, 235, 4, 5, 12, 13) G8_MRG(236, 237, 238, 239, 6, 7, 14, 15) G8_TEST
#define G8_OPERANDS \
  : "+v"(val[0]), "+v"(val[1]), "+v"(val[2]), "+v"(val[3]), "+v"(val[4]), "+v"(val[5]), "+v"(val[6]), "+v"(val[7]), \
    "+v"(off[0]), "+v"(off[1]), "+v"(off[2]), "+v"(off[3]), "+v"(off[4]), "+v"(off[5]), "+v"(off[6]), "+v"(off[7]), \
    [t0] "=&v"(t0), [t1] "=&v"(t1), [m] "=&s"(m) \
  : [rs] "s"(rs), [tag] "s"(tag), [oob] "v"(oob), [ts] "s"(ts), [lim] "s"(SPIN_TICKS), [gap] "n"(GAP) \
  : "memory", "vcc", "scc", "v208", "v209", "v210", "v211", "v212", "v213", "v214", "v215", "v216", "v217", "v218", "v219", "v220", "v221", "v222", "v223", \
    "v224", "v225", "v226", "v227", "v228", "v229", "v230", "v231", "v232", "v233", "v234", "v235", "v236", "v237", "v238", "v239"
template <int GAP> __device__ __forceinline__ void gather8(unsigned (&val)[8], int (&off)[8], __amdgpu_buffer_rsrc_t rs, unsigned tag, unsigned ts) {
#pragma unroll
  for (int i = 0; i < 8; ++i) val[i] = 0u;
  unsigned t0, t1; u64 m; const int oob = OOB;
  if constexpr (GAP == 0)
    asm volatile(G8_LOAD_A ".Lgloop%=:\n\ts_waitcnt vmcnt(0)\n\t" G8_MERGE_A G8_LOAD_A G8_AGAIN G8_OPERANDS);
  else
    asm volatile(G8_LOAD_A "s_sleep %[gap]\n\t" G8_LOAD_B ".Lgloop%=:\n\ts_waitcnt vmcnt(8)\n\t" G8_MERGE_A G8_LOAD_A "s_waitcnt vmcnt(8)\n\t" G8_MERGE_B G8_LOAD_B
                 G8_AGAIN G8_OPERANDS);
}

__global__ void __launch_bounds__(NTHR, 2)
fused_decode(const u16* x, const u16* kc, const u16* vc, const u16* wq, const u16* wo,
             const u16* w1, const u16* b1, const u16* w2, const u16* b2, u16* out, unsigned char* ws) {
  __shared__ __attribute__((aligned(16))) unsigned char lds_raw[L_END];
  LAS unsigned char* L = (LAS unsigned char*)lds_raw;
  const int bid = (blockIdx.x & 7) * 32 + (blockIdx.x >> 3), h = bid >> 3, j = bid & 7, b = j >> 2, sp = j & 3;
  const int tid = threadIdx.x, lane = tid & 63, w = __builtin_amdgcn_readfirstlane(tid >> 6), r = lane >> 3, c = lane & 7;
  LAS unsigned* ctr = (LAS unsigned*)(L + L_CT);
  LAS u64* pace = (LAS u64*)(L + L_CT + 8);
  LAS unsigned* done = (LAS unsigned*)(L + L_CT + 16);
  const unsigned ts = now();
  const int rot = (b * H + h) * ROT;
  const __amdgpu_buffer_rsrc_t wsr = __builtin_amdgcn_make_buffer_rsrc((void*)ws, 0, WS_END, RSRC3);

  if (w < NS) {
    if (tid == 0) { *ctr = NS * P0; *pace = 0ull; done[0] = 0u; done[1] = 0u; }
    const __amdgpu_buffer_rsrc_t kr = __builtin_amdgcn_make_buffer_rsrc((void*)kc, 0, NB * H * SK * DH * 2, RSRC3);
    const __amdgpu_buffer_rsrc_t vr = __builtin_amdgcn_make_buffer_rsrc((void*)vc, 0, NB * H * SK * DH * 2, RSRC3);
    const int rbase = ((b * H + h) * SK + sp * 8) * (DH * 2), voff = r * (DH * 2) + c * 16;
#define LD(dst, rs, t) { const int so_ = rbase + (((t) + rot) & (NU - 1)) * (32 * DH * 2); \
    _Pragma("unroll") for (int u = 0; u < 3; ++u) dst[u] = __builtin_amdgcn_raw_buffer_load_b128(rs, voff + u * 128, so_, AUX_NT); SCHED(); }
    const int oi = 5 * h + (lane < 5 ? lane : 0), ocol = 80 * j + oi % 80, oidx = (oi / 80) * D + ocol;
    v4u Kb[P0][3], Vb[P0][3];
#pragma unroll
    for (int i = 0; i < P0; ++i) {
      LD(Kb[i], kr, w + i * NS) VMCNT(PW); LD(Vb[i], vr, w + i * NS) if (i + 1 < P0) VMCNT(PW); }
    unsigned xres = __builtin_amdgcn_raw_buffer_load_b16(__builtin_amdgcn_make_buffer_rsrc((void*)x, 0, NB * D * 2, RSRC3), w == 5 ? oidx * 2 : OOB, 0, 0);
    unsigned b2v = __builtin_amdgcn_raw_buffer_load_b16(__builtin_amdgcn_make_buffer_rsrc((void*)b2, 0, D * 2, RSRC3), w == 5 ? ocol * 2 : OOB, 0, 0);
    barrier();
    const unsigned tag = lds_ld(ctr + 1);
    v4u qv[3];
#pragma unroll
    for (int u = 0; u < 3; ++u) qv[u] = *(LAS v4u*)(L + L_Q + u * 128 + c * 16);
    constexpr float C2 = 0.07216878364870322f * 1.4426950408889634f;
    float m = -3.0e38f, l = 0.f, s; f32x2 acc[12];
#pragma unroll
    for (int k = 0; k < 12; ++k) acc[k] = f32x2{0.f, 0.f};
#define QK(Kx) { float d0 = 0.f, d1 = 0.f; \
      _Pragma("unroll") for (int u = 0; u < 3; ++u) { d0 = dot2(Kx[u][0], qv[u][0], d0); d1 = dot2(Kx[u][1], qv[u][1], d1); d0 = dot2(Kx[u][2], qv[u][2], d0); d1 = dot2(Kx[u][3], qv[u][3], d1); } \
      s = d0 + d1; asm volatile("" : "+v"(s)); SCHED(); }
#define PV(Vx) { s += dpp<XOR1>(s); s += dpp<XOR2>(s); s += dpp<XOR7>(s); s *= C2; \
      const float mn = fmaxf(m, s), al = __builtin_amdgcn_exp2f(m - mn), p = __builtin_amdgcn_exp2f(s - mn); m = mn; l = fmaf(l, al, p); \
      _Pragma("unroll") for (int u = 0; u < 3; ++u) { SCHED(); \
        _Pragma("unroll") for (int k = 0; k < 4; ++k) acc[u * 4 + k] = __builtin_elementwise_fma(f32x2{p, p}, unpk(Vx[u][k]), acc[u * 4 + k] * al); \
        asm volatile("" : "+v"(acc[u * 4]), "+v"(acc[u * 4 + 1]), "+v"(acc[u * 4 + 2]), "+v"(acc[u * 4 + 3])); } SCHED(); }
#define REFILL() { LD(Kb[0], kr, nxt) PV(Vb[0]) LD(Vb[0], vr, nxt) lds_inc<false>(ctr); nxt += NS; }
    int nxt = w + NS * P0;
    static_assert(NS * (P0 + P0 / 2 + 1) <= NU, "the first refills load real units");
    QK(Kb[0]) REFILL()
#pragma unroll
    for (int i = 1; i < P0; ++i) { QK(Kb[i]) PV(Vb[i]) if (i % 2 == 0) { QK(Kb[0]) REFILL() } }
    while (nxt < NU) {
      QK(Kb[0])
#pragma unroll 1
      for (int spn = 0; spn < 256; ++spn) {
        const u64 pv = __hip_atomic_load(pace, RLX_WG);
        const float t0 = asf(__builtin_amdgcn_readfirstlane((unsigned)pv)), tau = asf(__builtin_amdgcn_readfirstlane((unsigned)(pv >> 32)));
        if (fmaf((float)nxt, tau, t0) <= (float)(int)(now() - ts)) break;
        __builtin_amdgcn_s_sleep(2);
      }
      SCHED();
      REFILL()
    }
    QK(Kb[0]) PV(Vb[0])
    {
      float M = fmaxf(m, dpp<ROR8>(m)); M = max16(M); M = max32(M);
      const float sc = __builtin_amdgcn_exp2f(m - M);
      l *= sc; l += dpp<ROR8>(l); l = tr16(l, l); l = tr32(l, l);
      float o[24];
#pragma unroll
      for (int k = 0; k < 12; ++k) { const f32x2 t = acc[k] * sc; o[2 * k] = t[0]; o[2 * k + 1] = t[1]; }
#pragma unroll
      for (int k = 0; k < 12; ++k) o[k] = tr32(o[k], o[k + 12]);
#pragma unroll
      for (int k = 0; k < 6; ++k) o[k] = tr16(o[k], o[k + 6]);
      LAS float* mg = (LAS float*)(L + L_MG) + w * 200;
#pragma unroll
      for (int k = 0; k < 3; ++k) mg[k * 64 + lane] = tr8(o[k], o[k + 3], r & 1);
      if (lane == 0) { mg[192] = M; mg[193] = l; }
    }
    lds_inc<true>(done);
    for (int spn = 0; lds_ld(done) < NS && spn < (1 << 22); ++spn) {}
    asm volatile("" ::: "memory");
    if (w < 3) {
      const LAS float* mg = (const LAS float*)(L + L_MG);
      float M = mg[192];
#pragma unroll
      for (int i = 1; i < NS; ++i) M = fmaxf(M, mg[i * 200 + 192]);
      float ls = 0.f, o = 0.f;
#pragma unroll
      for (int i = 0; i < NS; ++i) { const float e = __builtin_amdgcn_exp2f(mg[i * 200 + 192] - M); ls = fmaf(e, mg[i * 200 + 193], ls); o = fmaf(e, mg[i * 200 + tid], o); }
      gu64* px = (gu64*)(ws + WS_PX) + bid * 256;
      put(px + tid, tag, asu(o * __builtin_amdgcn_rcpf(ls)));
      if (tid == 0) put(px + 192, tag, asu(M + __builtin_amdgcn_logf(ls)));
    } else __builtin_amdgcn_s_sleep(6);
    static_assert(NS == 6, "six streaming waves");
    const int bb = w >= 3, di = (lane >> 5) * 12 + ((lane >> 4) & 1) * 6 + ((lane >> 3) & 1) * 3 + (w - 3 * bb);
    unsigned gv[8]; int go[8];
#pragma unroll
    for (int i = 0; i < 8; ++i) go[i] = WS_PX + (h * 8 + bb * 4 + (i & 3)) * 2048 + (i < 4 ? tid - bb * 192 : 192) * 8;
    {
      const u64 pv = __hip_atomic_load(pace, RLX_WG);
      const int tend = (int)fmaf((float)NU, asf(__builtin_amdgcn_readfirstlane((unsigned)(pv >> 32))), asf(__builtin_amdgcn_readfirstlane((unsigned)pv)));
      __builtin_amdgcn_s_sleep(6);
      const unsigned tb = now();
      while ((int)(now() - ts) < tend + TG1 && now() - tb < 300u) __builtin_amdgcn_s_sleep(1);
    }
    gather8<GAP1>(gv, go, wsr, tag, ts);
    { const float e0 = asf(gv[4]), e1 = asf(gv[5]), e2 = asf(gv[6]), e3 = asf(gv[7]), em = fmaxf(fmaxf(e0, e1), fmaxf(e2, e3));
      const float p0 = __builtin_amdgcn_exp2f(e0 - em), p1 = __builtin_amdgcn_exp2f(e1 - em), p2 = __builtin_amdgcn_exp2f(e2 - em), p3 = __builtin_amdgcn_exp2f(e3 - em);
      *(LAS u16*)(L + L_OA + (bb * DH + (di >> 3) * 64 + c * 8 + (di & 7)) * 2) =
          f2bf((p0 * asf(gv[0]) + p1 * asf(gv[1]) + p2 * asf(gv[2]) + p3 * asf(gv[3])) * __builtin_amdgcn_rcpf(p0 + p1 + p2 + p3));
    }
    barrier();
    if (w == 5) {
#pragma unroll
      for (int i = 0; i < 8; ++i) go[i] = (lane & 15) < 10 ? WS_RX + (j * 32 + h) * 2560 + i * 320 + (lane >> 4) * 80 + (lane & 15) * 8 : OOB;
      __builtin_amdgcn_s_sleep(PD2);
      gather8<0>(gv, go, wsr, tag, ts);
      float t = 0.f;
#pragma unroll
      for (int i = 0; i < 8; ++i) t += asf(gv[i]);
      t = tr16(t, t); t = tr32(t, t);
      asm volatile("" : "+v"(xres), "+v"(b2v));
      const float g = rbf(rbf(dpp<SHL5>(t)) + asf(b2v << 16));
      const float gate = rbf(g * __builtin_amdgcn_rcpf(1.f + __expf(-g)));
      if (lane < 5) out[oidx] = f2bf(asf(xres << 16) + rbf(rbf(t) * gate));
      if (blockIdx.x == 0 && lane == 0) __hip_atomic_store((GAS unsigned*)(ws + WS_EPOCH), tag, RLX_AGENT);
    }
  } else {
    __builtin_amdgcn_s_setprio(3);
    const int a = w - NS;
    const unsigned epoch = __hip_atomic_load((GAS unsigned*)(ws + WS_EPOCH), RLX_AGENT);
    const int xb = lane >> 5, xl = lane & 31, xo = xb * (D * 2) + xl * 40;
    const __amdgpu_buffer_rsrc_t xr = __builtin_amdgcn_make_buffer_rsrc((void*)x, 0, NB * D * 2, RSRC3);
    const v4u x0 = __builtin_amdgcn_raw_buffer_load_b128(xr, xo, 0, 0), x1 = __builtin_amdgcn_raw_buffer_load_b128(xr, xo + 16, 0, 0);
    const v2u x2 = __builtin_amdgcn_raw_buffer_load_b64(xr, xo + 32, 0, 0);
    u16 xs[3];
#pragma unroll
    for (int i = 0; i < 3; ++i) xs[i] = __builtin_amdgcn_raw_buffer_load_b16(xr, xl + 32 * i < 80 ? xb * (D * 2) + (80 * j + xl + 32 * i) * 2 : OOB, 0, 0);
    v4u wa[10], wb[10];
    { const __amdgpu_buffer_rsrc_t ra = __builtin_amdgcn_make_buffer_rsrc((void*)wq, 0, D * H * DH * 2, RSRC3);
      const __amdgpu_buffer_rsrc_t rb = __builtin_amdgcn_make_buffer_rsrc((void*)(a ? w1 : wq), 0, a ? D * F * 2 : D * H * DH * 2, RSRC3);
      const int sa = H * DH * 2, sb = a ? F * 2 : H * DH * 2;
      const int va = r * sa + c * 16, vb = (a && c >= 6) ? OOB : r * sb + c * 16;
      const int oa = 80 * j * sa + h * (DH * 2) + a * 256, ob = 80 * j * sb + (a ? h * 96 : h * (DH * 2) + 128);
#pragma unroll
      for (int u = 0; u < 10; ++u) { wa[u] = __builtin_amdgcn_raw_buffer_load_b128(ra, va, oa + u * 8 * sa, AUX_NT); wb[u] = __builtin_amdgcn_raw_buffer_load_b128(rb, vb, ob + u * 8 * sb, AUX_NT); }
    }
    SCHED();
    unsigned tag = (unsigned)__builtin_amdgcn_readfirstlane(epoch) * 0x9E3779B1u + 0x7F4A7C15u;
    tag = tag ? tag : 1u;
    if (a == 0 && lane == 0) ctr[1] = tag;
    {
      f32x2 v[10];
#pragma unroll
      for (int k = 0; k < 4; ++k) { v[k] = unpk(x0[k]); v[4 + k] = unpk(x1[k]); }
      v[8] = unpk(x2[0]); v[9] = unpk(x2[1]);
      float s = 0.f, q = 0.f;
#pragma unroll
      for (int k = 0; k < 10; ++k) s += v[k][0] + v[k][1];
      s = sum16(s); s = tr16(s, s);
      const float mean = s * (1.f / D);
#pragma unroll
      for (int k = 0; k < 10; ++k) { const f32x2 d = v[k] - mean; q = fmaf(d[0], d[0], fmaf(d[1], d[1], q)); }
      q = sum16(q); q = tr16(q, q);
      const float mb = rbf(mean), sd = rbf(sqrtf(rbf(rbf(q * (1.f / D)) + 1e-5f)));
      LAS float* xn = (LAS float*)(L + L_XN) + (a * 2 + xb) * 96 + xl;
#pragma unroll
      for (int i = 0; i < 3; ++i) xn[32 * i] = rbf(rbf(bf2f(xs[i]) - mb) / sd);
    }
    asm volatile("s_waitcnt lgkmcnt(0)" ::: "memory");
    { f32x2 p2[16];
#pragma unroll
      for (int k = 0; k < 16; ++k) p2[k] = f32x2{0.f, 0.f};
      const LAS float* xn = (const LAS float*)(L + L_XN) + a * 192 + r;
#pragma unroll
      for (int u = 0; u < 10; ++u) { const float xa = xn[8 * u], xc = xn[96 + 8 * u];
#pragma unroll
        for (int k = 0; k < 4; ++k) {
          const f32x2 fa = unpk(wa[u][k]), fb = unpk(wb[u][k]);
          p2[k] = __builtin_elementwise_fma(f32x2{xa, xa}, fa, p2[k]); p2[4 + k] = __builtin_elementwise_fma(f32x2{xa, xa}, fb, p2[4 + k]);
          p2[8 + k] = __builtin_elementwise_fma(f32x2{xc, xc}, fa, p2[8 + k]); p2[12 + k] = __builtin_elementwise_fma(f32x2{xc, xc}, fb, p2[12 + k]);
        } }
      float pa[32];
#pragma unroll
      for (int k = 0; k < 16; ++k) { pa[2 * k] = p2[k][0]; pa[2 * k + 1] = p2[k][1]; }
#pragma unroll
      for (int k = 0; k < 16; ++k) pa[k] = tr32(pa[k], pa[k + 16]);
#pragma unroll
      for (int k = 0; k < 8; ++k) pa[k] = tr16(pa[k], pa[k + 8]);
      gu64* qx = (gu64*)(ws + WS_QX) + bid * 512 + (lane >> 5) * 256 + (2 * a + ((lane >> 4) & 1)) * 64 + (lane & 15);
#pragma unroll
      for (int t = 0; t < 4; ++t) put(qx + t * 16, tag, asu(tr8(pa[t], pa[t + 4], r & 1)));
    }
    const int col = (lane & 7) * 8 + ((lane >> 3) & 1) * 4 + (lane >> 4);
    if (a == 0) {
      unsigned gv[24]; int go[24];
#pragma unroll
      for (int i = 0; i < 24; ++i) go[i] = WS_QX + ((h * 8 + i / 3) * 512 + b * 256 + (i % 3) * 64 + lane) * 8;
      { const unsigned tq = max(QD, now() - ts + QX);
        while (now() - ts < tq) __builtin_amdgcn_s_sleep(2); }
      gather<0>(gv, go, wsr, tag, ts);
#pragma unroll
      for (int u = 0; u < 3; ++u) { float q = 0.f;
#pragma unroll
        for (int p = 0; p < 8; ++p) q += asf(gv[p * 3 + u]);
        *(LAS u16*)(L + L_Q + (u * 64 + col) * 2) = f2bf(q); }
    }
    barrier();
    __builtin_amdgcn_s_setprio(0);
    if (a == 0) {
      while (lds_ld(ctr) < PH && now() - ts < SPIN_TICKS) __builtin_amdgcn_s_sleep(16);
      const bool live = (lane & 7) < 6;
      const float bias = bf2f(b1[48 * h + (live ? col : 0)]);
      unsigned gv[16]; int go[16];
#pragma unroll
      for (int i = 0; i < 16; ++i) go[i] = live ? WS_QX + ((h * 8) * 512 + i * 256 + 192 + lane) * 8 : OOB;
      gather<16>(gv, go, wsr, tag, ts);
      float hs[2] = {0.f, 0.f};
#pragma unroll
      for (int p = 0; p < 16; ++p) hs[p & 1] += asf(gv[p]);
      if (live) {
#pragma unroll
        for (int u = 0; u < 2; ++u) { float t = rbf(rbf(hs[u]) + bias); t = t > 0.f ? t : 0.f; *(LAS u16*)(L + L_HA + (u * 48 + col) * 2) = f2bf(t * t); } }
    } else {
      float lt = PINIT, l0 = (float)(int)(now() - ts) - (float)(NS * P0 + NS) * lt, up = 0.f, tp = 0.f, tb = 0.f;
      unsigned Tp = 0u;
      if (lane == 0) __hip_atomic_store(pace, ((u64)asu(lt) << 32) | asu(l0), RLX_WG);
      for (int rd = 0; rd < 8; ++rd) {
        const unsigned Tk = PT0 + rd * PDT;
        gu64* cl = (gu64*)(ws + WS_CT) + rd * 544;
        while (now() - ts < Tk && lds_ld(ctr) < NU - PSTOP) __builtin_amdgcn_s_sleep(8);
        const unsigned um = lds_ld(ctr);
        if (um >= NU - PSTOP) break;
        if (lane == 0) put(cl + blockIdx.x, tag, (ts << 16) | um);
        while (now() - ts < Tk + PLAG) __builtin_amdgcn_s_sleep(8);
        float su = 0.f, st = 0.f; int n = 0;
#pragma unroll
        for (int k = 0; k < 4; ++k) { const u64 v = __hip_atomic_load(cl + lane + 64 * k, RLX_AGENT); const bool ok = (unsigned)(v >> 32) == tag;
          su += ok ? (float)((unsigned)v & 0xffffu) : 0.f; st += ok ? (float)((int)((unsigned)v - (ts << 16)) >> 16) : 0.f; n += __builtin_popcountll(__ballot(ok)); }
        if (n < 224) continue;
        su = sum16(su); su = tr16(su, su); su = tr32(su, su); st = sum16(st); st = tr16(st, st); st = tr32(st, st);
        const float ub = su / (float)n, tm = (float)(Tk - Tp) / (ub - up);
        const bool first = Tp == 0u;
        up = ub; Tp = Tk;
        if (first) continue;
        if (tb == 0.f) tp = tb = tm * PEPS;
        else if (tm > tp * PTH) tp = fminf(tm * PTH, tp * PMX);
        else tp = fmaxf(tb, tp / PMX);
        const float tend = (float)Tk + st / (float)n + ((float)NU - ub) * tp, tnow = (float)(int)(now() - ts);
        const float ti = (tend - tnow) / ((float)NU - fminf((float)(lds_ld(ctr) + PA), (tnow - l0) / lt));
        if (ti > 0.f && ti < 1000.f) { lt = ti; l0 = tend - (float)NU * ti; if (lane == 0) __hip_atomic_store(pace, ((u64)asu(lt) << 32) | asu(l0), RLX_WG); }
      }
    }
    int vw[5], v2[4];
#pragma unroll
    for (int i = 0; i < 5; ++i) { const int id = i * 64 + lane, row = id / 10; vw[i] = row * (D * 2) + (id - row * 10) * 16; }
#pragma unroll
    for (int i = 0; i < 4; ++i) { const int id = (4 * a + i) * 64 + lane, row = id / 10; v2[i] = id < 480 ? row * (D * 2) + (id - row * 10) * 16 : OOB; }
    const __amdgpu_buffer_rsrc_t ro = __builtin_amdgcn_make_buffer_rsrc((void*)wo, 0, H * DH * D * 2, RSRC3), r2 = __builtin_amdgcn_make_buffer_rsrc((void*)w2, 0, F * D * 2, RSRC3);
    const int so = (DH * h + 96 * a) * (D * 2) + 160 * j, s2 = 48 * h * (D * 2) + 160 * j;
    const int vb = (lane >> 4) & 1, which = lane >> 5;
    gu64* rx[3];
#pragma unroll
    for (int t = 0; t < 3; ++t) {
      const int oi = vb * 80 + 16 * (3 * a + ((t == 2 && a) ? 1 : t)) + (lane & 15), hp = oi / 5;
      rx[t] = (gu64*)(ws + WS_RX) + ((j * 32 + hp) * 32 + h) * 10 + which * 5 + (oi - hp * 5);
    }
    asm volatile("" : "+v"(vw[0]), "+v"(vw[1]), "+v"(vw[2]), "+v"(vw[3]), "+v"(vw[4]), "+v"(v2[0]), "+v"(v2[1]), "+v"(v2[2]), "+v"(v2[3]), "+v"(rx[0]), "+v"(rx[1]), "+v"(rx[2]));
    while (lds_ld(ctr) < NU && now() - ts < SPIN_TICKS) __builtin_amdgcn_s_sleep(2);
#pragma unroll
    for (int i = 0; i < 19; ++i) {
      if (i < 4) __builtin_amdgcn_raw_ptr_buffer_load_lds(r2, (LAS void*)(L + L_WO + (30 + 4 * a + i) * 1024), 16, v2[i < 4 ? i : 0], s2, 0, AUX_NT);
      else __builtin_amdgcn_raw_ptr_buffer_load_lds(ro, (LAS void*)(L + L_WO + (15 * a + i - 4) * 1024), 16, vw[(i + 1) % 5], so + ((i - 4) / 5) * 32 * (D * 2), 0, AUX_NT);
      SCHED(); VMCNT(WPACE); }
    VMCNT(0);
    lds_inc<true>(done + 1);
    while (lds_ld(done + 1) < 2 && now() - ts < SPIN_TICKS) {}
    asm volatile("" ::: "memory");
    const LAS unsigned char* base = L + L_WO + (((lane & 15) >> 2) + 8 * (lane >> 4)) * 160 + a * 96 + (lane & 3) * 8;
    const LAS unsigned char* ah = L + L_HA + (lane & 1) * 96;
#define TRB(off) __builtin_amdgcn_ds_read_tr16_b64_v4i16((LAS v4s*)(base + (off)))
#define BFRAG(t, k) __builtin_bit_cast(v8bf, __builtin_shufflevector(TRB((t) * 32 + (k) * 32 * 160), TRB((t) * 32 + (k) * 32 * 160 + 4 * 160), 0, 1, 2, 3, 4, 5, 6, 7))
    const f32x4 Z = {0.f, 0.f, 0.f, 0.f};
    v8bf Bf[3][6]; float gsum[3];
#pragma unroll
    for (int t = 0; t < 3; ++t) {
      const int tt = (t == 2 && a) ? 1 : t;
#pragma unroll
      for (int k = 0; k < 6; ++k) Bf[t][k] = BFRAG(tt, k);
      f32x4 Ga = __builtin_amdgcn_mfma_f32_16x16x32_bf16(*(const LAS v8bf*)(ah + (lane >> 4) * 16), BFRAG(tt, 6), Z, 0, 0, 0);
      f32x4 Gb = __builtin_amdgcn_mfma_f32_16x16x16bf16_1k(*(const LAS v4s*)(ah + 64 + (lane >> 4) * 8), TRB(tt * 32 + 7 * 32 * 160 - 4 * (lane >> 4) * 160), Z, 0, 0, 0);
      asm volatile("" : "+v"(Ga), "+v"(Gb));
      gsum[t] = vb ? Ga[1] + Gb[1] : Ga[0] + Gb[0];
    }
#pragma unroll
    for (int t = 0; t < 3; ++t) asm volatile("" : "+v"(Bf[t][0]), "+v"(Bf[t][1]), "+v"(Bf[t][2]), "+v"(Bf[t][3]), "+v"(Bf[t][4]), "+v"(Bf[t][5]), "+v"(gsum[t]));
    barrier();
    const LAS unsigned char* ao = L + L_OA + (lane & 1) * (DH * 2) + (lane >> 4) * 16;
    v8bf A[6];
#pragma unroll
    for (int k = 0; k < 6; ++k) A[k] = *(const LAS v8bf*)(ao + k * 64);
    SCHED();
    f32x4 S[3] = {Z, Z, Z};
#pragma unroll
    for (int k = 0; k < 6; ++k)
#pragma unroll
      for (int t = 0; t < 3; ++t) S[t] = __builtin_amdgcn_mfma_f32_16x16x32_bf16(A[k], Bf[t][k], S[t], 0, 0, 0);
    asm volatile("" : "+v"(S[0]), "+v"(S[1]), "+v"(S[2]));
#pragma unroll
    for (int t = 0; t < 3; ++t) { float s0 = S[t][0], s1 = S[t][1]; asm volatile("" : "+v"(s0), "+v"(s1));
      float v = vb ? s1 : s0; v = which ? gsum[t] : v; asm volatile("" : "+v"(v));
      if (t < 2 || a == 0) put(rx[t], tag, asu(v)); }
  }
}

extern "C" void kernel_launch(void* const* d_in, const int* in_sizes, int n_in, void* d_out, int out_size, void* d_ws, size_t ws_size, hipStream_t stream) {
  fused_decode<<<256, NTHR, 0, stream>>>((const u16*)d_in[0], (const u16*)d_in[1], (const u16*)d_in[2], (const u16*)d_in[3], (const u16*)d_in[4],
                                         (const u16*)d_in[6], (const u16*)d_in[7], (const u16*)d_in[8], (const u16*)d_in[9], (u16*)d_out, (unsigned char*)d_ws);
}
```

```cpp
#include <hip/hip_runtime.h>
#include <hip/hip_bf16.h>
#include <stdint.h>

#define GAS __attribute__((address_space(1)))
#define LAS __attribute__((address_space(3)))
#define RLX_AGENT __ATOMIC_RELAXED, __HIP_MEMORY_SCOPE_AGENT
#define RLX_WG __ATOMIC_RELAXED, __HIP_MEMORY_SCOPE_WORKGROUP
typedef unsigned v4u __attribute__((ext_vector_type(4)));
typedef unsigned v2u __attribute__((ext_vector_type(2)));
typedef short v4s __attribute__((ext_vector_type(4)));
typedef __bf16 v8bf __attribute__((ext_vector_type(8)));
typedef float f32x4 __attribute__((ext_vector_type(4)));
typedef float f32x2 __attribute__((ext_vector_type(2)));
typedef __bf16 bf16x2 __attribute__((ext_vector_type(2)));
typedef unsigned short u16;
typedef unsigned long long u64;
typedef GAS u64 gu64;

constexpr int NB = 2, D = 640, H = 32, SK = 8192, DH = 192, F = 1536;
constexpr int NS = 6, NTHR = (NS + 2) * 64;
constexpr int NU = 256;
constexpr int P0 = 4;
constexpr int PW = 2;
constexpr int ROT = 4;
constexpr unsigned QD = 480;
constexpr unsigned QX = 60;
constexpr int TG1 = 195;
constexpr int GAP1 = 9;
constexpr int PD2 = 12;
constexpr int WPACE = 7;
constexpr unsigned PT0 = 1300;
constexpr unsigned PDT = 900;
constexpr unsigned PLAG = 180;
constexpr unsigned PSTOP = 40;
constexpr unsigned PA = 2;
constexpr float PINIT = 20.7f;
constexpr float PEPS = 1.01f;
constexpr float PTH = 1.003f;
constexpr float PMX = 1.012f;
constexpr int PH = 150;
constexpr int PWS = 21;
constexpr int PLEAD = 30;
constexpr int PLSP = 32;
constexpr int PWT = 1;
constexpr int SWARM = 1;
constexpr int CODEW = 1;
constexpr int AUX_NT = 2, AUX_SC1 = 16, RSRC3 = 0x00020000, OOB = 0x7ffffff0;
constexpr unsigned SPIN_TICKS = 2000000u;
constexpr int WS_EPOCH = 0;
constexpr int WS_QX = 4096;
constexpr int WS_PX = WS_QX + 256 * 512 * 8;
constexpr int WS_RX = WS_PX + 256 * 256 * 8;
constexpr int WS_CT = WS_RX + 8 * 32 * 320 * 8;
constexpr int WS_END = WS_CT + 8 * 544 * 8;
constexpr int L_XN = 0;
constexpr int L_Q = L_XN + 4 * 96 * 4;
constexpr int L_HA = L_Q + 384;
constexpr int L_OA = L_HA + 192;
constexpr int L_CT = L_OA + 768;
constexpr int L_MG = L_CT + 32;
constexpr int L_WO = L_MG + NS * 800;
constexpr int L_END = L_WO + 38 * 1024;
static_assert(L_WO % 16 == 0 && L_OA % 16 == 0 && L_HA % 16 == 0 && L_Q % 16 == 0 && L_CT % 16 == 0, "alignment");

__device__ __forceinline__ float asf(unsigned v) { return __builtin_bit_cast(float, v); }
__device__ __forceinline__ unsigned asu(float v) { return __builtin_bit_cast(unsigned, v); }
__device__ __forceinline__ float bf2f(u16 b) { return asf((unsigned)b << 16); }
__device__ __forceinline__ u16 f2bf(float f) { return __builtin_bit_cast(u16, (__bf16)f); }
__device__ __forceinline__ float rbf(float f) { return bf2f(f2bf(f)); }
__device__ __forceinline__ f32x2 unpk(unsigned v) { return f32x2{asf(v << 16), asf(v & 0xffff0000u)}; }
__device__ __forceinline__ float dot2(unsigned k, unsigned q, float acc) {
  return __builtin_amdgcn_fdot2_f32_bf16(__builtin_bit_cast(bf16x2, k), __builtin_bit_cast(bf16x2, q), acc, false);
}
template <int CTRL> __device__ __forceinline__ float dpp(float x) { return asf(__builtin_amdgcn_mov_dpp((int)asu(x), CTRL, 0xf, 0xf, true)); }
constexpr int XOR1 = 0xB1, XOR2 = 0x4E, XOR7 = 0x141, ROR8 = 0x128, ROR4 = 0x124, ROR2 = 0x122, ROR1 = 0x121, SHL5 = 0x105;
__device__ __forceinline__ float tr32(float x, float y) {
  auto s = __builtin_amdgcn_permlane32_swap(asu(x), asu(y), false, false); return asf(s[0]) + asf(s[1]);
}
__device__ __forceinline__ float tr16(float x, float y) {
  auto s = __builtin_amdgcn_permlane16_swap(asu(x), asu(y), false, false); return asf(s[0]) + asf(s[1]);
}
__device__ __forceinline__ float tr8(float x, float y, bool up) {
  const float keep = up ? y : x, send = up ? x : y; return keep + dpp<ROR8>(send);
}
__device__ __forceinline__ float sum16(float s) { s += dpp<ROR1>(s); s += dpp<ROR2>(s); s += dpp<ROR4>(s); return s + dpp<ROR8>(s); }
__device__ __forceinline__ float max16(float x) { auto s = __builtin_amdgcn_permlane16_swap(asu(x), asu(x), false, false); return fmaxf(asf(s[0]), asf(s[1])); }
__device__ __forceinline__ float max32(float x) { auto s = __builtin_amdgcn_permlane32_swap(asu(x), asu(x), false, false); return fmaxf(asf(s[0]), asf(s[1])); }
__device__ __forceinline__ unsigned now() { return (unsigned)__builtin_amdgcn_s_memrealtime(); }
__device__ __forceinline__ void barrier() { asm volatile("s_waitcnt lgkmcnt(0)\n\ts_barrier" ::: "memory"); }
__device__ __forceinline__ void put(gu64* g, unsigned tag, unsigned v) { __hip_atomic_store(g, ((u64)tag << 32) | v, RLX_AGENT); }
__device__ __forceinline__ unsigned lds_ld(LAS unsigned* p) { return __builtin_amdgcn_readfirstlane(__hip_atomic_load(p, RLX_WG)); }
template <bool ORDERED> __device__ __forceinline__ void lds_inc(LAS unsigned* p) {
  if (ORDERED) asm volatile("s_waitcnt lgkmcnt(0)" ::: "memory");
  asm volatile("s_mov_b64 exec, 1\n\tds_add_u32 %0, %1\n\ts_mov_b64 exec, -1" :: "v"((unsigned)(uintptr_t)p), "v"(1u) : "memory");
}
__device__ __forceinline__ void touch(const void* p, const void* q) {
  unsigned t0, t1; asm volatile("s_load_dword %0, %2, 0x0\n\ts_load_dword %1, %3, 0x0\n\ts_waitcnt lgkmcnt(0)" : "=&s"(t0), "=&s"(t1) : "s"(p), "s"(q) : "memory");
}
#define SCHED() do { asm volatile("" ::: "memory"); __builtin_amdgcn_sched_barrier(0); } while (0)
#define VMCNT(n) asm volatile("s_waitcnt vmcnt(%0)" :: "n"(n) : "memory")

template <int SLEEP, int N> __device__ __forceinline__ void gather(unsigned (&val)[N], int (&off)[N], __amdgpu_buffer_rsrc_t rs, unsigned tag, unsigned ts) {
#pragma unroll
  for (int i = 0; i < N; ++i) val[i] = 0u;
  for (;;) {
    v2u in[N];
    asm volatile("" ::: "memory");
#pragma unroll
    for (int i = 0; i < N; ++i) in[i] = __builtin_amdgcn_raw_buffer_load_b64(rs, off[i], 0, AUX_SC1);
    int left = OOB;
#pragma unroll
    for (int i = 0; i < N; ++i) { const bool got = in[i][1] == tag; val[i] = got ? in[i][0] : val[i]; off[i] = got ? OOB : off[i]; left = min(left, off[i]); }
    if (__all(left == OOB) || now() - ts > SPIN_TICKS) break;
    if (SLEEP) __builtin_amdgcn_s_sleep(SLEEP);
  }
}
#define G8_LD(a, b, o) "buffer_load_dwordx2 v[" #a ":" #b "], %" #o ", %[rs], 0 offen sc1\n\t"
#define G8_MRG(x0, t0, x1, t1, v0, v1, o0, o1) \
  "v_cmp_eq_u32_e32 vcc, %[tag], v" #t0 "\n\tv_cmp_eq_u32_e64 %[m], %[tag], v" #t1 "\n\ts_nop 0\n\t" \
  "v_cndmask_b32_e32 %" #o0 ", %" #o0 ", %[oob], vcc\n\tv_cndmask_b32_e64 %" #o1 ", %" #o1 ", %[oob], %[m]\n\t" \
  "v_cndmask_b32_e32 %" #v0 ", %" #v0 ", v" #x0 ", vcc\n\tv_cndmask_b32_e64 %" #v1 ", %" #v1 ", v" #x1 ", %[m]\n\t"
#define G8_TEST \
  "v_min3_u32 %[t0], %8, %9, %10\n\tv_min3_u32 %[t1], %11, %12, %13\n\tv_min3_u32 %[t0], %[t0], %14, %15\n\tv_min_u32_e32 %[t0], %[t0], %[t1]\n\t" \
  "v_cmp_ne_u32_e32 vcc, %[t0], %[oob]\n\ts_nop 1\n\ts_cbranch_vccz .Lgdone%=\n\t"
#define G8_AGAIN "s_memrealtime vcc\n\ts_waitcnt lgkmcnt(0)\n\ts_sub_u32 vcc_lo, vcc_lo, %[ts]\n\ts_cmp_lt_u32 vcc_lo, %[lim]\n\ts_cbranch_scc1 .Lgloop%=\n.Lgdone%=:"
#define G8_LOAD_A G8_LD(208, 209, 8) G8_LD(210, 211, 9) G8_LD(212, 213, 10) G8_LD(214, 215, 11) G8_LD(216, 217, 12) G8_LD(218, 219, 13) G8_LD(220, 221, 14) G8_LD(222, 223, 15)
#define G8_LOAD_B G8_LD(224, 225, 8) G8_LD(226, 227, 9) G8_LD(228, 229, 10) G8_LD(230, 231, 11) G8_LD(232, 233, 12) G8_LD(234, 235, 13) G8_LD(236, 237, 14) G8_LD(238, 239, 15)
#define G8_MERGE_A G8_MRG(208, 209, 210, 211, 0, 1, 8, 9) G8_MRG(212, 213, 214, 215, 2, 3, 10, 11) G8_MRG(216, 217, 218, 219, 4, 5, 12, 13) G8_MRG(220, 221, 222, 223, 6, 7, 14, 15) G8_TEST
#define G8_MERGE_B G8_MRG(224, 225, 226, 227, 0, 1, 8, 9) G8_MRG(228, 229, 230, 231, 2, 3, 10, 11) G8_MRG(232, 233, 234, 235, 4, 5, 12, 13) G8_MRG(236, 237, 238, 239, 6, 7, 14, 15) G8_TEST
#define G8_OPERANDS \
  : "+v"(val[0]), "+v"(val[1]), "+v"(val[2]), "+v"(val[3]), "+v"(val[4]), "+v"(val[5]), "+v"(val[6]), "+v"(val[7]), \
    "+v"(off[0]), "+v"(off[1]), "+v"(off[2]), "+v"(off[3]), "+v"(off[4]), "+v"(off[5]), "+v"(off[6]), "+v"(off[7]), \
    [t0] "=&v"(t0), [t1] "=&v"(t1), [m] "=&s"(m) \
  : [rs] "s"(rs), [tag] "s"(tag), [oob] "v"(oob), [ts] "s"(ts), [lim] "s"(SPIN_TICKS), [gap] "n"(GAP) \
  : "memory", "vcc", "scc", "v208", "v209", "v210", "v211", "v212", "v213", "v214", "v215", "v216", "v217", "v218", "v219", "v220", "v221", "v222", "v223", \
    "v224", "v225", "v226", "v227", "v228", "v229", "v230", "v231", "v232", "v233", "v234", "v235", "v236", "v237", "v238", "v239"
template <int GAP> __device__ __forceinline__ void gather8(unsigned (&val)[8], int (&off)[8], __amdgpu_buffer_rsrc_t rs, unsigned tag, unsigned ts) {
#pragma unroll
  for (int i = 0; i < 8; ++i) val[i] = 0u;
  unsigned t0, t1; u64 m; const int oob = OOB;
  if constexpr (GAP == 0)
    asm volatile(G8_LOAD_A ".Lgloop%=:\n\ts_waitcnt vmcnt(0)\n\t" G8_MERGE_A G8_LOAD_A G8_AGAIN G8_OPERANDS);
  else
    asm volatile(G8_LOAD_A "s_sleep %[gap]\n\t" G8_LOAD_B ".Lgloop%=:\n\ts_waitcnt vmcnt(8)\n\t" G8_MERGE_A G8_LOAD_A "s_waitcnt vmcnt(8)\n\t" G8_MERGE_B G8_LOAD_B
                 G8_AGAIN G8_OPERANDS);
}

__global__ void __launch_bounds__(NTHR, 2)
fused_decode(const u16* x, const u16* kc, const u16* vc, const u16* wq, const u16* wo,
             const u16* w1, const u16* b1, const u16* w2, const u16* b2, u16* out, unsigned char* ws) {
  __shared__ __attribute__((aligned(16))) unsigned char lds_raw[L_END];
  LAS unsigned char* L = (LAS unsigned char*)lds_raw;
  const int bid = (blockIdx.x & 7) * 32 + (blockIdx.x >> 3), h = bid >> 3, j = bid & 7, b = j >> 2, sp = j & 3;
  const int tid = threadIdx.x, lane = tid & 63, w = __builtin_amdgcn_readfirstlane(tid >> 6), r = lane >> 3, c = lane & 7;
  unsigned warm = 0;
  if (CODEW && w == 0) {
    u64 pc; unsigned len;
    asm volatile("s_getpc_b64 %0\n.Lfd_here:\n\ts_mov_b32 %1, .Lfd_end-.Lfd_here" : "=s"(pc), "=s"(len));
    warm = *(const GAS unsigned*)((pc & ~127ull) + (min((unsigned)(((blockIdx.x >> 3) % 3) * 8192 + lane * 128), len - 4u) & ~3u));
  }
  SCHED();
  LAS unsigned* ctr = (LAS unsigned*)(L + L_CT);
  LAS u64* pace = (LAS u64*)(L + L_CT + 8);
  LAS unsigned* done = (LAS unsigned*)(L + L_CT + 16);
  const unsigned ts = now();
  const int rot = (b * H + h) * ROT;
  const __amdgpu_buffer_rsrc_t wsr = __builtin_amdgcn_make_buffer_rsrc((void*)ws, 0, WS_END, RSRC3);

  if (w < NS) {
    if (tid == 0) { *ctr = NS * P0; *pace = 0ull; done[0] = 0u; done[1] = 0u; }
    const __amdgpu_buffer_rsrc_t kr = __builtin_amdgcn_make_buffer_rsrc((void*)kc, 0, NB * H * SK * DH * 2, RSRC3);
    const __amdgpu_buffer_rsrc_t vr = __builtin_amdgcn_make_buffer_rsrc((void*)vc, 0, NB * H * SK * DH * 2, RSRC3);
    const int rbase = ((b * H + h) * SK + sp * 8) * (DH * 2), voff = r * (DH * 2) + c * 16;
#define LD(dst, rs, t) { const int so_ = rbase + (((t) + rot) & (NU - 1)) * (32 * DH * 2); \
    _Pragma("unroll") for (int u = 0; u < 3; ++u) dst[u] = __builtin_amdgcn_raw_buffer_load_b128(rs, voff + u * 128, so_, AUX_NT); SCHED(); }
    const int oi = 5 * h + (lane < 5 ? lane : 0), ocol = 80 * j + oi % 80, oidx = (oi / 80) * D + ocol;
    v4u Kb[P0][3], Vb[P0][3];
#pragma unroll
    for (int i = 0; i < P0; ++i) {
      LD(Kb[i], kr, w + i * NS)
      if (SWARM && i == 0) {
        if (w == 5) touch((const unsigned char*)vc + rbase + (rot & (NU - 1)) * (32 * DH * 2), ws + 2048);
        if (w == 4) touch(wq + (size_t)80 * j * H * DH + h * DH, wq + (size_t)(80 * j + 79) * H * DH + h * DH);
        if (w == 3) touch(w1 + 80 * j * F + h * 48, w1 + (80 * j + 79) * F + h * 48);
        if (w == 2) touch(x, b2);
        if (w == 1) touch(ws + WS_END, ws + WS_END);
      }
      VMCNT(PW); LD(Vb[i], vr, w + i * NS) if (i + 1 < P0) VMCNT(PW); }
    unsigned xres = __builtin_amdgcn_raw_buffer_load_b16(__builtin_amdgcn_make_buffer_rsrc((void*)x, 0, NB * D * 2, RSRC3), w == 5 ? oidx * 2 : OOB, 0, 0);
    unsigned b2v = __builtin_amdgcn_raw_buffer_load_b16(__builtin_amdgcn_make_buffer_rsrc((void*)b2, 0, D * 2, RSRC3), w == 5 ? ocol * 2 : OOB, 0, 0);
    barrier();
    asm volatile("" :: "v"(warm));
    const unsigned tag = lds_ld(ctr + 1);
    v4u qv[3];
#pragma unroll
    for (int u = 0; u < 3; ++u) qv[u] = *(LAS v4u*)(L + L_Q + u * 128 + c * 16);
    constexpr float C2 = 0.07216878364870322f * 1.4426950408889634f;
    float m = -3.0e38f, l = 0.f, s; f32x2 acc[12];
#pragma unroll
    for (int k = 0; k < 12; ++k) acc[k] = f32x2{0.f, 0.f};
#define QK(Kx) { float d0 = 0.f, d1 = 0.f; \
      _Pragma("unroll") for (int u = 0; u < 3; ++u) { d0 = dot2(Kx[u][0], qv[u][0], d0); d1 = dot2(Kx[u][1], qv[u][1], d1); d0 = dot2(Kx[u][2], qv[u][2], d0); d1 = dot2(Kx[u][3], qv[u][3], d1); } \
      s = d0 + d1; asm volatile("" : "+v"(s)); SCHED(); }
#define PV(Vx) { s += dpp<XOR1>(s); s += dpp<XOR2>(s); s += dpp<XOR7>(s); s *= C2; \
      const float mn = fmaxf(m, s), al = __builtin_amdgcn_exp2f(m - mn), p = __builtin_amdgcn_exp2f(s - mn); m = mn; l = fmaf(l, al, p); \
      _Pragma("unroll") for (int u = 0; u < 3; ++u) { SCHED(); \
        _Pragma("unroll") for (int k = 0; k < 4; ++k) acc[u * 4 + k] = __builtin_elementwise_fma(f32x2{p, p}, unpk(Vx[u][k]), acc[u * 4 + k] * al); \
        asm volatile("" : "+v"(acc[u * 4]), "+v"(acc[u * 4 + 1]), "+v"(acc[u * 4 + 2]), "+v"(acc[u * 4 + 3])); } SCHED(); }
#define REFILL() { LD(Kb[0], kr, nxt) PV(Vb[0]) LD(Vb[0], vr, nxt) lds_inc<false>(ctr); nxt += NS; }
    int nxt = w + NS * P0;
    static_assert(NS * (P0 + P0 / 2 + 1) <= NU, "the first refills load real units");
    QK(Kb[0]) REFILL()
#pragma unroll
    for (int i = 1; i < P0; ++i) { QK(Kb[i]) PV(Vb[i]) if (i % 2 == 0) { QK(Kb[0]) REFILL() } }
    while (nxt < NU) {
      QK(Kb[0])
#pragma unroll 1
      for (int spn = 0; spn < 256; ++spn) {
        const u64 pv = __hip_atomic_load(pace, RLX_WG);
        const float t0 = asf(__builtin_amdgcn_readfirstlane((unsigned)pv)), tau = asf(__builtin_amdgcn_readfirstlane((unsigned)(pv >> 32)));
        if (fmaf((float)nxt, tau, t0) <= (float)(int)(now() - ts)) break;
        __builtin_amdgcn_s_sleep(2);
      }
      SCHED();
      REFILL()
    }
    QK(Kb[0]) PV(Vb[0])
    {
      float M = fmaxf(m, dpp<ROR8>(m)); M = max16(M); M = max32(M);
      const float sc = __builtin_amdgcn_exp2f(m - M);
      l *= sc; l += dpp<ROR8>(l); l = tr16(l, l); l = tr32(l, l);
      float o[24];
#pragma unroll
      for (int k = 0; k < 12; ++k) { const f32x2 t = acc[k] * sc; o[2 * k] = t[0]; o[2 * k + 1] = t[1]; }
#pragma unroll
      for (int k = 0; k < 12; ++k) o[k] = tr32(o[k], o[k + 12]);
#pragma unroll
      for (int k = 0; k < 6; ++k) o[k] = tr16(o[k], o[k + 6]);
      LAS float* mg = (LAS float*)(L + L_MG) + w * 200;
#pragma unroll
      for (int k = 0; k < 3; ++k) mg[k * 64 + lane] = tr8(o[k], o[k + 3], r & 1);
      if (lane == 0) { mg[192] = M; mg[193] = l; }
    }
    lds_inc<true>(done);
    for (int spn = 0; lds_ld(done) < NS && spn < (1 << 22); ++spn) {}
    asm volatile("" ::: "memory");
    if (w < 3) {
      const LAS float* mg = (const LAS float*)(L + L_MG);
      float M = mg[192];
#pragma unroll
      for (int i = 1; i < NS; ++i) M = fmaxf(M, mg[i * 200 + 192]);
      float ls = 0.f, o = 0.f;
#pragma unroll
      for (int i = 0; i < NS; ++i) { const float e = __builtin_amdgcn_exp2f(mg[i * 200 + 192] - M); ls = fmaf(e, mg[i * 200 + 193], ls); o = fmaf(e, mg[i * 200 + tid], o); }
      gu64* px = (gu64*)(ws + WS_PX) + bid * 256;
      put(px + tid, tag, asu(o * __builtin_amdgcn_rcpf(ls)));
      if (tid == 0) put(px + 192, tag, asu(M + __builtin_amdgcn_logf(ls)));
    } else __builtin_amdgcn_s_sleep(6);
    static_assert(NS == 6, "six streaming waves");
    const int bb = w >= 3, di = (lane >> 5) * 12 + ((lane >> 4) & 1) * 6 + ((lane >> 3) & 1) * 3 + (w - 3 * bb);
    unsigned gv[8]; int go[8];
#pragma unroll
    for (int i = 0; i < 8; ++i) go[i] = WS_PX + (h * 8 + bb * 4 + (i & 3)) * 2048 + (i < 4 ? tid - bb * 192 : 192) * 8;
    {
      const u64 pv = __hip_atomic_load(pace, RLX_WG);
      const int tend = (int)fmaf((float)NU, asf(__builtin_amdgcn_readfirstlane((unsigned)(pv >> 32))), asf(__builtin_amdgcn_readfirstlane((unsigned)pv)));
      __builtin_amdgcn_s_sleep(6);
      const unsigned tb = now();
      while ((int)(now() - ts) < tend + TG1 && now() - tb < 300u) __builtin_amdgcn_s_sleep(1);
    }
    gather8<GAP1>(gv, go, wsr, tag, ts);
    { const float e0 = asf(gv[4]), e1 = asf(gv[5]), e2 = asf(gv[6]), e3 = asf(gv[7]), em = fmaxf(fmaxf(e0, e1), fmaxf(e2, e3));
      const float p0 = __builtin_amdgcn_exp2f(e0 - em), p1 = __builtin_amdgcn_exp2f(e1 - em), p2 = __builtin_amdgcn_exp2f(e2 - em), p3 = __builtin_amdgcn_exp2f(e3 - em);
      *(LAS u16*)(L + L_OA + (bb * DH + (di >> 3) * 64 + c * 8 + (di & 7)) * 2) =
          f2bf((p0 * asf(gv[0]) + p1 * asf(gv[1]) + p2 * asf(gv[2]) + p3 * asf(gv[3])) * __builtin_amdgcn_rcpf(p0 + p1 + p2 + p3));
    }
    barrier();
    if (w == 5) {
#pragma unroll
      for (int i = 0; i < 8; ++i) go[i] = (lane & 15) < 10 ? WS_RX + (j * 32 + h) * 2560 + i * 320 + (lane >> 4) * 80 + (lane & 15) * 8 : OOB;
      __builtin_amdgcn_s_sleep(PD2);
      gather8<0>(gv, go, wsr, tag, ts);
      float t = 0.f;
#pragma unroll
      for (int i = 0; i < 8; ++i) t += asf(gv[i]);
      t = tr16(t, t); t = tr32(t, t);
      asm volatile("" : "+v"(xres), "+v"(b2v));
      const float g = rbf(rbf(dpp<SHL5>(t)) + asf(b2v << 16));
      const float gate = rbf(g * __builtin_amdgcn_rcpf(1.f + __expf(-g)));
      if (lane < 5) out[oidx] = f2bf(asf(xres << 16) + rbf(rbf(t) * gate));
      if (blockIdx.x == 0 && lane == 0) __hip_atomic_store((GAS unsigned*)(ws + WS_EPOCH), tag, RLX_AGENT);
    }
  } else {
    __builtin_amdgcn_s_setprio(3);
    const int a = w - NS;
    const unsigned epoch = __hip_atomic_load((GAS unsigned*)(ws + WS_EPOCH), RLX_AGENT);
    const int xb = lane >> 5, xl = lane & 31, xo = xb * (D * 2) + xl * 40;
    const __amdgpu_buffer_rsrc_t xr = __builtin_amdgcn_make_buffer_rsrc((void*)x, 0, NB * D * 2, RSRC3);
    const v4u x0 = __builtin_amdgcn_raw_buffer_load_b128(xr, xo, 0, 0), x1 = __builtin_amdgcn_raw_buffer_load_b128(xr, xo + 16, 0, 0);
    const v2u x2 = __builtin_amdgcn_raw_buffer_load_b64(xr, xo + 32, 0, 0);
    u16 xs[3];
#pragma unroll
    for (int i = 0; i < 3; ++i) xs[i] = __builtin_amdgcn_raw_buffer_load_b16(xr, xl + 32 * i < 80 ? xb * (D * 2) + (80 * j + xl + 32 * i) * 2 : OOB, 0, 0);
    v4u wa[10], wb[10];
    { const __amdgpu_buffer_rsrc_t ra = __builtin_amdgcn_make_buffer_rsrc((void*)wq, 0, D * H * DH * 2, RSRC3);
      const __amdgpu_buffer_rsrc_t rb = __builtin_amdgcn_make_buffer_rsrc((void*)(a ? w1 : wq), 0, a ? D * F * 2 : D * H * DH * 2, RSRC3);
      const int sa = H * DH * 2, sb = a ? F * 2 : H * DH * 2;
      const int va = r * sa + c * 16, vb = (a && c >= 6) ? OOB : r * sb + c * 16;
      const int oa = 80 * j * sa + h * (DH * 2) + a * 256, ob = 80 * j * sb + (a ? h * 96 : h * (DH * 2) + 128);
#pragma unroll
      for (int u = 0; u < 10; ++u) { wa[u] = __builtin_amdgcn_raw_buffer_load_b128(ra, va, oa + u * 8 * sa, AUX_NT); wb[u] = __builtin_amdgcn_raw_buffer_load_b128(rb, vb, ob + u * 8 * sb, AUX_NT); }
    }
    SCHED();
    unsigned tag = (unsigned)__builtin_amdgcn_readfirstlane(epoch) * 0x9E3779B1u + 0x7F4A7C15u;
    tag = tag ? tag : 1u;
    if (a == 0 && lane == 0) ctr[1] = tag;
    {
      f32x2 v[10];
#pragma unroll
      for (int k = 0; k < 4; ++k) { v[k] = unpk(x0[k]); v[4 + k] = unpk(x1[k]); }
      v[8] = unpk(x2[0]); v[9] = unpk(x2[1]);
      float s = 0.f, q = 0.f;
#pragma unroll
      for (int k = 0; k < 10; ++k) s += v[k][0] + v[k][1];
      s = sum16(s); s = tr16(s, s);
      const float mean = s * (1.f / D);
#pragma unroll
      for (int k = 0; k < 10; ++k) { const f32x2 d = v[k] - mean; q = fmaf(d[0], d[0], fmaf(d[1], d[1], q)); }
      q = sum16(q); q = tr16(q, q);
      const float mb = rbf(mean), sd = rbf(sqrtf(rbf(rbf(q * (1.f / D)) + 1e-5f)));
      LAS float* xn = (LAS float*)(L + L_XN) + (a * 2 + xb) * 96 + xl;
#pragma unroll
      for (int i = 0; i < 3; ++i) xn[32 * i] = rbf(rbf(bf2f(xs[i]) - mb) / sd);
    }
    asm volatile("s_waitcnt lgkmcnt(0)" ::: "memory");
    { f32x2 p2[16];
#pragma unroll
      for (int k = 0; k < 16; ++k) p2[k] = f32x2{0.f, 0.f};
      const LAS float* xn = (const LAS float*)(L + L_XN) + a * 192 + r;
#pragma unroll
      for (int u = 0; u < 10; ++u) { const float xa = xn[8 * u], xc = xn[96 + 8 * u];
#pragma unroll
        for (int k = 0; k < 4; ++k) {
          const f32x2 fa = unpk(wa[u][k]), fb = unpk(wb[u][k]);
          p2[k] = __builtin_elementwise_fma(f32x2{xa, xa}, fa, p2[k]); p2[4 + k] = __builtin_elementwise_fma(f32x2{xa, xa}, fb, p2[4 + k]);
          p2[8 + k] = __builtin_elementwise_fma(f32x2{xc, xc}, fa, p2[8 + k]); p2[12 + k] = __builtin_elementwise_fma(f32x2{xc, xc}, fb, p2[12 + k]);
        } }
      float pa[32];
#pragma unroll
      for (int k = 0; k < 16; ++k) { pa[2 * k] = p2[k][0]; pa[2 * k + 1] = p2[k][1]; }
#pragma unroll
      for (int k = 0; k < 16; ++k) pa[k] = tr32(pa[k], pa[k + 16]);
#pragma unroll
      for (int k = 0; k < 8; ++k) pa[k] = tr16(pa[k], pa[k + 8]);
      gu64* qx = (gu64*)(ws + WS_QX) + bid * 512 + (lane >> 5) * 256 + (2 * a + ((lane >> 4) & 1)) * 64 + (lane & 15);
#pragma unroll
      for (int t = 0; t < 4; ++t) put(qx + t * 16, tag, asu(tr8(pa[t], pa[t + 4], r & 1)));
    }
    const int col = (lane & 7) * 8 + ((lane >> 3) & 1) * 4 + (lane >> 4);
    if (a == 0) {
      unsigned gv[24]; int go[24];
#pragma unroll
      for (int i = 0; i < 24; ++i) go[i] = WS_QX + ((h * 8 + i / 3) * 512 + b * 256 + (i % 3) * 64 + lane) * 8;
      { const unsigned tq = max(QD, now() - ts + QX);
        while (now() - ts < tq) __builtin_amdgcn_s_sleep(2); }
      gather<0>(gv, go, wsr, tag, ts);
#pragma unroll
      for (int u = 0; u < 3; ++u) { float q = 0.f;
#pragma unroll
        for (int p = 0; p < 8; ++p) q += asf(gv[p * 3 + u]);
        *(LAS u16*)(L + L_Q + (u * 64 + col) * 2) = f2bf(q); }
    }
    barrier();
    __builtin_amdgcn_s_setprio(0);
    if (a == 0) {
      const unsigned char* kp = (const unsigned char*)kc + (size_t)(b * H + h) * (SK * DH * 2);
      const unsigned char* vp = (const unsigned char*)vc + (size_t)(b * H + h) * (SK * DH * 2);
      const int lead = PLEAD + ((blockIdx.x >> 3) & 31) * PLSP / 32;
      auto ahead = [&](int k0, int k1) {
#pragma unroll 1
        for (int k = k0; PWS && k < k1; k += PWS) {
          while ((int)lds_ld(ctr) < k - lead && now() - ts < SPIN_TICKS) __builtin_amdgcn_s_sleep(8);
          const int po = ((rot + k) & (NU - 1)) * (32 * DH * 2);
          touch(kp + po, vp + po);
        } };
      ahead(PWS, PH + PLEAD);
      if (PWT) { touch(wo + DH * h * D, wo + (DH * h + DH - 2) * D); touch(w2 + 48 * h * D, w2 + (48 * h + 46) * D); touch(b1, out); }
      while ((int)lds_ld(ctr) < PH && now() - ts < SPIN_TICKS) __builtin_amdgcn_s_sleep(16);
      const bool live = (lane & 7) < 6;
      const float bias = bf2f(b1[48 * h + (live ? col : 0)]);
      unsigned gv[16]; int go[16];
#pragma unroll
      for (int i = 0; i < 16; ++i) go[i] = live ? WS_QX + ((h * 8) * 512 + i * 256 + 192 + lane) * 8 : OOB;
      gather<16>(gv, go, wsr, tag, ts);
      float hs[2] = {0.f, 0.f};
#pragma unroll
      for (int p = 0; p < 16; ++p) hs[p & 1] += asf(gv[p]);
      if (live) {
#pragma unroll
        for (int u = 0; u < 2; ++u) { float t = rbf(rbf(hs[u]) + bias); t = t > 0.f ? t : 0.f; *(LAS u16*)(L + L_HA + (u * 48 + col) * 2) = f2bf(t * t); } }
      ahead(PWS ? (PH + PLEAD + PWS - 1) / (PWS + !PWS) * PWS : 0, NU);
    } else {
      float lt = PINIT, l0 = (float)(int)(now() - ts) - (float)(NS * P0 + NS) * lt, up = 0.f, tp = 0.f, tb = 0.f;
      unsigned Tp = 0u;
      if (lane == 0) __hip_atomic_store(pace, ((u64)asu(lt) << 32) | asu(l0), RLX_WG);
      for (int rd = 0; rd < 8; ++rd) {
        const unsigned Tk = PT0 + rd * PDT;
        gu64* cl = (gu64*)(ws + WS_CT) + rd * 544;
        while (now() - ts < Tk && lds_ld(ctr) < NU - PSTOP) __builtin_amdgcn_s_sleep(8);
        const unsigned um = lds_ld(ctr);
        if (um >= NU - PSTOP) break;
        if (lane == 0) put(cl + blockIdx.x, tag, (ts << 16) | um);
        while (now() - ts < Tk + PLAG) __builtin_amdgcn_s_sleep(8);
        float su = 0.f, st = 0.f; int n = 0;
#pragma unroll
        for (int k = 0; k < 4; ++k) { const u64 v = __hip_atomic_load(cl + lane + 64 * k, RLX_AGENT); const bool ok = (unsigned)(v >> 32) == tag;
          su += ok ? (float)((unsigned)v & 0xffffu) : 0.f; st += ok ? (float)((int)((unsigned)v - (ts << 16)) >> 16) : 0.f; n += __builtin_popcountll(__ballot(ok)); }
        if (n < 224) continue;
        su = sum16(su); su = tr16(su, su); su = tr32(su, su); st = sum16(st); st = tr16(st, st); st = tr32(st, st);
        const float ub = su / (float)n, tm = (float)(Tk - Tp) / (ub - up);
        const bool first = Tp == 0u;
        up = ub; Tp = Tk;
        if (first) continue;
        if (tb == 0.f) tp = tb = tm * PEPS;
        else if (tm > tp * PTH) tp = fminf(tm * PTH, tp * PMX);
        else tp = fmaxf(tb, tp / PMX);
        const float tend = (float)Tk + st / (float)n + ((float)NU - ub) * tp, tnow = (float)(int)(now() - ts);
        const float ti = (tend - tnow) / ((float)NU - fminf((float)(lds_ld(ctr) + PA), (tnow - l0) / lt));
        if (ti > 0.f && ti < 1000.f) { lt = ti; l0 = tend - (float)NU * ti; if (lane == 0) __hip_atomic_store(pace, ((u64)asu(lt) << 32) | asu(l0), RLX_WG); }
      }
    }
    int vw[5], v2[4];
#pragma unroll
    for (int i = 0; i < 5; ++i) { const int id = i * 64 + lane, row = id / 10; vw[i] = row * (D * 2) + (id - row * 10) * 16; }
#pragma unroll
    for (int i = 0; i < 4; ++i) { const int id = (4 * a + i) * 64 + lane, row = id / 10; v2[i] = id < 480 ? row * (D * 2) + (id - row * 10) * 16 : OOB; }
    const __amdgpu_buffer_rsrc_t ro = __builtin_amdgcn_make_buffer_rsrc((void*)wo, 0, H * DH * D * 2, RSRC3), r2 = __builtin_amdgcn_make_buffer_rsrc((void*)w2, 0, F * D * 2, RSRC3);
    const int so = (DH * h + 96 * a) * (D * 2) + 160 * j, s2 = 48 * h * (D * 2) + 160 * j;
    const int vb = (lane >> 4) & 1, which = lane >> 5;
    gu64* rx[3];
#pragma unroll
    for (int t = 0; t < 3; ++t) {
      const int oi = vb * 80 + 16 * (3 * a + ((t == 2 && a) ? 1 : t)) + (lane & 15), hp = oi / 5;
      rx[t] = (gu64*)(ws + WS_RX) + ((j * 32 + hp) * 32 + h) * 10 + which * 5 + (oi - hp * 5);
    }
    asm volatile("" : "+v"(vw[0]), "+v"(vw[1]), "+v"(vw[2]), "+v"(vw[3]), "+v"(vw[4]), "+v"(v2[0]), "+v"(v2[1]), "+v"(v2[2]), "+v"(v2[3]), "+v"(rx[0]), "+v"(rx[1]), "+v"(rx[2]));
    while (lds_ld(ctr) < NU && now() - ts < SPIN_TICKS) __builtin_amdgcn_s_sleep(2);
#pragma unroll
    for (int i = 0; i < 19; ++i) {
      if (i < 4) __builtin_amdgcn_raw_ptr_buffer_load_lds(r2, (LAS void*)(L + L_WO + (30 + 4 * a + i) * 1024), 16, v2[i < 4 ? i : 0], s2, 0, AUX_NT);
      else __builtin_amdgcn_raw_ptr_buffer_load_lds(ro, (LAS void*)(L + L_WO + (15 * a + i - 4) * 1024), 16, vw[(i + 1) % 5], so + ((i - 4) / 5) * 32 * (D * 2), 0, AUX_NT);
      SCHED(); VMCNT(WPACE); }
    VMCNT(0);
    lds_inc<true>(done + 1);
    while (lds_ld(done + 1) < 2 && now() - ts < SPIN_TICKS) {}
    asm volatile("" ::: "memory");
    const LAS unsigned char* base = L + L_WO + (((lane & 15) >> 2) + 8 * (lane >> 4)) * 160 + a * 96 + (lane & 3) * 8;
    const LAS unsigned char* ah = L + L_HA + (lane & 1) * 96;
#define TRB(off) __builtin_amdgcn_ds_read_tr16_b64_v4i16((LAS v4s*)(base + (off)))
#define BFRAG(t, k) __builtin_bit_cast(v8bf, __builtin_shufflevector(TRB((t) * 32 + (k) * 32 * 160), TRB((t) * 32 + (k) * 32 * 160 + 4 * 160), 0, 1, 2, 3, 4, 5, 6, 7))
    const f32x4 Z = {0.f, 0.f, 0.f, 0.f};
    v8bf Bf[3][6]; float gsum[3];
#pragma unroll
    for (int t = 0; t < 3; ++t) {
      const int tt = (t == 2 && a) ? 1 : t;
#pragma unroll
      for (int k = 0; k < 6; ++k) Bf[t][k] = BFRAG(tt, k);
      f32x4 Ga = __builtin_amdgcn_mfma_f32_16x16x32_bf16(*(const LAS v8bf*)(ah + (lane >> 4) * 16), BFRAG(tt, 6), Z, 0, 0, 0);
      f32x4 Gb = __builtin_amdgcn_mfma_f32_16x16x16bf16_1k(*(const LAS v4s*)(ah + 64 + (lane >> 4) * 8), TRB(tt * 32 + 7 * 32 * 160 - 4 * (lane >> 4) * 160), Z, 0, 0, 0);
      asm volatile("" : "+v"(Ga), "+v"(Gb));
      gsum[t] = vb ? Ga[1] + Gb[1] : Ga[0] + Gb[0];
    }
#pragma unroll
    for (int t = 0; t < 3; ++t) asm volatile("" : "+v"(Bf[t][0]), "+v"(Bf[t][1]), "+v"(Bf[t][2]), "+v"(Bf[t][3]), "+v"(Bf[t][4]), "+v"(Bf[t][5]), "+v"(gsum[t]));
    barrier();
    const LAS unsigned char* ao = L + L_OA + (lane & 1) * (DH * 2) + (lane >> 4) * 16;
    v8bf A[6];
#pragma unroll
    for (int k = 0; k < 6; ++k) A[k] = *(const LAS v8bf*)(ao + k * 64);
    SCHED();
    f32x4 S[3] = {Z, Z, Z};
#pragma unroll
    for (int k = 0; k < 6; ++k)
#pragma unroll
      for (int t = 0; t < 3; ++t) S[t] = __builtin_amdgcn_mfma_f32_16x16x32_bf16(A[k], Bf[t][k], S[t], 0, 0, 0);
    asm volatile("" : "+v"(S[0]), "+v"(S[1]), "+v"(S[2]));
#pragma unroll
    for (int t = 0; t < 3; ++t) { float s0 = S[t][0], s1 = S[t][1]; asm volatile("" : "+v"(s0), "+v"(s1));
      float v = vb ? s1 : s0; v = which ? gsum[t] : v; asm volatile("" : "+v"(v));
      if (t < 2 || a == 0) put(rx[t], tag, asu(v)); }
  }
  if (CODEW) asm volatile(".Lfd_end:");
}

extern "C" void kernel_launch(void* const* d_in, const int* in_sizes, int n_in, void* d_out, int out_size, void* d_ws, size_t ws_size, hipStream_t stream) {
  fused_decode<<<256, NTHR, 0, stream>>>((const u16*)d_in[0], (const u16*)d_in[1], (const u16*)d_in[2], (const u16*)d_in[3], (const u16*)d_in[4],
                                         (const u16*)d_in[6], (const u16*)d_in[7], (const u16*)d_in[8], (const u16*)d_in[9], (u16*)d_out, (unsigned char*)d_ws);
}
```

```cpp
#include <hip/hip_runtime.h>
#include <hip/hip_bf16.h>
#include <stdint.h>

#define GAS __attribute__((address_space(1)))
#define LAS __attribute__((address_space(3)))
#define RLX_AGENT __ATOMIC_RELAXED, __HIP_MEMORY_SCOPE_AGENT
#define RLX_WG __ATOMIC_RELAXED, __HIP_MEMORY_SCOPE_WORKGROUP
typedef unsigned v4u __attribute__((ext_vector_type(4)));
typedef unsigned v2u __attribute__((ext_vector_type(2)));
typedef short v4s __attribute__((ext_vector_type(4)));
typedef __bf16 v8bf __attribute__((ext_vector_type(8)));
typedef float f32x4 __attribute__((ext_vector_type(4)));
typedef float f32x2 __attribute__((ext_vector_type(2)));
typedef __bf16 bf16x2 __attribute__((ext_vector_type(2)));
typedef unsigned short u16;
typedef unsigned long long u64;
typedef GAS u64 gu64;

constexpr int NB = 2, D = 640, H = 32, SK = 8192, DH = 192, F = 1536;
constexpr int NS = 6, NTHR = (NS + 2) * 64;
constexpr int NU = 256;
constexpr int P0 = 5;
constexpr int PW = 2;
constexpr int ROT = 4;
constexpr unsigned QD = 480;
constexpr unsigned QX = 60;
constexpr int TG1 = 205;
constexpr int PD2 = 16;
constexpr int WPACE = 7;
constexpr unsigned PT0 = 1300;
constexpr unsigned PDT = 900;
constexpr unsigned PLAG = 180;
constexpr unsigned PSTOP = 40;
constexpr unsigned PA = 1;
constexpr float PINIT = 20.7f;
constexpr float PEPS = 1.02f;
constexpr float PTH = 1.003f;
constexpr float PMX = 1.012f;
constexpr int PH = 150;
constexpr int AUX_NT = 2, AUX_SC1 = 16, RSRC3 = 0x00020000, OOB = 0x7ffffff0;
constexpr unsigned SPIN_TICKS = 2000000u;
constexpr int WS_EPOCH = 0;
constexpr int WS_QX = 4096;
constexpr int WS_PX = WS_QX + 256 * 512 * 8;
constexpr int WS_RX = WS_PX + 256 * 256 * 8;
constexpr int WS_CT = WS_RX + 8 * 32 * 320 * 8;
constexpr int WS_END = WS_CT + 8 * 544 * 8;
constexpr int L_XN = 0;
constexpr int L_Q = L_XN + 4 * 96 * 4;
constexpr int L_HA = L_Q + 384;
constexpr int L_OA = L_HA + 192;
constexpr int L_CT = L_OA + 768;
constexpr int L_MG = L_CT + 32;
constexpr int L_WO = L_MG + NS * 800;
constexpr int L_END = L_WO + 38 * 1024;
static_assert(L_WO % 16 == 0 && L_OA % 16 == 0 && L_HA % 16 == 0 && L_Q % 16 == 0 && L_CT % 16 == 0, "alignment");

__device__ __forceinline__ float asf(unsigned v) { return __builtin_bit_cast(float, v); }
__device__ __forceinline__ unsigned asu(float v) { return __builtin_bit_cast(unsigned, v); }
__device__ __forceinline__ float bf2f(u16 b) { return asf((unsigned)b << 16); }
__device__ __forceinline__ u16 f2bf(float f) { return __builtin_bit_cast(u16, (__bf16)f); }
__device__ __forceinline__ float rbf(float f) { return bf2f(f2bf(f)); }
__device__ __forceinline__ f32x2 unpk(unsigned v) { return f32x2{asf(v << 16), asf(v & 0xffff0000u)}; }
__device__ __forceinline__ float dot2(unsigned k, unsigned q, float acc) {
  return __builtin_amdgcn_fdot2_f32_bf16(__builtin_bit_cast(bf16x2, k), __builtin_bit_cast(bf16x2, q), acc, false);
}
template <int CTRL> __device__ __forceinline__ float dpp(float x) { return asf(__builtin_amdgcn_mov_dpp((int)asu(x), CTRL, 0xf, 0xf, true)); }
constexpr int XOR1 = 0xB1, XOR2 = 0x4E, XOR7 = 0x141, ROR8 = 0x128, ROR4 = 0x124, ROR2 = 0x122, ROR1 = 0x121, SHL5 = 0x105;
__device__ __forceinline__ float tr32(float x, float y) {
  auto s = __builtin_amdgcn_permlane32_swap(asu(x), asu(y), false, false); return asf(s[0]) + asf(s[1]);
}
__device__ __forceinline__ float tr16(float x, float y) {
  auto s = __builtin_amdgcn_permlane16_swap(asu(x), asu(y), false, false); return asf(s[0]) + asf(s[1]);
}
__device__ __forceinline__ float tr8(float x, float y, bool up) {
  const float keep = up ? y : x, send = up ? x : y; return keep + dpp<ROR8>(send);
}
__device__ __forceinline__ float sum16(float s) { s += dpp<ROR1>(s); s += dpp<ROR2>(s); s += dpp<ROR4>(s); return s + dpp<ROR8>(s); }
__device__ __forceinline__ float max16(float x) { auto s = __builtin_amdgcn_permlane16_swap(asu(x), asu(x), false, false); return fmaxf(asf(s[0]), asf(s[1])); }
__device__ __forceinline__ float max32(float x) { auto s = __builtin_amdgcn_permlane32_swap(asu(x), asu(x), false, false); return fmaxf(asf(s[0]), asf(s[1])); }
__device__ __forceinline__ unsigned now() { return (unsigned)__builtin_amdgcn_s_memrealtime(); }
__device__ __forceinline__ void barrier() { asm volatile("s_waitcnt lgkmcnt(0)\n\ts_barrier" ::: "memory"); }
__device__ __forceinline__ void put(gu64* g, unsigned tag, unsigned v) { __hip_atomic_store(g, ((u64)tag << 32) | v, RLX_AGENT); }
__device__ __forceinline__ unsigned lds_ld(LAS unsigned* p) { return __builtin_amdgcn_readfirstlane(__hip_atomic_load(p, RLX_WG)); }
template <bool ORDERED> __device__ __forceinline__ void lds_inc(LAS unsigned* p) {
  if (ORDERED) asm volatile("s_waitcnt lgkmcnt(0)" ::: "memory");
  asm volatile("s_mov_b64 exec, 1\n\tds_add_u32 %0, %1\n\ts_mov_b64 exec, -1" :: "v"((unsigned)(uintptr_t)p), "v"(1u) : "memory");
}
__device__ __forceinline__ void touch(const void* p, const void* q) {
  unsigned t0, t1; asm volatile("s_load_dword %0, %2, 0x0\n\ts_load_dword %1, %3, 0x0\n\ts_waitcnt lgkmcnt(0)" : "=&s"(t0), "=&s"(t1) : "s"(p), "s"(q) : "memory");
}
#define SCHED() do { asm volatile("" ::: "memory"); __builtin_amdgcn_sched_barrier(0); } while (0)
#define VMCNT(n) asm volatile("s_waitcnt vmcnt(%0)" :: "n"(n) : "memory")

template <int SLEEP, int N> __device__ __forceinline__ void gather(unsigned (&val)[N], int (&off)[N], __amdgpu_buffer_rsrc_t rs, unsigned tag, unsigned ts) {
#pragma unroll
  for (int i = 0; i < N; ++i) val[i] = 0u;
  for (;;) {
    v2u in[N];
    asm volatile("" ::: "memory");
#pragma unroll
    for (int i = 0; i < N; ++i) in[i] = __builtin_amdgcn_raw_buffer_load_b64(rs, off[i], 0, AUX_SC1);
    int left = OOB;
#pragma unroll
    for (int i = 0; i < N; ++i) { const bool got = in[i][1] == tag; val[i] = got ? in[i][0] : val[i]; off[i] = got ? OOB : off[i]; left = min(left, off[i]); }
    if (__all(left == OOB) || now() - ts > SPIN_TICKS) break;
    if (SLEEP) __builtin_amdgcn_s_sleep(SLEEP);
  }
}

__global__ void __launch_bounds__(NTHR, 2)
fused_decode(const u16* x, const u16* kc, const u16* vc, const u16* wq, const u16* wo,
             const u16* w1, const u16* b1, const u16* w2, const u16* b2, u16* out, unsigned char* ws) {
  __shared__ __attribute__((aligned(16))) unsigned char lds_raw[L_END];
  LAS unsigned char* L = (LAS unsigned char*)lds_raw;
  const int bid = (blockIdx.x & 7) * 32 + (blockIdx.x >> 3), h = bid >> 3, j = bid & 7, b = j >> 2, sp = j & 3;
  const int tid = threadIdx.x, lane = tid & 63, w = __builtin_amdgcn_readfirstlane(tid >> 6), r = lane >> 3, c = lane & 7;
  LAS unsigned* ctr = (LAS unsigned*)(L + L_CT);
  LAS u64* pace = (LAS u64*)(L + L_CT + 8);
  LAS unsigned* done = (LAS unsigned*)(L + L_CT + 16);
  const unsigned ts = now();
  const int rot = (b * H + h) * ROT;
  const __amdgpu_buffer_rsrc_t wsr = __builtin_amdgcn_make_buffer_rsrc((void*)ws, 0, WS_END, RSRC3);

  if (__builtin_expect(w < NS, 1)) {
    if (tid == 0) { *ctr = NS * P0; *pace = 0ull; done[0] = 0u; done[1] = 0u; }
    const __amdgpu_buffer_rsrc_t kr = __builtin_amdgcn_make_buffer_rsrc((void*)kc, 0, NB * H * SK * DH * 2, RSRC3);
    const __amdgpu_buffer_rsrc_t vr = __builtin_amdgcn_make_buffer_rsrc((void*)vc, 0, NB * H * SK * DH * 2, RSRC3);
    const int rbase = ((b * H + h) * SK + sp * 8) * (DH * 2), voff = r * (DH * 2) + c * 16;
#define LD(dst, rs, t) { const int so_ = rbase + (((t) + rot) & (NU - 1)) * (32 * DH * 2); \
    _Pragma("unroll") for (int u = 0; u < 3; ++u) dst[u] = __builtin_amdgcn_raw_buffer_load_b128(rs, voff + u * 128, so_, AUX_NT); SCHED(); }
    const int oi = 5 * h + (lane < 5 ? lane : 0), ocol = 80 * j + oi % 80, oidx = (oi / 80) * D + ocol;
    v4u Kb[P0][3], Vb[P0][3];
#pragma unroll
    for (int i = 0; i < P0; ++i) {
      LD(Kb[i], kr, w + i * NS)
      if (i == 0) {
        if (w == 5) touch((const unsigned char*)vc + rbase + (rot & (NU - 1)) * (32 * DH * 2), ws + 2048);
        if (w == 4) touch(wq + (size_t)80 * j * H * DH + h * DH, wq + (size_t)(80 * j + 79) * H * DH + h * DH);
        if (w == 3) touch(w1 + 80 * j * F + h * 48, w1 + (80 * j + 79) * F + h * 48);
        if (w == 2) touch(x, b2);
        if (w == 1) touch(ws + WS_END, ws + WS_END);
      }
      VMCNT(PW); LD(Vb[i], vr, w + i * NS) if (i + 1 < P0) VMCNT(PW); }
    unsigned xres = __builtin_amdgcn_raw_buffer_load_b16(__builtin_amdgcn_make_buffer_rsrc((void*)x, 0, NB * D * 2, RSRC3), w == 5 ? oidx * 2 : OOB, 0, 0);
    unsigned b2v = __builtin_amdgcn_raw_buffer_load_b16(__builtin_amdgcn_make_buffer_rsrc((void*)b2, 0, D * 2, RSRC3), w == 5 ? ocol * 2 : OOB, 0, 0);
    barrier();
    const unsigned tag = lds_ld(ctr + 1);
    v4u qv[3];
#pragma unroll
    for (int u = 0; u < 3; ++u) qv[u] = *(LAS v4u*)(L + L_Q + u * 128 + c * 16);
    constexpr float C2 = 0.07216878364870322f * 1.4426950408889634f;
    float m = -3.0e38f, l = 0.f, s; f32x2 acc[12];
#pragma unroll
    for (int k = 0; k < 12; ++k) acc[k] = f32x2{0.f, 0.f};
#define QK(Kx) { float d0 = 0.f, d1 = 0.f; \
      _Pragma("unroll") for (int u = 0; u < 3; ++u) { d0 = dot2(Kx[u][0], qv[u][0], d0); d1 = dot2(Kx[u][1], qv[u][1], d1); d0 = dot2(Kx[u][2], qv[u][2], d0); d1 = dot2(Kx[u][3], qv[u][3], d1); } \
      s = d0 + d1; asm volatile("" : "+v"(s)); SCHED(); }
#define PV(Vx) { s += dpp<XOR1>(s); s += dpp<XOR2>(s); s += dpp<XOR7>(s); s *= C2; \
      const float mn = fmaxf(m, s), al = __builtin_amdgcn_exp2f(m - mn), p = __builtin_amdgcn_exp2f(s - mn); m = mn; l = fmaf(l, al, p); \
      _Pragma("unroll") for (int u = 0; u < 3; ++u) { SCHED(); \
        _Pragma("unroll") for (int k = 0; k < 4; ++k) acc[u * 4 + k] = __builtin_elementwise_fma(f32x2{p, p}, unpk(Vx[u][k]), acc[u * 4 + k] * al); \
        asm volatile("" : "+v"(acc[u * 4]), "+v"(acc[u * 4 + 1]), "+v"(acc[u * 4 + 2]), "+v"(acc[u * 4 + 3])); } SCHED(); }
#define REFILL() { LD(Kb[0], kr, nxt) PV(Vb[0]) LD(Vb[0], vr, nxt) lds_inc<false>(ctr); nxt += NS; }
    int nxt = w + NS * P0;
    static_assert(NS * (P0 + P0 / 2 + 1) <= NU, "the first refills load real units");
    QK(Kb[0]) REFILL()
#pragma unroll
    for (int i = 1; i < P0; ++i) { QK(Kb[i]) PV(Vb[i]) if (i % 2 == 0) { QK(Kb[0]) REFILL() } }
    while (nxt < NU) {
      QK(Kb[0])
#pragma unroll 1
      for (int spn = 0; spn < 256; ++spn) {
        const u64 pv = __hip_atomic_load(pace, RLX_WG);
        const float t0 = asf(__builtin_amdgcn_readfirstlane((unsigned)pv)), tau = asf(__builtin_amdgcn_readfirstlane((unsigned)(pv >> 32)));
        if (fmaf((float)nxt, tau, t0) <= (float)(int)(now() - ts)) break;
        __builtin_amdgcn_s_sleep(2);
      }
      SCHED();
      REFILL()
    }
    QK(Kb[0]) PV(Vb[0])
    {
      float M = fmaxf(m, dpp<ROR8>(m)); M = max16(M); M = max32(M);
      const float sc = __builtin_amdgcn_exp2f(m - M);
      l *= sc; l += dpp<ROR8>(l); l = tr16(l, l); l = tr32(l, l);
      float o[24];
#pragma unroll
      for (int k = 0; k < 12; ++k) { const f32x2 t = acc[k] * sc; o[2 * k] = t[0]; o[2 * k + 1] = t[1]; }
#pragma unroll
      for (int k = 0; k < 12; ++k) o[k] = tr32(o[k], o[k + 12]);
#pragma unroll
      for (int k = 0; k < 6; ++k) o[k] = tr16(o[k], o[k + 6]);
      LAS float* mg = (LAS float*)(L + L_MG) + w * 200;
#pragma unroll
      for (int k = 0; k < 3; ++k) mg[k * 64 + lane] = tr8(o[k], o[k + 3], r & 1);
      if (lane == 0) { mg[192] = M; mg[193] = l; }
    }
    lds_inc<true>(done);
    for (int spn = 0; lds_ld(done) < NS && spn < (1 << 22); ++spn) {}
    asm volatile("" ::: "memory");
    if (w < 3) {
      const LAS float* mg = (const LAS float*)(L + L_MG);
      float M = mg[192];
#pragma unroll
      for (int i = 1; i < NS; ++i) M = fmaxf(M, mg[i * 200 + 192]);
      float ls = 0.f, o = 0.f;
#pragma unroll
      for (int i = 0; i < NS; ++i) { const float e = __builtin_amdgcn_exp2f(mg[i * 200 + 192] - M); ls = fmaf(e, mg[i * 200 + 193], ls); o = fmaf(e, mg[i * 200 + tid], o); }
      gu64* px = (gu64*)(ws + WS_PX) + bid * 256;
      put(px + tid, tag, asu(o * __builtin_amdgcn_rcpf(ls)));
      if (tid == 0) put(px + 192, tag, asu(M + __builtin_amdgcn_logf(ls)));
    } else __builtin_amdgcn_s_sleep(6);
    static_assert(NS == 6, "six streaming waves");
    const int bb = w >= 3, di = (lane >> 5) * 12 + ((lane >> 4) & 1) * 6 + ((lane >> 3) & 1) * 3 + (w - 3 * bb);
    unsigned gv[8]; int go[8];
#pragma unroll
    for (int i = 0; i < 8; ++i) go[i] = WS_PX + (h * 8 + bb * 4 + (i & 3)) * 2048 + (i < 4 ? tid - bb * 192 : 192) * 8;
    {
      const u64 pv = __hip_atomic_load(pace, RLX_WG);
      const int tend = (int)fmaf((float)NU, asf(__builtin_amdgcn_readfirstlane((unsigned)(pv >> 32))), asf(__builtin_amdgcn_readfirstlane((unsigned)pv)));
      __builtin_amdgcn_s_sleep(6);
      const unsigned tb = now();
      while ((int)(now() - ts) < tend + TG1 && now() - tb < 300u) __builtin_amdgcn_s_sleep(1);
    }
    gather<0>(gv, go, wsr, tag, ts);
    { const float e0 = asf(gv[4]), e1 = asf(gv[5]), e2 = asf(gv[6]), e3 = asf(gv[7]), em = fmaxf(fmaxf(e0, e1), fmaxf(e2, e3));
      const float p0 = __builtin_amdgcn_exp2f(e0 - em), p1 = __builtin_amdgcn_exp2f(e1 - em), p2 = __builtin_amdgcn_exp2f(e2 - em), p3 = __builtin_amdgcn_exp2f(e3 - em);
      *(LAS u16*)(L + L_OA + (bb * DH + (di >> 3) * 64 + c * 8 + (di & 7)) * 2) =
          f2bf((p0 * asf(gv[0]) + p1 * asf(gv[1]) + p2 * asf(gv[2]) + p3 * asf(gv[3])) * __builtin_amdgcn_rcpf(p0 + p1 + p2 + p3));
    }
    barrier();
    if (w == 5) {
#pragma unroll
      for (int i = 0; i < 8; ++i) go[i] = (lane & 15) < 10 ? WS_RX + (j * 32 + h) * 2560 + i * 320 + (lane >> 4) * 80 + (lane & 15) * 8 : OOB;
      __builtin_amdgcn_s_sleep(PD2);
      gather<0>(gv, go, wsr, tag, ts);
      float t = 0.f;
#pragma unroll
      for (int i = 0; i < 8; ++i) t += asf(gv[i]);
      t = tr16(t, t); t = tr32(t, t);
      asm volatile("" : "+v"(xres), "+v"(b2v));
      const float g = rbf(rbf(dpp<SHL5>(t)) + asf(b2v << 16));
      const float gate = rbf(g * __builtin_amdgcn_rcpf(1.f + __expf(-g)));
      if (lane < 5) out[oidx] = f2bf(asf(xres << 16) + rbf(rbf(t) * gate));
      if (blockIdx.x == 0 && lane == 0) __hip_atomic_store((GAS unsigned*)(ws + WS_EPOCH), tag, RLX_AGENT);
    }
  } else {
    __builtin_amdgcn_s_setprio(3);
    const int a = w - NS;
    const unsigned epoch = __hip_atomic_load((GAS unsigned*)(ws + WS_EPOCH), RLX_AGENT);
    const int xb = lane >> 5, xl = lane & 31, xo = xb * (D * 2) + xl * 40;
    const __amdgpu_buffer_rsrc_t xr = __builtin_amdgcn_make_buffer_rsrc((void*)x, 0, NB * D * 2, RSRC3);
    const v4u x0 = __builtin_amdgcn_raw_buffer_load_b128(xr, xo, 0, 0), x1 = __builtin_amdgcn_raw_buffer_load_b128(xr, xo + 16, 0, 0);
    const v2u x2 = __builtin_amdgcn_raw_buffer_load_b64(xr, xo + 32, 0, 0);
    u16 xs[3];
#pragma unroll
    for (int i = 0; i < 3; ++i) xs[i] = __builtin_amdgcn_raw_buffer_load_b16(xr, xl + 32 * i < 80 ? xb * (D * 2) + (80 * j + xl + 32 * i) * 2 : OOB, 0, 0);
    v4u wa[10], wb[10];
    { const __amdgpu_buffer_rsrc_t ra = __builtin_amdgcn_make_buffer_rsrc((void*)wq, 0, D * H * DH * 2, RSRC3);
      const __amdgpu_buffer_rsrc_t rb = __builtin_amdgcn_make_buffer_rsrc((void*)(a ? w1 : wq), 0, a ? D * F * 2 : D * H * DH * 2, RSRC3);
      const int sa = H * DH * 2, sb = a ? F * 2 : H * DH * 2;
      const int va = r * sa + c * 16, vb = (a && c >= 6) ? OOB : r * sb + c * 16;
      const int oa = 80 * j * sa + h * (DH * 2) + a * 256, ob = 80 * j * sb + (a ? h * 96 : h * (DH * 2) + 128);
#pragma unroll
      for (int u = 0; u < 10; ++u) { wa[u] = __builtin_amdgcn_raw_buffer_load_b128(ra, va, oa + u * 8 * sa, AUX_NT); wb[u] = __builtin_amdgcn_raw_buffer_load_b128(rb, vb, ob + u * 8 * sb, AUX_NT); }
    }
    SCHED();
    unsigned tag = (unsigned)__builtin_amdgcn_readfirstlane(epoch) * 0x9E3779B1u + 0x7F4A7C15u;
    tag = tag ? tag : 1u;
    if (a == 0 && lane == 0) ctr[1] = tag;
    {
      f32x2 v[10];
#pragma unroll
      for (int k = 0; k < 4; ++k) { v[k] = unpk(x0[k]); v[4 + k] = unpk(x1[k]); }
      v[8] = unpk(x2[0]); v[9] = unpk(x2[1]);
      float s = 0.f, q = 0.f;
#pragma unroll
      for (int k = 0; k < 10; ++k) s += v[k][0] + v[k][1];
      s = sum16(s); s = tr16(s, s);
      const float mean = s * (1.f / D);
#pragma unroll
      for (int k = 0; k < 10; ++k) { const f32x2 d = v[k] - mean; q = fmaf(d[0], d[0], fmaf(d[1], d[1], q)); }
      q = sum16(q); q = tr16(q, q);
      const float mb = rbf(mean), sd = rbf(sqrtf(rbf(rbf(q * (1.f / D)) + 1e-5f)));
      LAS float* xn = (LAS float*)(L + L_XN) + (a * 2 + xb) * 96 + xl;
#pragma unroll
      for (int i = 0; i < 3; ++i) xn[32 * i] = rbf(rbf(bf2f(xs[i]) - mb) / sd);
    }
    asm volatile("s_waitcnt lgkmcnt(0)" ::: "memory");
    { f32x2 p2[16];
#pragma unroll
      for (int k = 0; k < 16; ++k) p2[k] = f32x2{0.f, 0.f};
      const LAS float* xn = (const LAS float*)(L + L_XN) + a * 192 + r;
#pragma unroll
      for (int u = 0; u < 10; ++u) { const float xa = xn[8 * u], xc = xn[96 + 8 * u];
#pragma unroll
        for (int k = 0; k < 4; ++k) {
          const f32x2 fa = unpk(wa[u][k]), fb = unpk(wb[u][k]);
          p2[k] = __builtin_elementwise_fma(f32x2{xa, xa}, fa, p2[k]); p2[4 + k] = __builtin_elementwise_fma(f32x2{xa, xa}, fb, p2[4 + k]);
          p2[8 + k] = __builtin_elementwise_fma(f32x2{xc, xc}, fa, p2[8 + k]); p2[12 + k] = __builtin_elementwise_fma(f32x2{xc, xc}, fb, p2[12 + k]);
        } }
      float pa[32];
#pragma unroll
      for (int k = 0; k < 16; ++k) { pa[2 * k] = p2[k][0]; pa[2 * k + 1] = p2[k][1]; }
#pragma unroll
      for (int k = 0; k < 16; ++k) pa[k] = tr32(pa[k], pa[k + 16]);
#pragma unroll
      for (int k = 0; k < 8; ++k) pa[k] = tr16(pa[k], pa[k + 8]);
      gu64* qx = (gu64*)(ws + WS_QX) + bid * 512 + (lane >> 5) * 256 + (2 * a + ((lane >> 4) & 1)) * 64 + (lane & 15);
#pragma unroll
      for (int t = 0; t < 4; ++t) put(qx + t * 16, tag, asu(tr8(pa[t], pa[t + 4], r & 1)));
    }
    const int col = (lane & 7) * 8 + ((lane >> 3) & 1) * 4 + (lane >> 4);
    if (a == 0) {
      unsigned gv[24]; int go[24];
#pragma unroll
      for (int i = 0; i < 24; ++i) go[i] = WS_QX + ((h * 8 + i / 3) * 512 + b * 256 + (i % 3) * 64 + lane) * 8;
      { const unsigned tq = max(QD, now() - ts + QX);
        while (now() - ts < tq) __builtin_amdgcn_s_sleep(2); }
      gather<0>(gv, go, wsr, tag, ts);
#pragma unroll
      for (int u = 0; u < 3; ++u) { float q = 0.f;
#pragma unroll
        for (int p = 0; p < 8; ++p) q += asf(gv[p * 3 + u]);
        *(LAS u16*)(L + L_Q + (u * 64 + col) * 2) = f2bf(q); }
    }
    barrier();
    __builtin_amdgcn_s_setprio(0);
    if (a == 0) {
      touch(wo + DH * h * D, wo + (DH * h + DH - 2) * D); touch(w2 + 48 * h * D, w2 + (48 * h + 46) * D); touch(b1, out);
      while ((int)lds_ld(ctr) < PH && now() - ts < SPIN_TICKS) __builtin_amdgcn_s_sleep(16);
      const bool live = (lane & 7) < 6;
      const float bias = bf2f(b1[48 * h + (live ? col : 0)]);
      unsigned gv[16]; int go[16];
#pragma unroll
      for (int i = 0; i < 16; ++i) go[i] = live ? WS_QX + ((h * 8) * 512 + i * 256 + 192 + lane) * 8 : OOB;
      gather<16>(gv, go, wsr, tag, ts);
      float hs[2] = {0.f, 0.f};
#pragma unroll
      for (int p = 0; p < 16; ++p) hs[p & 1] += asf(gv[p]);
      if (live) {
#pragma unroll
        for (int u = 0; u < 2; ++u) { float t = rbf(rbf(hs[u]) + bias); t = t > 0.f ? t : 0.f; *(LAS u16*)(L + L_HA + (u * 48 + col) * 2) = f2bf(t * t); } }
    } else {
      float lt = PINIT, l0 = (float)(int)(now() - ts) - (float)(NS * P0 + NS) * lt, up = 0.f, tp = 0.f, tb = 0.f;
      unsigned Tp = 0u;
      if (lane == 0) __hip_atomic_store(pace, ((u64)asu(lt) << 32) | asu(l0), RLX_WG);
      for (int rd = 0; rd < 8; ++rd) {
        const unsigned Tk = PT0 + rd * PDT;
        gu64* cl = (gu64*)(ws + WS_CT) + rd * 544;
        while (now() - ts < Tk && lds_ld(ctr) < NU - PSTOP) __builtin_amdgcn_s_sleep(8);
        const unsigned um = lds_ld(ctr);
        if (um >= NU - PSTOP) break;
        if (lane == 0) put(cl + blockIdx.x, tag, (ts << 16) | um);
        while (now() - ts < Tk + PLAG) __builtin_amdgcn_s_sleep(8);
        float su = 0.f, st = 0.f; int n = 0;
#pragma unroll
        for (int k = 0; k < 4; ++k) { const u64 v = __hip_atomic_load(cl + lane + 64 * k, RLX_AGENT); const bool ok = (unsigned)(v >> 32) == tag;
          su += ok ? (float)((unsigned)v & 0xffffu) : 0.f; st += ok ? (float)((int)((unsigned)v - (ts << 16)) >> 16) : 0.f; n += __builtin_popcountll(__ballot(ok)); }
        if (n < 224) continue;
        su = sum16(su); su = tr16(su, su); su = tr32(su, su); st = sum16(st); st = tr16(st, st); st = tr32(st, st);
        const float ub = su / (float)n, tm = (float)(Tk - Tp) / (ub - up);
        const bool first = Tp == 0u;
        up = ub; Tp = Tk;
        if (first) continue;
        if (tb == 0.f) tp = tb = tm * PEPS;
        else if (tm > tp * PTH) tp = fminf(tm * PTH, tp * PMX);
        else tp = fmaxf(tb, tp / PMX);
        const float tend = (float)Tk + st / (float)n + ((float)NU - ub) * tp, tnow = (float)(int)(now() - ts);
        const float ti = (tend - tnow) / ((float)NU - fminf((float)(lds_ld(ctr) + PA), (tnow - l0) / lt));
        if (ti > 0.f && ti < 1000.f) { lt = ti; l0 = tend - (float)NU * ti; if (lane == 0) __hip_atomic_store(pace, ((u64)asu(lt) << 32) | asu(l0), RLX_WG); }
      }
    }
    int vw[5], v2[4];
#pragma unroll
    for (int i = 0; i < 5; ++i) { const int id = i * 64 + lane, row = id / 10; vw[i] = row * (D * 2) + (id - row * 10) * 16; }
#pragma unroll
    for (int i = 0; i < 4; ++i) { const int id = (4 * a + i) * 64 + lane, row = id / 10; v2[i] = id < 480 ? row * (D * 2) + (id - row * 10) * 16 : OOB; }
    const __amdgpu_buffer_rsrc_t ro = __builtin_amdgcn_make_buffer_rsrc((void*)wo, 0, H * DH * D * 2, RSRC3), r2 = __builtin_amdgcn_make_buffer_rsrc((void*)w2, 0, F * D * 2, RSRC3);
    const int so = (DH * h + 96 * a) * (D * 2) + 160 * j, s2 = 48 * h * (D * 2) + 160 * j;
    const int vb = (lane >> 4) & 1, which = lane >> 5;
    gu64* rx[3];
#pragma unroll
    for (int t = 0; t < 3; ++t) {
      const int oi = vb * 80 + 16 * (3 * a + ((t == 2 && a) ? 1 : t)) + (lane & 15), hp = oi / 5;
      rx[t] = (gu64*)(ws + WS_RX) + ((j * 32 + hp) * 32 + h) * 10 + which * 5 + (oi - hp * 5);
    }
    asm volatile("" : "+v"(vw[0]), "+v"(vw[1]), "+v"(vw[2]), "+v"(vw[3]), "+v"(vw[4]), "+v"(v2[0]), "+v"(v2[1]), "+v"(v2[2]), "+v"(v2[3]), "+v"(rx[0]), "+v"(rx[1]), "+v"(rx[2]));
    while (lds_ld(ctr) < NU && now() - ts < SPIN_TICKS) __builtin_amdgcn_s_sleep(2);
#pragma unroll
    for (int i = 0; i < 19; ++i) {
      if (i < 4) __builtin_amdgcn_raw_ptr_buffer_load_lds(r2, (LAS void*)(L + L_WO + (30 + 4 * a + i) * 1024), 16, v2[i < 4 ? i : 0], s2, 0, AUX_NT);
      else __builtin_amdgcn_raw_ptr_buffer_load_lds(ro, (LAS void*)(L + L_WO + (15 * a + i - 4) * 1024), 16, vw[(i + 1) % 5], so + ((i - 4) / 5) * 32 * (D * 2), 0, AUX_NT);
      SCHED(); VMCNT(WPACE); }
    VMCNT(0);
    lds_inc<true>(done + 1);
    while (lds_ld(done + 1) < 2 && now() - ts < SPIN_TICKS) {}
    asm volatile("" ::: "memory");
    const LAS unsigned char* base = L + L_WO + (((lane & 15) >> 2) + 8 * (lane >> 4)) * 160 + a * 96 + (lane & 3) * 8;
    const LAS unsigned char* ah = L + L_HA + (lane & 1) * 96;
#define TRB(off) __builtin_amdgcn_ds_read_tr16_b64_v4i16((LAS v4s*)(base + (off)))
#define BFRAG(t, k) __builtin_bit_cast(v8bf, __builtin_shufflevector(TRB((t) * 32 + (k) * 32 * 160), TRB((t) * 32 + (k) * 32 * 160 + 4 * 160), 0, 1, 2, 3, 4, 5, 6, 7))
    const f32x4 Z = {0.f, 0.f, 0.f, 0.f};
    v8bf Bf[3][6]; float gsum[3];
#pragma unroll
    for (int t = 0; t < 3; ++t) {
      const int tt = (t == 2 && a) ? 1 : t;
#pragma unroll
      for (int k = 0; k < 6; ++k) Bf[t][k] = BFRAG(tt, k);
      f32x4 Ga = __builtin_amdgcn_mfma_f32_16x16x32_bf16(*(const LAS v8bf*)(ah + (lane >> 4) * 16), BFRAG(tt, 6), Z, 0, 0, 0);
      f32x4 Gb = __builtin_amdgcn_mfma_f32_16x16x16bf16_1k(*(const LAS v4s*)(ah + 64 + (lane >> 4) * 8), TRB(tt * 32 + 7 * 32 * 160 - 4 * (lane >> 4) * 160), Z, 0, 0, 0);
      asm volatile("" : "+v"(Ga), "+v"(Gb));
      gsum[t] = vb ? Ga[1] + Gb[1] : Ga[0] + Gb[0];
    }
#pragma unroll
    for (int t = 0; t < 3; ++t) asm volatile("" : "+v"(Bf[t][0]), "+v"(Bf[t][1]), "+v"(Bf[t][2]), "+v"(Bf[t][3]), "+v"(Bf[t][4]), "+v"(Bf[t][5]), "+v"(gsum[t]));
    barrier();
    const LAS unsigned char* ao = L + L_OA + (lane & 1) * (DH * 2) + (lane >> 4) * 16;
    v8bf A[6];
#pragma unroll
    for (int k = 0; k < 6; ++k) A[k] = *(const LAS v8bf*)(ao + k * 64);
    SCHED();
    f32x4 S[3] = {Z, Z, Z};
#pragma unroll
    for (int k = 0; k < 6; ++k)
#pragma unroll
      for (int t = 0; t < 3; ++t) S[t] = __builtin_amdgcn_mfma_f32_16x16x32_bf16(A[k], Bf[t][k], S[t], 0, 0, 0);
    asm volatile("" : "+v"(S[0]), "+v"(S[1]), "+v"(S[2]));
#pragma unroll
    for (int t = 0; t < 3; ++t) { float s0 = S[t][0], s1 = S[t][1]; asm volatile("" : "+v"(s0), "+v"(s1));
      float v = vb ? s1 : s0; v = which ? gsum[t] : v; asm volatile("" : "+v"(v));
      if (t < 2 || a == 0) put(rx[t], tag, asu(v)); }
  }
}

extern "C" void kernel_launch(void* const* d_in, const int* in_sizes, int n_in, void* d_out, int out_size, void* d_ws, size_t ws_size, hipStream_t stream) {
  fused_decode<<<256, NTHR, 0, stream>>>((const u16*)d_in[0], (const u16*)d_in[1], (const u16*)d_in[2], (const u16*)d_in[3], (const u16*)d_in[4],
                                         (const u16*)d_in[6], (const u16*)d_in[7], (const u16*)d_in[8], (const u16*)d_in[9], (u16*)d_out, (unsigned char*)d_ws);
}
```

```cpp
#include <hip/hip_runtime.h>
#include <hip/hip_bf16.h>
#include <stdint.h>

#define GAS __attribute__((address_space(1)))
#define LAS __attribute__((address_space(3)))
#define RLX_AGENT __ATOMIC_RELAXED, __HIP_MEMORY_SCOPE_AGENT
#define RLX_WG __ATOMIC_RELAXED, __HIP_MEMORY_SCOPE_WORKGROUP
typedef unsigned v4u __attribute__((ext_vector_type(4)));
typedef unsigned v2u __attribute__((ext_vector_type(2)));
typedef short v4s __attribute__((ext_vector_type(4)));
typedef __bf16 v8bf __attribute__((ext_vector_type(8)));
typedef float f32x4 __attribute__((ext_vector_type(4)));
typedef float f32x2 __attribute__((ext_vector_type(2)));
typedef __bf16 bf16x2 __attribute__((ext_vector_type(2)));
typedef unsigned short u16;
typedef unsigned long long u64;
typedef GAS u64 gu64;

constexpr int NB = 2, D = 640, H = 32, SK = 8192, DH = 192, F = 1536;
constexpr int NS = 6, NTHR = (NS + 2) * 64;
constexpr int NU = 256;
constexpr int P0 = 5;
constexpr int PW = 2;
constexpr int ROT = 4;
constexpr unsigned QD = 480;
constexpr unsigned QX = 60;
constexpr int TG1 = 205;
constexpr int PD2 = 16;
constexpr int WPACE = 7;
constexpr unsigned PT0 = 1300;
constexpr unsigned PDT = 900;
constexpr unsigned PLAG = 180;
constexpr unsigned PSTOP = 40;
constexpr unsigned PA = 1;
constexpr float PINIT = 20.7f;
constexpr float PEPS = 1.02f;
constexpr float PTH = 1.003f;
constexpr float PMX = 1.012f;
constexpr int PH = 150;
constexpr float PCAP = 23.9f;
constexpr float PHI = 44.f;
constexpr float PREC = 0.96f;
constexpr int AUX_NT = 2, AUX_SC1 = 16, RSRC3 = 0x00020000, OOB = 0x7ffffff0;
constexpr unsigned SPIN_TICKS = 2000000u;
constexpr int WS_EPOCH = 0;
constexpr int WS_QX = 4096;
constexpr int WS_PX = WS_QX + 256 * 512 * 8;
constexpr int WS_RX = WS_PX + 256 * 256 * 8;
constexpr int WS_CT = WS_RX + 8 * 32 * 320 * 8;
constexpr int WS_END = WS_CT + 8 * 544 * 8;
constexpr int L_XN = 0;
constexpr int L_Q = L_XN + 4 * 96 * 4;
constexpr int L_HA = L_Q + 384;
constexpr int L_OA = L_HA + 192;
constexpr int L_CT = L_OA + 768;
constexpr int L_MG = L_CT + 32;
constexpr int L_WO = L_MG + NS * 800;
constexpr int L_END = L_WO + 38 * 1024;
static_assert(L_WO % 16 == 0 && L_OA % 16 == 0 && L_HA % 16 == 0 && L_Q % 16 == 0 && L_CT % 16 == 0, "alignment");

__device__ __forceinline__ float asf(unsigned v) { return __builtin_bit_cast(float, v); }
__device__ __forceinline__ unsigned asu(float v) { return __builtin_bit_cast(unsigned, v); }
__device__ __forceinline__ float bf2f(u16 b) { return asf((unsigned)b << 16); }
__device__ __forceinline__ u16 f2bf(float f) { return __builtin_bit_cast(u16, (__bf16)f); }
__device__ __forceinline__ float rbf(float f) { return bf2f(f2bf(f)); }
__device__ __forceinline__ f32x2 unpk(unsigned v) { return f32x2{asf(v << 16), asf(v & 0xffff0000u)}; }
__device__ __forceinline__ float dot2(unsigned k, unsigned q, float acc) {
  return __builtin_amdgcn_fdot2_f32_bf16(__builtin_bit_cast(bf16x2, k), __builtin_bit_cast(bf16x2, q), acc, false);
}
template <int CTRL> __device__ __forceinline__ float dpp(float x) { return asf(__builtin_amdgcn_mov_dpp((int)asu(x), CTRL, 0xf, 0xf, true)); }
constexpr int XOR1 = 0xB1, XOR2 = 0x4E, XOR7 = 0x141, ROR8 = 0x128, ROR4 = 0x124, ROR2 = 0x122, ROR1 = 0x121, SHL5 = 0x105;
__device__ __forceinline__ float tr32(float x, float y) {
  auto s = __builtin_amdgcn_permlane32_swap(asu(x), asu(y), false, false); return asf(s[0]) + asf(s[1]);
}
__device__ __forceinline__ float tr16(float x, float y) {
  auto s = __builtin_amdgcn_permlane16_swap(asu(x), asu(y), false, false); return asf(s[0]) + asf(s[1]);
}
__device__ __forceinline__ float tr8(float x, float y, bool up) {
  const float keep = up ? y : x, send = up ? x : y; return keep + dpp<ROR8>(send);
}
__device__ __forceinline__ float sum16(float s) { s += dpp<ROR1>(s); s += dpp<ROR2>(s); s += dpp<ROR4>(s); return s + dpp<ROR8>(s); }
__device__ __forceinline__ float max16(float x) { auto s = __builtin_amdgcn_permlane16_swap(asu(x), asu(x), false, false); return fmaxf(asf(s[0]), asf(s[1])); }
__device__ __forceinline__ float max32(float x) { auto s = __builtin_amdgcn_permlane32_swap(asu(x), asu(x), false, false); return fmaxf(asf(s[0]), asf(s[1])); }
__device__ __forceinline__ unsigned now() { return (unsigned)__builtin_amdgcn_s_memrealtime(); }
__device__ __forceinline__ void barrier() { asm volatile("s_waitcnt lgkmcnt(0)\n\ts_barrier" ::: "memory"); }
__device__ __forceinline__ void put(gu64* g, unsigned tag, unsigned v) { __hip_atomic_store(g, ((u64)tag << 32) | v, RLX_AGENT); }
__device__ __forceinline__ unsigned lds_ld(LAS unsigned* p) { return __builtin_amdgcn_readfirstlane(__hip_atomic_load(p, RLX_WG)); }
template <bool ORDERED> __device__ __forceinline__ void lds_inc(LAS unsigned* p, unsigned v = 1u) {
  if (ORDERED) asm volatile("s_waitcnt lgkmcnt(0)" ::: "memory");
  asm volatile("s_mov_b64 exec, 1\n\tds_add_u32 %0, %1\n\ts_mov_b64 exec, -1" :: "v"((unsigned)(uintptr_t)p), "v"(v) : "memory");
}
__device__ __forceinline__ void touch(const void* p, const void* q) {
  unsigned t0, t1; asm volatile("s_load_dword %0, %2, 0x0\n\ts_load_dword %1, %3, 0x0\n\ts_waitcnt lgkmcnt(0)" : "=&s"(t0), "=&s"(t1) : "s"(p), "s"(q) : "memory");
}
#define SCHED() do { asm volatile("" ::: "memory"); __builtin_amdgcn_sched_barrier(0); } while (0)
#define VMCNT(n) asm volatile("s_waitcnt vmcnt(%0)" :: "n"(n) : "memory")

template <int SLEEP, int N> __device__ __forceinline__ void gather(unsigned (&val)[N], int (&off)[N], __amdgpu_buffer_rsrc_t rs, unsigned tag, unsigned ts) {
#pragma unroll
  for (int i = 0; i < N; ++i) val[i] = 0u;
  for (;;) {
    v2u in[N];
    asm volatile("" ::: "memory");
#pragma unroll
    for (int i = 0; i < N; ++i) in[i] = __builtin_amdgcn_raw_buffer_load_b64(rs, off[i], 0, AUX_SC1);
    int left = OOB;
#pragma unroll
    for (int i = 0; i < N; ++i) { const bool got = in[i][1] == tag; val[i] = got ? in[i][0] : val[i]; off[i] = got ? OOB : off[i]; left = min(left, off[i]); }
    if (__all(left == OOB) || now() - ts > SPIN_TICKS) break;
    if (SLEEP) __builtin_amdgcn_s_sleep(SLEEP);
  }
}

__global__ void __launch_bounds__(NTHR, 2)
fused_decode(const u16* x, const u16* kc, const u16* vc, const u16* wq, const u16* wo,
             const u16* w1, const u16* b1, const u16* w2, const u16* b2, u16* out, unsigned char* ws) {
  __shared__ __attribute__((aligned(16))) unsigned char lds_raw[L_END];
  LAS unsigned char* L = (LAS unsigned char*)lds_raw;
  const int bid = (blockIdx.x & 7) * 32 + (blockIdx.x >> 3), h = bid >> 3, j = bid & 7, b = j >> 2, sp = j & 3;
  const int tid = threadIdx.x, lane = tid & 63, w = __builtin_amdgcn_readfirstlane(tid >> 6), r = lane >> 3, c = lane & 7;
  LAS unsigned* ctr = (LAS unsigned*)(L + L_CT);
  LAS u64* pace = (LAS u64*)(L + L_CT + 8);
  LAS unsigned* done = (LAS unsigned*)(L + L_CT + 16);
  const unsigned ts = now();
  const int rot = (b * H + h) * ROT;
  const __amdgpu_buffer_rsrc_t wsr = __builtin_amdgcn_make_buffer_rsrc((void*)ws, 0, WS_END, RSRC3);

  if (__builtin_expect(w < NS, 1)) {
    if (tid == 0) { *ctr = NS * P0; *pace = 0ull; done[0] = 0u; done[1] = 0u; done[2] = 0u; }
    const __amdgpu_buffer_rsrc_t kr = __builtin_amdgcn_make_buffer_rsrc((void*)kc, 0, NB * H * SK * DH * 2, RSRC3);
    const __amdgpu_buffer_rsrc_t vr = __builtin_amdgcn_make_buffer_rsrc((void*)vc, 0, NB * H * SK * DH * 2, RSRC3);
    const int rbase = ((b * H + h) * SK + sp * 8) * (DH * 2), voff = r * (DH * 2) + c * 16;
#define LD(dst, rs, t) { const int so_ = rbase + (((t) + rot) & (NU - 1)) * (32 * DH * 2); \
    _Pragma("unroll") for (int u = 0; u < 3; ++u) dst[u] = __builtin_amdgcn_raw_buffer_load_b128(rs, voff + u * 128, so_, AUX_NT); SCHED(); }
    const int oi = 5 * h + (lane < 5 ? lane : 0), ocol = 80 * j + oi % 80, oidx = (oi / 80) * D + ocol;
    v4u Kb[P0][3], Vb[P0][3];
#pragma unroll
    for (int i = 0; i < P0; ++i) {
      LD(Kb[i], kr, w + i * NS)
      if (i == 0) {
        if (w == 5) touch((const unsigned char*)vc + rbase + (rot & (NU - 1)) * (32 * DH * 2), ws + 2048);
        if (w == 4) touch(wq + (size_t)80 * j * H * DH + h * DH, wq + (size_t)(80 * j + 79) * H * DH + h * DH);
        if (w == 3) touch(w1 + 80 * j * F + h * 48, w1 + (80 * j + 79) * F + h * 48);
        if (w == 2) touch(x, b2);
        if (w == 1) touch(ws + WS_END, ws + WS_END);
      }
      VMCNT(PW);
      LD(Vb[i], vr, w + i * NS) if (i + 1 < P0) VMCNT(PW); }
    unsigned xres = __builtin_amdgcn_raw_buffer_load_b16(__builtin_amdgcn_make_buffer_rsrc((void*)x, 0, NB * D * 2, RSRC3), w == 5 ? oidx * 2 : OOB, 0, 0);
    unsigned b2v = __builtin_amdgcn_raw_buffer_load_b16(__builtin_amdgcn_make_buffer_rsrc((void*)b2, 0, D * 2, RSRC3), w == 5 ? ocol * 2 : OOB, 0, 0);
    barrier();
    const unsigned tag = lds_ld(ctr + 1);
    v4u qv[3];
#pragma unroll
    for (int u = 0; u < 3; ++u) qv[u] = *(LAS v4u*)(L + L_Q + u * 128 + c * 16);
    constexpr float C2 = 0.07216878364870322f * 1.4426950408889634f;
    float m = -3.0e38f, l = 0.f, s; f32x2 acc[12];
#pragma unroll
    for (int k = 0; k < 12; ++k) acc[k] = f32x2{0.f, 0.f};
#define QK(Kx) { float d0 = 0.f, d1 = 0.f; \
      _Pragma("unroll") for (int u = 0; u < 3; ++u) { d0 = dot2(Kx[u][0], qv[u][0], d0); d1 = dot2(Kx[u][1], qv[u][1], d1); d0 = dot2(Kx[u][2], qv[u][2], d0); d1 = dot2(Kx[u][3], qv[u][3], d1); } \
      s = d0 + d1; asm volatile("" : "+v"(s)); SCHED(); }
#define PV(Vx) { s += dpp<XOR1>(s); s += dpp<XOR2>(s); s += dpp<XOR7>(s); s *= C2; \
      const float mn = fmaxf(m, s), al = __builtin_amdgcn_exp2f(m - mn), p = __builtin_amdgcn_exp2f(s - mn); m = mn; l = fmaf(l, al, p); \
      _Pragma("unroll") for (int u = 0; u < 3; ++u) { SCHED(); \
        _Pragma("unroll") for (int k = 0; k < 4; ++k) acc[u * 4 + k] = __builtin_elementwise_fma(f32x2{p, p}, unpk(Vx[u][k]), acc[u * 4 + k] * al); \
        asm volatile("" : "+v"(acc[u * 4]), "+v"(acc[u * 4 + 1]), "+v"(acc[u * 4 + 2]), "+v"(acc[u * 4 + 3])); } SCHED(); }
#define REFILL() { LD(Kb[0], kr, nxt) PV(Vb[0]) LD(Vb[0], vr, nxt) lds_inc<false>(ctr); nxt += NS; }
    int nxt = w + NS * P0;
    static_assert(NS * (P0 + P0 / 2 + 1) <= NU, "the first refills load real units");
    QK(Kb[0]) REFILL()
#pragma unroll
    for (int i = 1; i < P0; ++i) { QK(Kb[i]) PV(Vb[i]) if (i % 2 == 0) { QK(Kb[0]) REFILL() } }
    while (nxt < NU) {
      QK(Kb[0])
#pragma unroll 1
      for (int spn = 0; spn < 256; ++spn) {
        const u64 pv = __hip_atomic_load(pace, RLX_WG);
        const float t0 = asf(__builtin_amdgcn_readfirstlane((unsigned)pv)), tau = asf(__builtin_amdgcn_readfirstlane((unsigned)(pv >> 32)));
        const float early = fmaf((float)nxt, tau, t0) - (float)(int)(now() - ts);
        if (early <= 0.f) break;
        if (spn == 0) lds_inc<false>(done + 2, (unsigned)early);
        __builtin_amdgcn_s_sleep(2);
      }
      SCHED();
      REFILL()
    }
    QK(Kb[0]) PV(Vb[0])
    {
      float M = fmaxf(m, dpp<ROR8>(m)); M = max16(M); M = max32(M);
      const float sc = __builtin_amdgcn_exp2f(m - M);
      l *= sc; l += dpp<ROR8>(l); l = tr16(l, l); l = tr32(l, l);
      float o[24];
#pragma unroll
      for (int k = 0; k < 12; ++k) { const f32x2 t = acc[k] * sc; o[2 * k] = t[0]; o[2 * k + 1] = t[1]; }
#pragma unroll
      for (int k = 0; k < 12; ++k) o[k] = tr32(o[k], o[k + 12]);
#pragma unroll
      for (int k = 0; k < 6; ++k) o[k] = tr16(o[k], o[k + 6]);
      LAS float* mg = (LAS float*)(L + L_MG) + w * 200;
#pragma unroll
      for (int k = 0; k < 3; ++k) mg[k * 64 + lane] = tr8(o[k], o[k + 3], r & 1);
      if (lane == 0) { mg[192] = M; mg[193] = l; }
    }
    lds_inc<true>(done);
    for (int spn = 0; lds_ld(done) < NS && spn < (1 << 22); ++spn) {}
    asm volatile("" ::: "memory");
    if (w < 3) {
      const LAS float* mg = (const LAS float*)(L + L_MG);
      float M = mg[192];
#pragma unroll
      for (int i = 1; i < NS; ++i) M = fmaxf(M, mg[i * 200 + 192]);
      float ls = 0.f, o = 0.f;
#pragma unroll
      for (int i = 0; i < NS; ++i) { const float e = __builtin_amdgcn_exp2f(mg[i * 200 + 192] - M); ls = fmaf(e, mg[i * 200 + 193], ls); o = fmaf(e, mg[i * 200 + tid], o); }
      gu64* px = (gu64*)(ws + WS_PX) + bid * 256;
      put(px + tid, tag, asu(o * __builtin_amdgcn_rcpf(ls)));
      if (tid == 0) put(px + 192, tag, asu(M + __builtin_amdgcn_logf(ls)));
    } else __builtin_amdgcn_s_sleep(6);
    static_assert(NS == 6, "six streaming waves");
    const int bb = w >= 3, di = (lane >> 5) * 12 + ((lane >> 4) & 1) * 6 + ((lane >> 3) & 1) * 3 + (w - 3 * bb);
    unsigned gv[8]; int go[8];
#pragma unroll
    for (int i = 0; i < 8; ++i) go[i] = WS_PX + (h * 8 + bb * 4 + (i & 3)) * 2048 + (i < 4 ? tid - bb * 192 : 192) * 8;
    {
      const u64 pv = __hip_atomic_load(pace, RLX_WG);
      const int tend = (int)fmaf((float)NU, asf(__builtin_amdgcn_readfirstlane((unsigned)(pv >> 32))), asf(__builtin_amdgcn_readfirstlane((unsigned)pv)));
      __builtin_amdgcn_s_sleep(6);
      const unsigned tb = now();
      while ((int)(now() - ts) < tend + TG1 && now() - tb < 300u) __builtin_amdgcn_s_sleep(1);
    }
    gather<0>(gv, go, wsr, tag, ts);
    { const float e0 = asf(gv[4]), e1 = asf(gv[5]), e2 = asf(gv[6]), e3 = asf(gv[7]), em = fmaxf(fmaxf(e0, e1), fmaxf(e2, e3));
      const float p0 = __builtin_amdgcn_exp2f(e0 - em), p1 = __builtin_amdgcn_exp2f(e1 - em), p2 = __builtin_amdgcn_exp2f(e2 - em), p3 = __builtin_amdgcn_exp2f(e3 - em);
      *(LAS u16*)(L + L_OA + (bb * DH + (di >> 3) * 64 + c * 8 + (di & 7)) * 2) =
          f2bf((p0 * asf(gv[0]) + p1 * asf(gv[1]) + p2 * asf(gv[2]) + p3 * asf(gv[3])) * __builtin_amdgcn_rcpf(p0 + p1 + p2 + p3));
    }
    barrier();
    if (w == 5) {
#pragma unroll
      for (int i = 0; i < 8; ++i) go[i] = (lane & 15) < 10 ? WS_RX + (j * 32 + h) * 2560 + i * 320 + (lane >> 4) * 80 + (lane & 15) * 8 : OOB;
      __builtin_amdgcn_s_sleep(PD2);
      gather<0>(gv, go, wsr, tag, ts);
      float t = 0.f;
#pragma unroll
      for (int i = 0; i < 8; ++i) t += asf(gv[i]);
      t = tr16(t, t); t = tr32(t, t);
      asm volatile("" : "+v"(xres), "+v"(b2v));
      const float g = rbf(rbf(dpp<SHL5>(t)) + asf(b2v << 16));
      const float gate = rbf(g * __builtin_amdgcn_rcpf(1.f + __expf(-g)));
      __builtin_amdgcn_raw_buffer_store_b16(f2bf(asf(xres << 16) + rbf(rbf(t) * gate)), __builtin_amdgcn_make_buffer_rsrc((void*)out, 0, NB * D * 2, RSRC3),
                                            lane < 5 ? oidx * 2 : OOB, 0, AUX_SC1);
      if (blockIdx.x == 0 && lane == 0) __hip_atomic_store((GAS unsigned*)(ws + WS_EPOCH), tag, RLX_AGENT);
    }
  } else {
    __builtin_amdgcn_s_setprio(3);
    const int a = w - NS;
    const unsigned epoch = __hip_atomic_load((GAS unsigned*)(ws + WS_EPOCH), RLX_AGENT);
    const int xb = lane >> 5, xl = lane & 31, xo = xb * (D * 2) + xl * 40;
    const __amdgpu_buffer_rsrc_t xr = __builtin_amdgcn_make_buffer_rsrc((void*)x, 0, NB * D * 2, RSRC3);
    const v4u x0 = __builtin_amdgcn_raw_buffer_load_b128(xr, xo, 0, 0), x1 = __builtin_amdgcn_raw_buffer_load_b128(xr, xo + 16, 0, 0);
    const v2u x2 = __builtin_amdgcn_raw_buffer_load_b64(xr, xo + 32, 0, 0);
    u16 xs[3];
#pragma unroll
    for (int i = 0; i < 3; ++i) xs[i] = __builtin_amdgcn_raw_buffer_load_b16(xr, xl + 32 * i < 80 ? xb * (D * 2) + (80 * j + xl + 32 * i) * 2 : OOB, 0, 0);
    v4u wa[10], wb[10];
    { const __amdgpu_buffer_rsrc_t ra = __builtin_amdgcn_make_buffer_rsrc((void*)wq, 0, D * H * DH * 2, RSRC3);
      const __amdgpu_buffer_rsrc_t rb = __builtin_amdgcn_make_buffer_rsrc((void*)(a ? w1 : wq), 0, a ? D * F * 2 : D * H * DH * 2, RSRC3);
      const int sa = H * DH * 2, sb = a ? F * 2 : H * DH * 2;
      const int va = r * sa + c * 16, vb = (a && c >= 6) ? OOB : r * sb + c * 16;
      const int oa = 80 * j * sa + h * (DH * 2) + a * 256, ob = 80 * j * sb + (a ? h * 96 : h * (DH * 2) + 128);
#pragma unroll
      for (int u = 0; u < 10; ++u) { wa[u] = __builtin_amdgcn_raw_buffer_load_b128(ra, va, oa + u * 8 * sa, AUX_NT); wb[u] = __builtin_amdgcn_raw_buffer_load_b128(rb, vb, ob + u * 8 * sb, AUX_NT); }
    }
    SCHED();
    unsigned tag = (unsigned)__builtin_amdgcn_readfirstlane(epoch) * 0x9E3779B1u + 0x7F4A7C15u;
    tag = tag ? tag : 1u;
    if (a == 0 && lane == 0) ctr[1] = tag;
    {
      f32x2 v[10];
#pragma unroll
      for (int k = 0; k < 4; ++k) { v[k] = unpk(x0[k]); v[4 + k] = unpk(x1[k]); }
      v[8] = unpk(x2[0]); v[9] = unpk(x2[1]);
      float s = 0.f, q = 0.f;
#pragma unroll
      for (int k = 0; k < 10; ++k) s += v[k][0] + v[k][1];
      s = sum16(s); s = tr16(s, s);
      const float mean = s * (1.f / D);
#pragma unroll
      for (int k = 0; k < 10; ++k) { const f32x2 d = v[k] - mean; q = fmaf(d[0], d[0], fmaf(d[1], d[1], q)); }
      q = sum16(q); q = tr16(q, q);
      const float mb = rbf(mean), sd = rbf(sqrtf(rbf(rbf(q * (1.f / D)) + 1e-5f)));
      LAS float* xn = (LAS float*)(L + L_XN) + (a * 2 + xb) * 96 + xl;
#pragma unroll
      for (int i = 0; i < 3; ++i) xn[32 * i] = rbf(rbf(bf2f(xs[i]) - mb) / sd);
    }
    asm volatile("s_waitcnt lgkmcnt(0)" ::: "memory");
    { f32x2 p2[16];
#pragma unroll
      for (int k = 0; k < 16; ++k) p2[k] = f32x2{0.f, 0.f};
      const LAS float* xn = (const LAS float*)(L + L_XN) + a * 192 + r;
#pragma unroll
      for (int u = 0; u < 10; ++u) { const float xa = xn[8 * u], xc = xn[96 + 8 * u];
#pragma unroll
        for (int k = 0; k < 4; ++k) {
          const f32x2 fa = unpk(wa[u][k]), fb = unpk(wb[u][k]);
          p2[k] = __builtin_elementwise_fma(f32x2{xa, xa}, fa, p2[k]); p2[4 + k] = __builtin_elementwise_fma(f32x2{xa, xa}, fb, p2[4 + k]);
          p2[8 + k] = __builtin_elementwise_fma(f32x2{xc, xc}, fa, p2[8 + k]); p2[12 + k] = __builtin_elementwise_fma(f32x2{xc, xc}, fb, p2[12 + k]);
        } }
      float pa[32];
#pragma unroll
      for (int k = 0; k < 16; ++k) { pa[2 * k] = p2[k][0]; pa[2 * k + 1] = p2[k][1]; }
#pragma unroll
      for (int k = 0; k < 16; ++k) pa[k] = tr32(pa[k], pa[k + 16]);
#pragma unroll
      for (int k = 0; k < 8; ++k) pa[k] = tr16(pa[k], pa[k + 8]);
      gu64* qx = (gu64*)(ws + WS_QX) + bid * 512 + (lane >> 5) * 256 + (2 * a + ((lane >> 4) & 1)) * 64 + (lane & 15);
#pragma unroll
      for (int t = 0; t < 4; ++t) put(qx + t * 16, tag, asu(tr8(pa[t], pa[t + 4], r & 1)));
    }
    const int col = (lane & 7) * 8 + ((lane >> 3) & 1) * 4 + (lane >> 4);
    if (a == 0) {
      unsigned gv[24]; int go[24];
#pragma unroll
      for (int i = 0; i < 24; ++i) go[i] = WS_QX + ((h * 8 + i / 3) * 512 + b * 256 + (i % 3) * 64 + lane) * 8;
      { const unsigned tq = max(QD, now() - ts + QX);
        while (now() - ts < tq) __builtin_amdgcn_s_sleep(2); }
      gather<0>(gv, go, wsr, tag, ts);
#pragma unroll
      for (int u = 0; u < 3; ++u) { float q = 0.f;
#pragma unroll
        for (int p = 0; p < 8; ++p) q += asf(gv[p * 3 + u]);
        *(LAS u16*)(L + L_Q + (u * 64 + col) * 2) = f2bf(q); }
    }
    barrier();
    __builtin_amdgcn_s_setprio(0);
    if (a == 0) {
      touch(wo + DH * h * D, wo + (DH * h + DH - 2) * D); touch(w2 + 48 * h * D, w2 + (48 * h + 46) * D); touch(b1, out);
      while ((int)lds_ld(ctr) < PH && now() - ts < SPIN_TICKS) __builtin_amdgcn_s_sleep(16);
      const bool live = (lane & 7) < 6;
      const float bias = bf2f(b1[48 * h + (live ? col : 0)]);
      unsigned gv[16]; int go[16];
#pragma unroll
      for (int i = 0; i < 16; ++i) go[i] = live ? WS_QX + ((h * 8) * 512 + i * 256 + 192 + lane) * 8 : OOB;
      gather<16>(gv, go, wsr, tag, ts);
      float hs[2] = {0.f, 0.f};
#pragma unroll
      for (int p = 0; p < 16; ++p) hs[p & 1] += asf(gv[p]);
      if (live) {
#pragma unroll
        for (int u = 0; u < 2; ++u) { float t = rbf(rbf(hs[u]) + bias); t = t > 0.f ? t : 0.f; *(LAS u16*)(L + L_HA + (u * 48 + col) * 2) = f2bf(t * t); } }
    } else {
      float lt = PINIT, l0 = (float)(int)(now() - ts) - (float)(NS * P0 + NS) * lt, up = 0.f, tp = 0.f, tb = 0.f;
      unsigned Tp = 0u, hp = 0u, uq = 0u;
      if (lane == 0) __hip_atomic_store(pace, ((u64)asu(lt) << 32) | asu(l0), RLX_WG);
      for (int rd = 0; rd < 8; ++rd) {
        const unsigned Tk = PT0 + rd * PDT;
        gu64* cl = (gu64*)(ws + WS_CT) + rd * 544;
        while (now() - ts < Tk && lds_ld(ctr) < NU - PSTOP) __builtin_amdgcn_s_sleep(8);
        const unsigned um = lds_ld(ctr), hm = lds_ld(done + 2), hw = min((hm - hp) / max(um - uq, 1u), 127u);
        hp = hm; uq = um;
        if (um >= NU - PSTOP) break;
        if (lane == 0) put(cl + blockIdx.x, tag, (ts << 16) | (hw << 9) | um);
        while (now() - ts < Tk + PLAG) __builtin_amdgcn_s_sleep(8);
        float su = 0.f, st = 0.f, sh = 0.f; int n = 0;
#pragma unroll
        for (int k = 0; k < 4; ++k) { const u64 v = __hip_atomic_load(cl + lane + 64 * k, RLX_AGENT); const bool ok = (unsigned)(v >> 32) == tag;
          su += ok ? (float)((unsigned)v & 0x1ffu) : 0.f; sh += ok ? (float)(((unsigned)v >> 9) & 127u) : 0.f; st += ok ? (float)((int)((unsigned)v - (ts << 16)) >> 16) : 0.f; n += __builtin_popcountll(__ballot(ok)); }
        if (n < 224) continue;
        su = sum16(su); su = tr16(su, su); su = tr32(su, su); st = sum16(st); st = tr16(st, st); st = tr32(st, st); sh = sum16(sh); sh = tr16(sh, sh); sh = tr32(sh, sh);
        const float ub = su / (float)n, tm = (float)(Tk - Tp) / (ub - up);
        const bool first = Tp == 0u;
        up = ub; Tp = Tk;
        if (first) continue;
        if (tb == 0.f) tp = tb = fminf(tm * PEPS, PCAP);
        else if (sh > PHI * (float)n) tp = tb = tp * PREC;
        else if (tm > tp * PTH) tp = fminf(tm * PTH, tp * PMX);
        else tp = fmaxf(tb, tp / PMX);
        const float tend = (float)Tk + st / (float)n + ((float)NU - ub) * tp, tnow = (float)(int)(now() - ts);
        const float ti = (tend - tnow) / ((float)NU - fminf((float)(lds_ld(ctr) + PA), (tnow - l0) / lt));
        if (ti > 0.f && ti < 1000.f) { lt = ti; l0 = tend - (float)NU * ti; if (lane == 0) __hip_atomic_store(pace, ((u64)asu(lt) << 32) | asu(l0), RLX_WG); }
      }
    }
    int vw[5], v2[4];
#pragma unroll
    for (int i = 0; i < 5; ++i) { const int id = i * 64 + lane, row = id / 10; vw[i] = row * (D * 2) + (id - row * 10) * 16; }
#pragma unroll
    for (int i = 0; i < 4; ++i) { const int id = (4 * a + i) * 64 + lane, row = id / 10; v2[i] = id < 480 ? row * (D * 2) + (id - row * 10) * 16 : OOB; }
    const __amdgpu_buffer_rsrc_t ro = __builtin_amdgcn_make_buffer_rsrc((void*)wo, 0, H * DH * D * 2, RSRC3), r2 = __builtin_amdgcn_make_buffer_rsrc((void*)w2, 0, F * D * 2, RSRC3);
    const int so = (DH * h + 96 * a) * (D * 2) + 160 * j, s2 = 48 * h * (D * 2) + 160 * j;
    const int vb = (lane >> 4) & 1, which = lane >> 5;
    gu64* rx[3];
#pragma unroll
    for (int t = 0; t < 3; ++t) {
      const int oi = vb * 80 + 16 * (3 * a + ((t == 2 && a) ? 1 : t)) + (lane & 15), hp = oi / 5;
      rx[t] = (gu64*)(ws + WS_RX) + ((j * 32 + hp) * 32 + h) * 10 + which * 5 + (oi - hp * 5);
    }
    asm volatile("" : "+v"(vw[0]), "+v"(vw[1]), "+v"(vw[2]), "+v"(vw[3]), "+v"(vw[4]), "+v"(v2[0]), "+v"(v2[1]), "+v"(v2[2]), "+v"(v2[3]), "+v"(rx[0]), "+v"(rx[1]), "+v"(rx[2]));
    while (lds_ld(ctr) < NU && now() - ts < SPIN_TICKS) __builtin_amdgcn_s_sleep(2);
#pragma unroll
    for (int i = 0; i < 19; ++i) {
      if (i < 4) __builtin_amdgcn_raw_ptr_buffer_load_lds(r2, (LAS void*)(L + L_WO + (30 + 4 * a + i) * 1024), 16, v2[i < 4 ? i : 0], s2, 0, AUX_NT);
      else __builtin_amdgcn_raw_ptr_buffer_load_lds(ro, (LAS void*)(L + L_WO + (15 * a + i - 4) * 1024), 16, vw[(i + 1) % 5], so + ((i - 4) / 5) * 32 * (D * 2), 0, AUX_NT);
      SCHED(); VMCNT(WPACE); }
    VMCNT(0);
    lds_inc<true>(done + 1);
    while (lds_ld(done + 1) < 2 && now() - ts < SPIN_TICKS) {}
    asm volatile("" ::: "memory");
    const LAS unsigned char* base = L + L_WO + (((lane & 15) >> 2) + 8 * (lane >> 4)) * 160 + a * 96 + (lane & 3) * 8;
    const LAS unsigned char* ah = L + L_HA + (lane & 1) * 96;
#define TRB(off) __builtin_amdgcn_ds_read_tr16_b64_v4i16((LAS v4s*)(base + (off)))
#define BFRAG(t, k) __builtin_bit_cast(v8bf, __builtin_shufflevector(TRB((t) * 32 + (k) * 32 * 160), TRB((t) * 32 + (k) * 32 * 160 + 4 * 160), 0, 1, 2, 3, 4, 5, 6, 7))
    const f32x4 Z = {0.f, 0.f, 0.f, 0.f};
    v8bf Bf[3][6]; float gsum[3];
#pragma unroll
    for (int t = 0; t < 3; ++t) {
      const int tt = (t == 2 && a) ? 1 : t;
#pragma unroll
      for (int k = 0; k < 6; ++k) Bf[t][k] = BFRAG(tt, k);
      f32x4 Ga = __builtin_amdgcn_mfma_f32_16x16x32_bf16(*(const LAS v8bf*)(ah + (lane >> 4) * 16), BFRAG(tt, 6), Z, 0, 0, 0);
      f32x4 Gb = __builtin_amdgcn_mfma_f32_16x16x16bf16_1k(*(const LAS v4s*)(ah + 64 + (lane >> 4) * 8), TRB(tt * 32 + 7 * 32 * 160 - 4 * (lane >> 4) * 160), Z, 0, 0, 0);
      asm volatile("" : "+v"(Ga), "+v"(Gb));
      gsum[t] = vb ? Ga[1] + Gb[1] : Ga[0] + Gb[0];
    }
#pragma unroll
    for (int t = 0; t < 3; ++t) asm volatile("" : "+v"(Bf[t][0]), "+v"(Bf[t][1]), "+v"(Bf[t][2]), "+v"(Bf[t][3]), "+v"(Bf[t][4]), "+v"(Bf[t][5]), "+v"(gsum[t]));
    barrier();
    const LAS unsigned char* ao = L + L_OA + (lane & 1) * (DH * 2) + (lane >> 4) * 16;
    v8bf A[6];
#pragma unroll
    for (int k = 0; k < 6; ++k) A[k] = *(const LAS v8bf*)(ao + k * 64);
    SCHED();
    f32x4 S[3] = {Z, Z, Z};
#pragma unroll
    for (int k = 0; k < 6; ++k)
#pragma unroll
      for (int t = 0; t < 3; ++t) S[t] = __builtin_amdgcn_mfma_f32_16x16x32_bf16(A[k], Bf[t][k], S[t], 0, 0, 0);
    asm volatile("" : "+v"(S[0]), "+v"(S[1]), "+v"(S[2]));
#pragma unroll
    for (int t = 0; t < 3; ++t) { float s0 = S[t][0], s1 = S[t][1]; asm volatile("" : "+v"(s0), "+v"(s1));
      float v = vb ? s1 : s0; v = which ? gsum[t] : v; asm volatile("" : "+v"(v));
      if (t < 2 || a == 0) put(rx[t], tag, asu(v)); }
  }
}

extern "C" void kernel_launch(void* const* d_in, const int* in_sizes, int n_in, void* d_out, int out_size, void* d_ws, size_t ws_size, hipStream_t stream) {
  fused_decode<<<256, NTHR, 0, stream>>>((const u16*)d_in[0], (const u16*)d_in[1], (const u16*)d_in[2], (const u16*)d_in[3], (const u16*)d_in[4],
                                         (const u16*)d_in[6], (const u16*)d_in[7], (const u16*)d_in[8], (const u16*)d_in[9], (u16*)d_out, (unsigned char*)d_ws);
}
```

```cpp
#include <hip/hip_runtime.h>
#include <hip/hip_bf16.h>
#include <stdint.h>

#define GAS __attribute__((address_space(1)))
#define LAS __attribute__((address_space(3)))
#define RLX_AGENT __ATOMIC_RELAXED, __HIP_MEMORY_SCOPE_AGENT
#define RLX_WG __ATOMIC_RELAXED, __HIP_MEMORY_SCOPE_WORKGROUP
typedef unsigned v4u __attribute__((ext_vector_type(4)));
typedef unsigned v2u __attribute__((ext_vector_type(2)));
typedef short v4s __attribute__((ext_vector_type(4)));
typedef __bf16 v8bf __attribute__((ext_vector_type(8)));
typedef float f32x4 __attribute__((ext_vector_type(4)));
typedef float f32x2 __attribute__((ext_vector_type(2)));
typedef __bf16 bf16x2 __attribute__((ext_vector_type(2)));
typedef unsigned short u16;
typedef unsigned long long u64;
typedef GAS u64 gu64;

constexpr int NB = 2, D = 640, H = 32, SK = 8192, DH = 192, F = 1536;
constexpr int NS = 6, NTHR = (NS + 2) * 64;
constexpr int NU = 256, NI = 2 * NU;
constexpr int P0 = 8;
constexpr int PW = 2;
constexpr int ROT = 4;
constexpr unsigned QD = 450;
constexpr unsigned QX = 45;
constexpr int PH = 250;
constexpr int PL = 300;
constexpr int TG1 = 192;
constexpr int PD2 = 16;
constexpr int WPACE = 7;
constexpr unsigned PT0 = 1300;
constexpr unsigned PDT = 500;
constexpr unsigned PLAG = 180;
constexpr unsigned PSTOP = 60;
constexpr unsigned PA = 1;
constexpr float PDL = 3.f;
constexpr float PINIT = 10.35f;
constexpr float PCAP = 11.95f;
constexpr float PVF = 10.9f;
constexpr float PTR = 15.f;
constexpr float PTH = 1.002f;
constexpr float PHL = 8.f;
constexpr float PT2 = 1.03f;
constexpr float PEPS = 1.015f;
constexpr float PEPV = 1.018f;
constexpr float PFL = 0.995f;
constexpr float PHT = 24.f;
constexpr float PG = 0.0015f;
constexpr float PG2 = 0.003f;
constexpr float PMX = 0.035f;
constexpr float PNU = 380.f;
constexpr float PSL = 30.f;
constexpr int AUX_NT = 2, AUX_SC1 = 16, RSRC3 = 0x00020000, OOB = 0x7ffffff0;
constexpr unsigned SPIN_TICKS = 2000000u;
constexpr int WS_EPOCH = 0;
constexpr int WS_QX = 4096;
constexpr int WS_PX = WS_QX + 256 * 512 * 8;
constexpr int WS_RX = WS_PX + 256 * 256 * 8;
constexpr int WS_CT = WS_RX + 8 * 32 * 320 * 8;
constexpr int WS_END = WS_CT + 12 * 544 * 8;
constexpr int L_XN = 0;
constexpr int L_Q = L_XN + 4 * 96 * 4;
constexpr int L_HA = L_Q + 384;
constexpr int L_OA = L_HA + 192;
constexpr int L_CT = L_OA + 768;
constexpr int L_ML = L_CT + 32;
constexpr int L_MG = L_ML + 96;
constexpr int L_SC = L_MG + NS * 768;
constexpr int L_WO = L_SC + NU * 32 + 32;
constexpr int L_END = 81 * 1024;
static_assert(L_WO + 38 * 1024 <= L_END, "LDS");
static_assert(L_WO % 16 == 0 && L_OA % 16 == 0 && L_HA % 16 == 0 && L_Q % 16 == 0 && L_CT % 16 == 0, "alignment");

__device__ __forceinline__ float asf(unsigned v) { return __builtin_bit_cast(float, v); }
__device__ __forceinline__ unsigned asu(float v) { return __builtin_bit_cast(unsigned, v); }
__device__ __forceinline__ float bf2f(u16 b) { return asf((unsigned)b << 16); }
__device__ __forceinline__ u16 f2bf(float f) { return __builtin_bit_cast(u16, (__bf16)f); }
__device__ __forceinline__ float rbf(float f) { return bf2f(f2bf(f)); }
__device__ __forceinline__ f32x2 unpk(unsigned v) { return f32x2{asf(v << 16), asf(v & 0xffff0000u)}; }
__device__ __forceinline__ float dot2(unsigned k, unsigned q, float acc) {
  return __builtin_amdgcn_fdot2_f32_bf16(__builtin_bit_cast(bf16x2, k), __builtin_bit_cast(bf16x2, q), acc, false);
}
template <int CTRL> __device__ __forceinline__ float dpp(float x) { return asf(__builtin_amdgcn_mov_dpp((int)asu(x), CTRL, 0xf, 0xf, true)); }
constexpr int XOR1 = 0xB1, XOR2 = 0x4E, XOR7 = 0x141, ROR8 = 0x128, ROR4 = 0x124, ROR2 = 0x122, ROR1 = 0x121, SHL5 = 0x105;
__device__ __forceinline__ float tr32(float x, float y) {
  auto s = __builtin_amdgcn_permlane32_swap(asu(x), asu(y), false, false); return asf(s[0]) + asf(s[1]);
}
__device__ __forceinline__ float tr16(float x, float y) {
  auto s = __builtin_amdgcn_permlane16_swap(asu(x), asu(y), false, false); return asf(s[0]) + asf(s[1]);
}
__device__ __forceinline__ float tr8(float x, float y, bool up) {
  const float keep = up ? y : x, send = up ? x : y; return keep + dpp<ROR8>(send);
}
__device__ __forceinline__ float sum16(float s) { s += dpp<ROR1>(s); s += dpp<ROR2>(s); s += dpp<ROR4>(s); return s + dpp<ROR8>(s); }
__device__ __forceinline__ float max16(float x) { auto s = __builtin_amdgcn_permlane16_swap(asu(x), asu(x), false, false); return fmaxf(asf(s[0]), asf(s[1])); }
__device__ __forceinline__ float max32(float x) { auto s = __builtin_amdgcn_permlane32_swap(asu(x), asu(x), false, false); return fmaxf(asf(s[0]), asf(s[1])); }
__device__ __forceinline__ unsigned now() { return (unsigned)__builtin_amdgcn_s_memrealtime(); }
__device__ __forceinline__ void barrier() { asm volatile("s_waitcnt lgkmcnt(0)\n\ts_barrier" ::: "memory"); }
__device__ __forceinline__ void put(gu64* g, unsigned tag, unsigned v) { __hip_atomic_store(g, ((u64)tag << 32) | v, RLX_AGENT); }
__device__ __forceinline__ unsigned lds_ld(LAS unsigned* p) { return __builtin_amdgcn_readfirstlane(__hip_atomic_load(p, RLX_WG)); }
template <bool ORDERED> __device__ __forceinline__ void lds_inc(LAS unsigned* p, unsigned v = 1u) {
  if (ORDERED) asm volatile("s_waitcnt lgkmcnt(0)" ::: "memory");
  asm volatile("s_mov_b64 exec, 1\n\tds_add_u32 %0, %1\n\ts_mov_b64 exec, -1" :: "v"((unsigned)(uintptr_t)p), "v"(v) : "memory");
}
__device__ __forceinline__ void touch(const void* p, const void* q) {
  unsigned t0, t1; asm volatile("s_load_dword %0, %2, 0x0\n\ts_load_dword %1, %3, 0x0\n\ts_waitcnt lgkmcnt(0)" : "=&s"(t0), "=&s"(t1) : "s"(p), "s"(q) : "memory");
}
#define SCHED() do { asm volatile("" ::: "memory"); __builtin_amdgcn_sched_barrier(0); } while (0)
#define VMCNT(n) asm volatile("s_waitcnt vmcnt(%0)" :: "n"(n) : "memory")

template <int SLEEP, int N> __device__ __forceinline__ void gather(unsigned (&val)[N], int (&off)[N], __amdgpu_buffer_rsrc_t rs, unsigned tag, unsigned ts) {
#pragma unroll
  for (int i = 0; i < N; ++i) val[i] = 0u;
  for (;;) {
    v2u in[N];
    asm volatile("" ::: "memory");
#pragma unroll
    for (int i = 0; i < N; ++i) in[i] = __builtin_amdgcn_raw_buffer_load_b64(rs, off[i], 0, AUX_SC1);
    int left = OOB;
#pragma unroll
    for (int i = 0; i < N; ++i) { const bool got = in[i][1] == tag; val[i] = got ? in[i][0] : val[i]; off[i] = got ? OOB : off[i]; left = min(left, off[i]); }
    if (__all(left == OOB) || now() - ts > SPIN_TICKS) break;
    if (SLEEP) __builtin_amdgcn_s_sleep(SLEEP);
  }
}

__global__ void __launch_bounds__(NTHR, 2)
fused_decode(const u16* x, const u16* kc, const u16* vc, const u16* wq, const u16* wo,
             const u16* w1, const u16* b1, const u16* w2, const u16* b2, u16* out, unsigned char* ws) {
  __shared__ __attribute__((aligned(16))) unsigned char lds_raw[L_END];
  LAS unsigned char* L = (LAS unsigned char*)lds_raw;
  const int bid = (blockIdx.x & 7) * 32 + (blockIdx.x >> 3), h = bid >> 3, j = bid & 7, b = j >> 2, sp = j & 3;
  const int tid = threadIdx.x, lane = tid & 63, w = __builtin_amdgcn_readfirstlane(tid >> 6), r = lane >> 3, c = lane & 7;
  LAS unsigned* ctr = (LAS unsigned*)(L + L_CT);
  LAS u64* pace = (LAS u64*)(L + L_CT + 8);
  LAS unsigned* done = (LAS unsigned*)(L + L_CT + 16);
  const unsigned ts = now();
  const int rot = (b * H + h) * ROT;
  const __amdgpu_buffer_rsrc_t wsr = __builtin_amdgcn_make_buffer_rsrc((void*)ws, 0, WS_END, RSRC3);

  if (__builtin_expect(w < NS, 1)) {
    if (tid == 0) { *ctr = NS * P0; *pace = 0ull; done[0] = 0u; done[1] = 0u; done[2] = 0u; done[3] = 0u; }
    const int rbase = ((b * H + h) * SK + sp * 8) * (DH * 2), voff = r * (DH * 2) + c * 16;
#define LD(dst, p, t) { const __amdgpu_buffer_rsrc_t rs_ = __builtin_amdgcn_make_buffer_rsrc((void*)(p), 0, NB * H * SK * DH * 2, RSRC3); \
    const int so_ = rbase + (((t) + rot) & (NU - 1)) * (32 * DH * 2); \
    _Pragma("unroll") for (int u = 0; u < 3; ++u) dst[u] = __builtin_amdgcn_raw_buffer_load_b128(rs_, voff + u * 128, so_, AUX_NT); SCHED(); }
    const int oi = 5 * h + (lane < 5 ? lane : 0), ocol = 80 * j + oi % 80, oidx = (oi / 80) * D + ocol;
    v4u R[P0][3];
#pragma unroll
    for (int i = 0; i < P0; ++i) {
      LD(R[i], kc, w + i * NS)
      if (i == 0) {
        if (w == 5) touch(ws + 2048, ws + WS_END);
        if (w == 4) touch(wq + (size_t)80 * j * H * DH + h * DH, wq + (size_t)(80 * j + 79) * H * DH + h * DH);
        if (w == 3) touch(w1 + 80 * j * F + h * 48, w1 + (80 * j + 79) * F + h * 48);
        if (w == 2) touch(x, b2);
      }
      if (i + 1 < P0) VMCNT(PW);
    }
    unsigned xres = __builtin_amdgcn_raw_buffer_load_b16(__builtin_amdgcn_make_buffer_rsrc((void*)x, 0, NB * D * 2, RSRC3), w == 5 ? oidx * 2 : OOB, 0, 0);
    unsigned b2v = __builtin_amdgcn_raw_buffer_load_b16(__builtin_amdgcn_make_buffer_rsrc((void*)b2, 0, D * 2, RSRC3), w == 5 ? ocol * 2 : OOB, 0, 0);
    barrier();
    const unsigned tag = lds_ld(ctr + 1);
    v4u qv[3];
#pragma unroll
    for (int u = 0; u < 3; ++u) qv[u] = *(LAS v4u*)(L + L_Q + u * 128 + c * 16);
    constexpr float C2 = 0.07216878364870322f * 1.4426950408889634f;
    LAS float* sc = (LAS float*)(L + L_SC) + r;
    LAS float* ml = (LAS float*)(L + L_ML);
    float m = -3.0e38f;
#define QK(X, t) { float d0 = 0.f, d1 = 0.f; \
      _Pragma("unroll") for (int u = 0; u < 3; ++u) { d0 = dot2(X[u][0], qv[u][0], d0); d1 = dot2(X[u][1], qv[u][1], d1); d0 = dot2(X[u][2], qv[u][2], d0); d1 = dot2(X[u][3], qv[u][3], d1); } \
      float s = d0 + d1; s += dpp<XOR1>(s); s += dpp<XOR2>(s); s += dpp<XOR7>(s); s *= C2; m = fmaxf(m, s); sc[(t) * 8] = s; asm volatile("" : "+v"(m)); SCHED(); }
    f32x2 acc[12];
#define PV(X, t) { const float p = sc[(t) * 8]; \
      _Pragma("unroll") for (int u = 0; u < 3; ++u) { SCHED(); \
        _Pragma("unroll") for (int k = 0; k < 4; ++k) acc[u * 4 + k] = __builtin_elementwise_fma(f32x2{p, p}, unpk(X[u][k]), acc[u * 4 + k]); \
        asm volatile("" : "+v"(acc[u * 4]), "+v"(acc[u * 4 + 1]), "+v"(acc[u * 4 + 2]), "+v"(acc[u * 4 + 3])); } SCHED(); }
    const int n = w < 2 ? 44 : 42;
#define UNIT(k) ((k) < 42 ? w + NS * (k) : 168 + w + 2 * (k))
    const float step = (float)NU / (float)n, lead = (float)w * (1.f / NS);
    u64 line = 0ull;
#define REQ(X, V, k) { \
      const float due = fmaf(fmaf((float)(k) + lead, step, (float)(V * NU)), asf(__builtin_amdgcn_readfirstlane((unsigned)(line >> 32))), \
                             asf(__builtin_amdgcn_readfirstlane((unsigned)line))); \
      float early = due - (float)(int)(now() - ts); \
      if (early > 0.f) { lds_inc<false>(done + 3, (unsigned)early); \
        _Pragma("unroll 1") for (int spn = 0; spn < 512 && early > 0.f; ++spn) { __builtin_amdgcn_s_sleep(1); early = due - (float)(int)(now() - ts); } } \
      SCHED(); LD(X, V ? vc : kc, UNIT(k)) lds_inc<false>(ctr); line = __hip_atomic_load(pace, RLX_WG); SCHED(); }
    static_assert(P0 % 2 == 0 && P0 + 2 <= 42 && NS == 6 && NU == 256, "even counts: two items in flight, in R[0] and R[1]");
    QK(R[0], w) REQ(R[0], 0, P0) QK(R[1], w + NS) REQ(R[1], 0, P0 + 1)
#pragma unroll
    for (int i = 2; i < P0; ++i) QK(R[i], w + i * NS)
    for (int k = P0; k + 2 < n; k += 2) { QK(R[0], UNIT(k)) REQ(R[0], 0, k + 2) QK(R[1], UNIT(k + 1)) REQ(R[1], 0, k + 3) }
    QK(R[0], UNIT(n - 2)) REQ(R[0], 1, 0) QK(R[1], UNIT(n - 1)) REQ(R[1], 1, 1)
    {
      float M = fmaxf(m, dpp<ROR8>(m)); M = max16(M); M = max32(M);
      float l = 0.f;
#pragma unroll
      for (int i = 0; i < 6; ++i) { const int k = 8 * i + r;
        LAS float* q = (LAS float*)(L + L_SC) + (k < n ? UNIT(k) : NU) * 8 + c;
        const float p = __builtin_amdgcn_exp2f(*q - M); *q = p; l += k < n ? p : 0.f; }
      l = sum16(l); l = tr16(l, l); l = tr32(l, l);
      if (lane == 0) { ml[w] = M; ml[8 + w] = l; }
      lds_inc<true>(done);
    }
#pragma unroll
    for (int k = 0; k < 12; ++k) acc[k] = f32x2{0.f, 0.f};
    for (int k = 0; k + 2 < n; k += 2) { PV(R[0], UNIT(k)) REQ(R[0], 1, k + 2) PV(R[1], UNIT(k + 1)) REQ(R[1], 1, k + 3) }
    PV(R[0], UNIT(n - 2)) PV(R[1], UNIT(n - 1))
    {
      float o[24];
#pragma unroll
      for (int k = 0; k < 12; ++k) { o[2 * k] = acc[k][0]; o[2 * k + 1] = acc[k][1]; }
#pragma unroll
      for (int k = 0; k < 12; ++k) o[k] = tr32(o[k], o[k + 12]);
#pragma unroll
      for (int k = 0; k < 6; ++k) o[k] = tr16(o[k], o[k + 6]);
      LAS float* mg = (LAS float*)(L + L_MG) + w * 192;
#pragma unroll
      for (int k = 0; k < 3; ++k) mg[k * 64 + lane] = tr8(o[k], o[k + 3], r & 1);
    }
    lds_inc<true>(done + 1);
    for (int spn = 0; lds_ld(done + 1) < NS + 1 && spn < (1 << 22); ++spn) {}
    asm volatile("" ::: "memory");
    if (w < 3) {
      const LAS float* mg = (const LAS float*)(L + L_MG);
      float o = 0.f;
#pragma unroll
      for (int i = 0; i < NS; ++i) o = fmaf(ml[16 + i], mg[i * 192 + tid], o);
      put((gu64*)(ws + WS_PX) + bid * 256 + tid, tag, asu(o));
    } else __builtin_amdgcn_s_sleep(4);
    static_assert(NS == 6, "six streaming waves");
    const int bb = w >= 3, di = (lane >> 5) * 12 + ((lane >> 4) & 1) * 6 + ((lane >> 3) & 1) * 3 + (w - 3 * bb);
    unsigned g4[4]; int o4[4];
#pragma unroll
    for (int i = 0; i < 4; ++i) o4[i] = WS_PX + (h * 8 + bb * 4 + i) * 2048 + (tid - bb * 192) * 8;
    {
      const u64 pv = __hip_atomic_load(pace, RLX_WG);
      const int tend = (int)fmaf((float)NI, asf(__builtin_amdgcn_readfirstlane((unsigned)(pv >> 32))), asf(__builtin_amdgcn_readfirstlane((unsigned)pv)));
      __builtin_amdgcn_s_sleep(6);
      const unsigned tb = now();
      while ((int)(now() - ts) < tend + TG1 && now() - tb < 300u) __builtin_amdgcn_s_sleep(1);
    }
    gather<0>(g4, o4, wsr, tag, ts);
    *(LAS u16*)(L + L_OA + (bb * DH + (di >> 3) * 64 + c * 8 + (di & 7)) * 2) = f2bf((asf(g4[0]) + asf(g4[1])) + (asf(g4[2]) + asf(g4[3])));
    barrier();
    if (w == 5) {
      unsigned gv[8]; int go[8];
#pragma unroll
      for (int i = 0; i < 8; ++i) go[i] = (lane & 15) < 10 ? WS_RX + (j * 32 + h) * 2560 + i * 320 + (lane >> 4) * 80 + (lane & 15) * 8 : OOB;
      __builtin_amdgcn_s_sleep(PD2);
      gather<0>(gv, go, wsr, tag, ts);
      float t = 0.f;
#pragma unroll
      for (int i = 0; i < 8; ++i) t += asf(gv[i]);
      t = tr16(t, t); t = tr32(t, t);
      asm volatile("" : "+v"(xres), "+v"(b2v));
      const float g = rbf(rbf(dpp<SHL5>(t)) + asf(b2v << 16));
      const float gate = rbf(g * __builtin_amdgcn_rcpf(1.f + __expf(-g)));
      __builtin_amdgcn_raw_buffer_store_b16(f2bf(asf(xres << 16) + rbf(rbf(t) * gate)), __builtin_amdgcn_make_buffer_rsrc((void*)out, 0, NB * D * 2, RSRC3),
                                            lane < 5 ? oidx * 2 : OOB, 0, AUX_SC1);
      if (blockIdx.x == 0 && lane == 0) __hip_atomic_store((GAS unsigned*)(ws + WS_EPOCH), tag, RLX_AGENT);
    }
  } else {
    __builtin_amdgcn_s_setprio(3);
    const int a = w - NS;
    const unsigned epoch = __hip_atomic_load((GAS unsigned*)(ws + WS_EPOCH), RLX_AGENT);
    const int xb = lane >> 5, xl = lane & 31, xo = xb * (D * 2) + xl * 40;
    const __amdgpu_buffer_rsrc_t xr = __builtin_amdgcn_make_buffer_rsrc((void*)x, 0, NB * D * 2, RSRC3);
    const v4u x0 = __builtin_amdgcn_raw_buffer_load_b128(xr, xo, 0, 0), x1 = __builtin_amdgcn_raw_buffer_load_b128(xr, xo + 16, 0, 0);
    const v2u x2 = __builtin_amdgcn_raw_buffer_load_b64(xr, xo + 32, 0, 0);
    u16 xs[3];
#pragma unroll
    for (int i = 0; i < 3; ++i) xs[i] = __builtin_amdgcn_raw_buffer_load_b16(xr, xl + 32 * i < 80 ? xb * (D * 2) + (80 * j + xl + 32 * i) * 2 : OOB, 0, 0);
    v4u wa[10], wb[10];
    { const __amdgpu_buffer_rsrc_t ra = __builtin_amdgcn_make_buffer_rsrc((void*)wq, 0, D * H * DH * 2, RSRC3);
      const __amdgpu_buffer_rsrc_t rb = __builtin_amdgcn_make_buffer_rsrc((void*)(a ? w1 : wq), 0, a ? D * F * 2 : D * H * DH * 2, RSRC3);
      const int sa = H * DH * 2, sb = a ? F * 2 : H * DH * 2;
      const int va = r * sa + c * 16, vb = (a && c >= 6) ? OOB : r * sb + c * 16;
      const int oa = 80 * j * sa + h * (DH * 2) + a * 256, ob = 80 * j * sb + (a ? h * 96 : h * (DH * 2) + 128);
#pragma unroll
      for (int u = 0; u < 10; ++u) { wa[u] = __builtin_amdgcn_raw_buffer_load_b128(ra, va, oa + u * 8 * sa, AUX_NT); wb[u] = __builtin_amdgcn_raw_buffer_load_b128(rb, vb, ob + u * 8 * sb, AUX_NT); }
    }
    SCHED();
    unsigned tag = (unsigned)__builtin_amdgcn_readfirstlane(epoch) * 0x9E3779B1u + 0x7F4A7C15u;
    tag = tag ? tag : 1u;
    if (a == 0 && lane == 0) ctr[1] = tag;
    {
      f32x2 v[10];
#pragma unroll
      for (int k = 0; k < 4; ++k) { v[k] = unpk(x0[k]); v[4 + k] = unpk(x1[k]); }
      v[8] = unpk(x2[0]); v[9] = unpk(x2[1]);
      float s = 0.f, q = 0.f;
#pragma unroll
      for (int k = 0; k < 10; ++k) s += v[k][0] + v[k][1];
      s = sum16(s); s = tr16(s, s);
      const float mean = s * (1.f / D);
#pragma unroll
      for (int k = 0; k < 10; ++k) { const f32x2 d = v[k] - mean; q = fmaf(d[0], d[0], fmaf(d[1], d[1], q)); }
      q = sum16(q); q = tr16(q, q);
      const float mb = rbf(mean), sd = rbf(sqrtf(rbf(rbf(q * (1.f / D)) + 1e-5f)));
      LAS float* xn = (LAS float*)(L + L_XN) + (a * 2 + xb) * 96 + xl;
#pragma unroll
      for (int i = 0; i < 3; ++i) xn[32 * i] = rbf(rbf(bf2f(xs[i]) - mb) / sd);
    }
    asm volatile("s_waitcnt lgkmcnt(0)" ::: "memory");
    { f32x2 p2[16];
#pragma unroll
      for (int k = 0; k < 16; ++k) p2[k] = f32x2{0.f, 0.f};
      const LAS float* xn = (const LAS float*)(L + L_XN) + a * 192 + r;
#pragma unroll
      for (int u = 0; u < 10; ++u) { const float xa = xn[8 * u], xc = xn[96 + 8 * u];
#pragma unroll
        for (int k = 0; k < 4; ++k) {
          const f32x2 fa = unpk(wa[u][k]), fb = unpk(wb[u][k]);
          p2[k] = __builtin_elementwise_fma(f32x2{xa, xa}, fa, p2[k]); p2[4 + k] = __builtin_elementwise_fma(f32x2{xa, xa}, fb, p2[4 + k]);
          p2[8 + k] = __builtin_elementwise_fma(f32x2{xc, xc}, fa, p2[8 + k]); p2[12 + k] = __builtin_elementwise_fma(f32x2{xc, xc}, fb, p2[12 + k]);
        } }
      float pa[32];
#pragma unroll
      for (int k = 0; k < 16; ++k) { pa[2 * k] = p2[k][0]; pa[2 * k + 1] = p2[k][1]; }
#pragma unroll
      for (int k = 0; k < 16; ++k) pa[k] = tr32(pa[k], pa[k + 16]);
#pragma unroll
      for (int k = 0; k < 8; ++k) pa[k] = tr16(pa[k], pa[k + 8]);
      gu64* qx = (gu64*)(ws + WS_QX) + bid * 512 + (lane >> 5) * 256 + (2 * a + ((lane >> 4) & 1)) * 64 + (lane & 15);
#pragma unroll
      for (int t = 0; t < 4; ++t) put(qx + t * 16, tag, asu(tr8(pa[t], pa[t + 4], r & 1)));
    }
    const int col = (lane & 7) * 8 + ((lane >> 3) & 1) * 4 + (lane >> 4);
    if (a == 0) {
      unsigned gv[24]; int go[24];
#pragma unroll
      for (int i = 0; i < 24; ++i) go[i] = WS_QX + ((h * 8 + i / 3) * 512 + b * 256 + (i % 3) * 64 + lane) * 8;
      { const unsigned tq = max(QD, now() - ts + QX);
        while (now() - ts < tq) __builtin_amdgcn_s_sleep(2); }
      gather<0>(gv, go, wsr, tag, ts);
#pragma unroll
      for (int u = 0; u < 3; ++u) { float q = 0.f;
#pragma unroll
        for (int p = 0; p < 8; ++p) q += asf(gv[p * 3 + u]);
        *(LAS u16*)(L + L_Q + (u * 64 + col) * 2) = f2bf(q); }
    }
    barrier();
    __builtin_amdgcn_s_setprio(0);
    const __amdgpu_buffer_rsrc_t ro = __builtin_amdgcn_make_buffer_rsrc((void*)wo, 0, H * DH * D * 2, RSRC3), r2 = __builtin_amdgcn_make_buffer_rsrc((void*)w2, 0, F * D * 2, RSRC3);
    const int so = DH * h * (D * 2) + 160 * j, s2 = 48 * h * (D * 2) + 160 * j;
    auto woff = [](int id) { const int row = id / 10; return row * (D * 2) + (id - row * 10) * 16; };
    if (a == 0) {
      { const unsigned char* vp = (const unsigned char*)vc + (size_t)(b * H + h) * (SK * DH * 2);
        touch(vp + (rot & (NU - 1)) * (32 * DH * 2), b1); }
      touch(wo + DH * h * D, wo + (DH * h + DH - 2) * D); touch(w2 + 48 * h * D, w2 + (48 * h + 46) * D); touch(out, out);
      while ((int)lds_ld(ctr) < PH && now() - ts < SPIN_TICKS) __builtin_amdgcn_s_sleep(8);
#pragma unroll 1
      for (int n = 0; n < 8; ++n) {
        __builtin_amdgcn_raw_ptr_buffer_load_lds(r2, (LAS void*)(L + L_WO + (30 + n) * 1024), 16, n * 64 + lane < 480 ? woff(n * 64 + lane) : OOB, s2, 0, AUX_NT);
        __builtin_amdgcn_s_sleep(3);
      }
      const bool live = (lane & 7) < 6;
      const float bias = bf2f(b1[48 * h + (live ? col : 0)]);
      unsigned gv[16]; int go[16];
#pragma unroll
      for (int i = 0; i < 16; ++i) go[i] = live ? WS_QX + ((h * 8) * 512 + i * 256 + 192 + lane) * 8 : OOB;
      gather<16>(gv, go, wsr, tag, ts);
      float hs[2] = {0.f, 0.f};
#pragma unroll
      for (int p = 0; p < 16; ++p) hs[p & 1] += asf(gv[p]);
      if (live) {
#pragma unroll
        for (int u = 0; u < 2; ++u) { float t = rbf(rbf(hs[u]) + bias); t = t > 0.f ? t : 0.f; *(LAS u16*)(L + L_HA + (u * 48 + col) * 2) = f2bf(t * t); } }
      while (lds_ld(done) < NS && now() - ts < SPIN_TICKS) __builtin_amdgcn_s_sleep(8);
      asm volatile("" ::: "memory");
      LAS float* ml = (LAS float*)(L + L_ML);
      const float mw = ml[lane < NS ? lane : 0];
      { float M = ml[0], ls = 0.f;
#pragma unroll
        for (int i = 1; i < NS; ++i) M = fmaxf(M, ml[i]);
#pragma unroll
        for (int i = 0; i < NS; ++i) ls = fmaf(__builtin_amdgcn_exp2f(ml[i] - M), ml[8 + i], ls);
        if (lane == 0) put((gu64*)(ws + WS_PX) + bid * 256 + 192, tag, asu(M + __builtin_amdgcn_logf(ls))); }
      while ((int)lds_ld(ctr) < PL && now() - ts < SPIN_TICKS) __builtin_amdgcn_s_sleep(16);
      { unsigned ev[4]; int eo[4];
#pragma unroll
        for (int i = 0; i < 4; ++i) eo[i] = WS_PX + (h * 8 + b * 4 + i) * 2048 + 192 * 8;
        gather<16>(ev, eo, wsr, tag, ts);
        const float e0 = asf(ev[0]), e1 = asf(ev[1]), e2 = asf(ev[2]), e3 = asf(ev[3]), em = fmaxf(fmaxf(e0, e1), fmaxf(e2, e3));
        const float sum = (__builtin_amdgcn_exp2f(e0 - em) + __builtin_amdgcn_exp2f(e1 - em)) + (__builtin_amdgcn_exp2f(e2 - em) + __builtin_amdgcn_exp2f(e3 - em));
        if (lane < NS) ml[16 + lane] = __builtin_amdgcn_exp2f(mw - em) * __builtin_amdgcn_rcpf(sum);
        lds_inc<true>(done + 1); }
    } else {
      float lt = PINIT, l0 = (float)(int)(now() - ts) - (float)(NS * P0 + 2 * NS) * lt, up = 0.f, tp = 0.f, tf = 0.f, plan = 0.f;
      unsigned Tp = 0u, hp = 0u, uq = 0u;
      bool fresh = false;
      if (lane == 0) __hip_atomic_store(pace, ((u64)asu(lt) << 32) | asu(l0), RLX_WG);
      for (int rd = 0; rd < 12; ++rd) {
        const unsigned Tk = PT0 + rd * PDT;
        gu64* cl = (gu64*)(ws + WS_CT) + rd * 544;
        while (now() - ts < Tk && lds_ld(ctr) < NI - PSTOP) __builtin_amdgcn_s_sleep(8);
        const unsigned um = lds_ld(ctr), hm = lds_ld(done + 3), hw = min((hm - hp) / max(um - uq, 1u), 63u);
        hp = hm; uq = um;
        if (um >= NI - PSTOP) break;
        if (lane == 0) put(cl + blockIdx.x, tag, (ts << 16) | (hw << 10) | um);
        while (now() - ts < Tk + PLAG) __builtin_amdgcn_s_sleep(8);
        float su = 0.f, st = 0.f, sh = 0.f; int n = 0;
#pragma unroll
        for (int k = 0; k < 4; ++k) { const u64 v = __hip_atomic_load(cl + lane + 64 * k, RLX_AGENT); const bool ok = (unsigned)(v >> 32) == tag;
          su += ok ? (float)((unsigned)v & 0x3ffu) : 0.f; sh += ok ? (float)(((unsigned)v >> 10) & 63u) : 0.f; st += ok ? (float)((int)((unsigned)v - (ts << 16)) >> 16) : 0.f; n += __builtin_popcountll(__ballot(ok)); }
        if (n < 224) continue;
        su = sum16(su); su = tr16(su, su); su = tr32(su, su); st = sum16(st); st = tr16(st, st); st = tr32(st, st); sh = sum16(sh); sh = tr16(sh, sh); sh = tr32(sh, sh);
        const float ub = su / (float)n, tm = (float)(Tk - Tp) / (ub - up);
        const bool first = Tp == 0u;
        const float uv = up;
        up = ub; Tp = Tk;
        if (first) continue;
        const float hb = sh / (float)n;
        const bool mixed = fresh || (uv < NU + PTR && ub > NU - PTR);
        fresh = false;
        if (uv < NU - PTR && ub >= NU - PTR && tp > 0.f) { tf = 0.f; tp = fminf(tp, PVF); }
        else if (tp == 0.f || (!mixed && tm > plan * (hb < PHL ? PTH : PT2))) {
          tp = fminf(tm * (ub < NU ? PEPS : PEPV), PCAP); tf = tp * PFL; fresh = true; }
        else if (mixed) {}
        else tp = fmaxf(tf, tp * fminf(fmaxf(1.f - PG * (hb - PHT) - PG2 * fmaxf(hb - PHT - 3.f, 0.f), ub < PNU ? 1.f - PMX : 1.f), 1.f + PMX));
        const float left = (float)NI - PDL - ub, tend = (float)Tk + st / (float)n + left * tp + PSL, tnow = (float)(int)(now() - ts);
        plan = tp + PSL / left;
        const float ti = (tend - tnow) / ((float)NI - fminf((float)(lds_ld(ctr) + PA), (tnow - l0) / lt));
        if (ti > 0.f && ti < 1000.f) { lt = ti; l0 = tend - (float)NI * ti; if (lane == 0) __hip_atomic_store(pace, ((u64)asu(lt) << 32) | asu(l0), RLX_WG); }
      }
    }
    int vw[5];
#pragma unroll
    for (int i = 0; i < 5; ++i) vw[i] = woff(i * 64 + lane);
    const int vb = (lane >> 4) & 1, which = lane >> 5;
    gu64* rx[3];
#pragma unroll
    for (int t = 0; t < 3; ++t) {
      const int oi = vb * 80 + 16 * (3 * a + ((t == 2 && a) ? 1 : t)) + (lane & 15), hp = oi / 5;
      rx[t] = (gu64*)(ws + WS_RX) + ((j * 32 + hp) * 32 + h) * 10 + which * 5 + (oi - hp * 5);
    }
    asm volatile("" : "+v"(vw[0]), "+v"(vw[1]), "+v"(vw[2]), "+v"(vw[3]), "+v"(vw[4]), "+v"(rx[0]), "+v"(rx[1]), "+v"(rx[2]));
    while (lds_ld(ctr) < NI && now() - ts < SPIN_TICKS) __builtin_amdgcn_s_sleep(2);
#pragma unroll
    for (int i = 0; i < 15; ++i) {
      __builtin_amdgcn_raw_ptr_buffer_load_lds(ro, (LAS void*)(L + L_WO + (15 * a + i) * 1024), 16, vw[i % 5], so + (3 * a + i / 5) * 32 * (D * 2), 0, AUX_NT);
      SCHED(); VMCNT(WPACE); }
    VMCNT(0);
    lds_inc<true>(done + 2);
    while (lds_ld(done + 2) < 2 && now() - ts < SPIN_TICKS) {}
    asm volatile("" ::: "memory");
    const LAS unsigned char* base = L + L_WO + (((lane & 15) >> 2) + 8 * (lane >> 4)) * 160 + a * 96 + (lane & 3) * 8;
    const LAS unsigned char* ah = L + L_HA + (lane & 1) * 96;
#define TRB(off) __builtin_amdgcn_ds_read_tr16_b64_v4i16((LAS v4s*)(base + (off)))
#define BFRAG(t, k) __builtin_bit_cast(v8bf, __builtin_shufflevector(TRB((t) * 32 + (k) * 32 * 160), TRB((t) * 32 + (k) * 32 * 160 + 4 * 160), 0, 1, 2, 3, 4, 5, 6, 7))
    const f32x4 Z = {0.f, 0.f, 0.f, 0.f};
    v8bf Bf[3][6]; float gsum[3];
#pragma unroll
    for (int t = 0; t < 3; ++t) {
      const int tt = (t == 2 && a) ? 1 : t;
#pragma unroll
      for (int k = 0; k < 6; ++k) Bf[t][k] = BFRAG(tt, k);
      f32x4 Ga = __builtin_amdgcn_mfma_f32_16x16x32_bf16(*(const LAS v8bf*)(ah + (lane >> 4) * 16), BFRAG(tt, 6), Z, 0, 0, 0);
      f32x4 Gb = __builtin_amdgcn_mfma_f32_16x16x16bf16_1k(*(const LAS v4s*)(ah + 64 + (lane >> 4) * 8), TRB(tt * 32 + 7 * 32 * 160 - 4 * (lane >> 4) * 160), Z, 0, 0, 0);
      asm volatile("" : "+v"(Ga), "+v"(Gb));
      gsum[t] = vb ? Ga[1] + Gb[1] : Ga[0] + Gb[0];
    }
#pragma unroll
    for (int t = 0; t < 3; ++t) asm volatile("" : "+v"(Bf[t][0]), "+v"(Bf[t][1]), "+v"(Bf[t][2]), "+v"(Bf[t][3]), "+v"(Bf[t][4]), "+v"(Bf[t][5]), "+v"(gsum[t]));
    barrier();
    const LAS unsigned char* ao = L + L_OA + (lane & 1) * (DH * 2) + (lane >> 4) * 16;
    v8bf A[6];
#pragma unroll
    for (int k = 0; k < 6; ++k) A[k] = *(const LAS v8bf*)(ao + k * 64);
    SCHED();
    f32x4 S[3] = {Z, Z, Z};
#pragma unroll
    for (int k = 0; k < 6; ++k)
#pragma unroll
      for (int t = 0; t < 3; ++t) S[t] = __builtin_amdgcn_mfma_f32_16x16x32_bf16(A[k], Bf[t][k], S[t], 0, 0, 0);
    asm volatile("" : "+v"(S[0]), "+v"(S[1]), "+v"(S[2]));
#pragma unroll
    for (int t = 0; t < 3; ++t) { float s0 = S[t][0], s1 = S[t][1]; asm volatile("" : "+v"(s0), "+v"(s1));
      float v = vb ? s1 : s0; v = which ? gsum[t] : v; asm volatile("" : "+v"(v));
      if (t < 2 || a == 0) put(rx[t], tag, asu(v)); }
  }
}

extern "C" void kernel_launch(void* const* d_in, const int* in_sizes, int n_in, void* d_out, int out_size, void* d_ws, size_t ws_size, hipStream_t stream) {
  fused_decode<<<256, NTHR, 0, stream>>>((const u16*)d_in[0], (const u16*)d_in[1], (const u16*)d_in[2], (const u16*)d_in[3], (const u16*)d_in[4],
                                         (const u16*)d_in[6], (const u16*)d_in[7], (const u16*)d_in[8], (const u16*)d_in[9], (u16*)d_out, (unsigned char*)d_ws);
}
```

```cpp
#include <hip/hip_runtime.h>
#include <hip/hip_bf16.h>
#include <stdint.h>

#define GAS __attribute__((address_space(1)))
#define LAS __attribute__((address_space(3)))
#define RLX_AGENT __ATOMIC_RELAXED, __HIP_MEMORY_SCOPE_AGENT
#define RLX_WG __ATOMIC_RELAXED, __HIP_MEMORY_SCOPE_WORKGROUP
typedef unsigned v4u __attribute__((ext_vector_type(4)));
typedef unsigned v2u __attribute__((ext_vector_type(2)));
typedef short v4s __attribute__((ext_vector_type(4)));
typedef __bf16 v8bf __attribute__((ext_vector_type(8)));
typedef float f32x4 __attribute__((ext_vector_type(4)));
typedef float f32x2 __attribute__((ext_vector_type(2)));
typedef __bf16 bf16x2 __attribute__((ext_vector_type(2)));
typedef unsigned short u16;
typedef unsigned long long u64;
typedef GAS u64 gu64;

constexpr int NB = 2, D = 640, H = 32, SK = 8192, DH = 192, F = 1536;
constexpr int NS = 6, NTHR = (NS + 2) * 64;
constexpr int NU = 256, NI = 2 * NU;
constexpr int P0 = 8;
constexpr int PW = 2;
constexpr int ROT = 5;
constexpr unsigned QD = 450;
constexpr unsigned QX = 45;
constexpr int PH = 250;
constexpr int PL = 300;
constexpr int TG1 = 192;
constexpr unsigned TGX = 300;
constexpr int PD2 = 16;
constexpr int WPACE = 7;
constexpr unsigned PT0 = 1300;
constexpr unsigned PDT = 500;
constexpr unsigned PLAG = 180;
constexpr unsigned PSTOP = 60;
constexpr unsigned PA = 1;
constexpr float PDL = 3.f;
constexpr float PINIT = 10.35f;
constexpr float PCAP = 11.95f;
constexpr float PVF = 10.9f;
constexpr float PTR = 15.f;
constexpr float PTH = 1.002f;
constexpr float PHL = 8.f;
constexpr float PT2 = 1.03f;
constexpr float PEPS = 1.015f;
constexpr float PEPV = 1.018f;
constexpr float PFL = 0.995f;
constexpr float PHT = 24.f;
constexpr float PG = 0.0015f;
constexpr float PG2 = 0.003f;
constexpr float PMX = 0.035f;
constexpr float PNU = 380.f;
constexpr float PSL = 30.f;
constexpr int AUX_NT = 2, AUX_SC1 = 16, RSRC3 = 0x00020000, OOB = 0x7ffffff0;
constexpr unsigned SPIN_TICKS = 2000000u;
constexpr int WS_EPOCH = 0;
constexpr int WS_QX = 4096;
constexpr int WS_PX = WS_QX + 256 * 512 * 8;
constexpr int WS_RX = WS_PX + 256 * 256 * 8;
constexpr int WS_CT = WS_RX + 8 * 32 * 320 * 8;
constexpr int WS_END = WS_CT + 12 * 544 * 8;
constexpr int L_XN = 0;
constexpr int L_Q = L_XN + 4 * 96 * 4;
constexpr int L_HA = L_Q + 384;
constexpr int L_OA = L_HA + 192;
constexpr int L_CT = L_OA + 768;
constexpr int L_ML = L_CT + 32;
constexpr int L_MG = L_ML + 96;
constexpr int L_SC = L_MG + NS * 768;
constexpr int L_WO = L_SC + NU * 32 + 32;
constexpr int L_END = 81 * 1024;
static_assert(L_WO + 38 * 1024 <= L_END, "LDS");
static_assert(L_WO % 16 == 0 && L_OA % 16 == 0 && L_HA % 16 == 0 && L_Q % 16 == 0 && L_CT % 16 == 0, "alignment");

__device__ __forceinline__ float asf(unsigned v) { return __builtin_bit_cast(float, v); }
__device__ __forceinline__ unsigned asu(float v) { return __builtin_bit_cast(unsigned, v); }
__device__ __forceinline__ float bf2f(u16 b) { return asf((unsigned)b << 16); }
__device__ __forceinline__ u16 f2bf(float f) { return __builtin_bit_cast(u16, (__bf16)f); }
__device__ __forceinline__ float rbf(float f) { return bf2f(f2bf(f)); }
__device__ __forceinline__ f32x2 unpk(unsigned v) { return f32x2{asf(v << 16), asf(v & 0xffff0000u)}; }
__device__ __forceinline__ float dot2(unsigned k, unsigned q, float acc) {
  return __builtin_amdgcn_fdot2_f32_bf16(__builtin_bit_cast(bf16x2, k), __builtin_bit_cast(bf16x2, q), acc, false);
}
template <int CTRL> __device__ __forceinline__ float dpp(float x) { return asf(__builtin_amdgcn_mov_dpp((int)asu(x), CTRL, 0xf, 0xf, true)); }
constexpr int XOR1 = 0xB1, XOR2 = 0x4E, XOR7 = 0x141, ROR8 = 0x128, ROR4 = 0x124, ROR2 = 0x122, ROR1 = 0x121, SHL5 = 0x105;
__device__ __forceinline__ float tr32(float x, float y) {
  auto s = __builtin_amdgcn_permlane32_swap(asu(x), asu(y), false, false); return asf(s[0]) + asf(s[1]);
}
__device__ __forceinline__ float tr16(float x, float y) {
  auto s = __builtin_amdgcn_permlane16_swap(asu(x), asu(y), false, false); return asf(s[0]) + asf(s[1]);
}
__device__ __forceinline__ float tr8(float x, float y, bool up) {
  const float keep = up ? y : x, send = up ? x : y; return keep + dpp<ROR8>(send);
}
__device__ __forceinline__ float sum16(float s) { s += dpp<ROR1>(s); s += dpp<ROR2>(s); s += dpp<ROR4>(s); return s + dpp<ROR8>(s); }
__device__ __forceinline__ float max16(float x) { auto s = __builtin_amdgcn_permlane16_swap(asu(x), asu(x), false, false); return fmaxf(asf(s[0]), asf(s[1])); }
__device__ __forceinline__ float max32(float x) { auto s = __builtin_amdgcn_permlane32_swap(asu(x), asu(x), false, false); return fmaxf(asf(s[0]), asf(s[1])); }
__device__ __forceinline__ unsigned now() { return (unsigned)__builtin_amdgcn_s_memrealtime(); }
__device__ __forceinline__ void barrier() { asm volatile("s_waitcnt lgkmcnt(0)\n\ts_barrier" ::: "memory"); }
__device__ __forceinline__ void put(gu64* g, unsigned tag, unsigned v) { __hip_atomic_store(g, ((u64)tag << 32) | v, RLX_AGENT); }
__device__ __forceinline__ unsigned lds_ld(LAS unsigned* p) { return __builtin_amdgcn_readfirstlane(__hip_atomic_load(p, RLX_WG)); }
template <bool ORDERED> __device__ __forceinline__ void lds_inc(LAS unsigned* p, unsigned v = 1u) {
  if (ORDERED) asm volatile("s_waitcnt lgkmcnt(0)" ::: "memory");
  asm volatile("s_mov_b64 exec, 1\n\tds_add_u32 %0, %1\n\ts_mov_b64 exec, -1" :: "v"((unsigned)(uintptr_t)p), "v"(v) : "memory");
}
__device__ __forceinline__ void touch(const void* p, const void* q) {
  unsigned t0, t1; asm volatile("s_load_dword %0, %2, 0x0\n\ts_load_dword %1, %3, 0x0\n\ts_waitcnt lgkmcnt(0)" : "=&s"(t0), "=&s"(t1) : "s"(p), "s"(q) : "memory");
}
#define SCHED() do { asm volatile("" ::: "memory"); __builtin_amdgcn_sched_barrier(0); } while (0)
#define VMCNT(n) asm volatile("s_waitcnt vmcnt(%0)" :: "n"(n) : "memory")

template <int SLEEP, int N> __device__ __forceinline__ void gather(unsigned (&val)[N], int (&off)[N], __amdgpu_buffer_rsrc_t rs, unsigned tag, unsigned ts) {
#pragma unroll
  for (int i = 0; i < N; ++i) val[i] = 0u;
  for (;;) {
    v2u in[N];
    asm volatile("" ::: "memory");
#pragma unroll
    for (int i = 0; i < N; ++i) in[i] = __builtin_amdgcn_raw_buffer_load_b64(rs, off[i], 0, AUX_SC1);
    int left = OOB;
#pragma unroll
    for (int i = 0; i < N; ++i) { const bool got = in[i][1] == tag; val[i] = got ? in[i][0] : val[i]; off[i] = got ? OOB : off[i]; left = min(left, off[i]); }
    if (__all(left == OOB) || now() - ts > SPIN_TICKS) break;
    if (SLEEP) __builtin_amdgcn_s_sleep(SLEEP);
  }
}

__global__ void __launch_bounds__(NTHR, 2)
fused_decode(const u16* x, const u16* kc, const u16* vc, const u16* wq, const u16* wo,
             const u16* w1, const u16* b1, const u16* w2, const u16* b2, u16* out, unsigned char* ws) {
  __shared__ __attribute__((aligned(16))) unsigned char lds_raw[L_END];
  LAS unsigned char* L = (LAS unsigned char*)lds_raw;
  const int bid = (blockIdx.x & 7) * 32 + (blockIdx.x >> 3), h = bid >> 3, j = bid & 7, b = j >> 2, sp = j & 3;
  const int tid = threadIdx.x, lane = tid & 63, w = __builtin_amdgcn_readfirstlane(tid >> 6), r = lane >> 3, c = lane & 7;
  LAS unsigned* ctr = (LAS unsigned*)(L + L_CT);
  LAS u64* pace = (LAS u64*)(L + L_CT + 8);
  LAS unsigned* done = (LAS unsigned*)(L + L_CT + 16);
  const unsigned ts = now();
  const int rot = (b * H + h) * ROT;
  const __amdgpu_buffer_rsrc_t wsr = __builtin_amdgcn_make_buffer_rsrc((void*)ws, 0, WS_END, RSRC3);

  if (__builtin_expect(w < NS, 1)) {
    if (tid == 0) { *ctr = NS * P0; *pace = 0ull; done[0] = 0u; done[1] = 0u; done[2] = 0u; done[3] = 0u; }
    const int rbase = ((b * H + h) * SK + sp * 8) * (DH * 2), voff = r * (DH * 2) + c * 16;
#define LD(dst, p, t) { const __amdgpu_buffer_rsrc_t rs_ = __builtin_amdgcn_make_buffer_rsrc((void*)(p), 0, NB * H * SK * DH * 2, RSRC3); \
    const int so_ = rbase + (((t) + rot) & (NU - 1)) * (32 * DH * 2); \
    _Pragma("unroll") for (int u = 0; u < 3; ++u) dst[u] = __builtin_amdgcn_raw_buffer_load_b128(rs_, voff + u * 128, so_, AUX_NT); SCHED(); }
    const int oi = 5 * h + (lane < 5 ? lane : 0), ocol = 80 * j + oi % 80, oidx = (oi / 80) * D + ocol;
    v4u R[P0][3];
#pragma unroll
    for (int i = 0; i < P0; ++i) {
      LD(R[i], kc, w + i * NS)
      if (i == 0) {
        if (w == 5) touch(ws + 2048, ws + WS_END);
        if (w == 4) touch(wq + (size_t)80 * j * H * DH + h * DH, wq + (size_t)(80 * j + 79) * H * DH + h * DH);
        if (w == 3) touch(w1 + 80 * j * F + h * 48, w1 + (80 * j + 79) * F + h * 48);
        if (w == 2) touch(x, b2);
      }
      if (i + 1 < P0) VMCNT(PW);
    }
    unsigned xres = __builtin_amdgcn_raw_buffer_load_b16(__builtin_amdgcn_make_buffer_rsrc((void*)x, 0, NB * D * 2, RSRC3), w == 5 ? oidx * 2 : OOB, 0, 0);
    unsigned b2v = __builtin_amdgcn_raw_buffer_load_b16(__builtin_amdgcn_make_buffer_rsrc((void*)b2, 0, D * 2, RSRC3), w == 5 ? ocol * 2 : OOB, 0, 0);
    barrier();
    const unsigned tag = lds_ld(ctr + 1);
    v4u qv[3];
#pragma unroll
    for (int u = 0; u < 3; ++u) qv[u] = *(LAS v4u*)(L + L_Q + u * 128 + c * 16);
    constexpr float SCALE = 0.07216878364870322f, LOG2E = 1.4426950408889634f;
    LAS float* sc = (LAS float*)(L + L_SC) + r;
    LAS float* ml = (LAS float*)(L + L_ML);
    float m = -3.0e38f;
#define QK(X, t) { float d0 = 0.f, d1 = 0.f; \
      _Pragma("unroll") for (int u = 0; u < 3; ++u) { d0 = dot2(X[u][0], qv[u][0], d0); d1 = dot2(X[u][1], qv[u][1], d1); d0 = dot2(X[u][2], qv[u][2], d0); d1 = dot2(X[u][3], qv[u][3], d1); } \
      float s = d0 + d1; s += dpp<XOR1>(s); s += dpp<XOR2>(s); s += dpp<XOR7>(s); s = rbf(rbf(s) * SCALE) * LOG2E; m = fmaxf(m, s); sc[(t) * 8] = s; asm volatile("" : "+v"(m)); SCHED(); }
    f32x2 acc[12];
#define PV(X, t) { const float p = sc[(t) * 8]; \
      _Pragma("unroll") for (int u = 0; u < 3; ++u) { SCHED(); \
        _Pragma("unroll") for (int k = 0; k < 4; ++k) acc[u * 4 + k] = __builtin_elementwise_fma(f32x2{p, p}, unpk(X[u][k]), acc[u * 4 + k]); \
        asm volatile("" : "+v"(acc[u * 4]), "+v"(acc[u * 4 + 1]), "+v"(acc[u * 4 + 2]), "+v"(acc[u * 4 + 3])); } SCHED(); }
    const int n = w < 2 ? 44 : 42;
#define UNIT(k) ((k) < 42 ? w + NS * (k) : 168 + w + 2 * (k))
    const float step = (float)NU / (float)n, lead = (float)w * (1.f / NS);
    u64 line = 0ull;
#define REQ(X, V, k) { \
      const float due = fmaf(fmaf((float)(k) + lead, step, (float)(V * NU)), asf(__builtin_amdgcn_readfirstlane((unsigned)(line >> 32))), \
                             asf(__builtin_amdgcn_readfirstlane((unsigned)line))); \
      float early = due - (float)(int)(now() - ts); \
      if (early > 0.f) { lds_inc<false>(done + 3, (unsigned)early); \
        _Pragma("unroll 1") for (int spn = 0; spn < 512 && early > 0.f; ++spn) { __builtin_amdgcn_s_sleep(1); early = due - (float)(int)(now() - ts); } } \
      SCHED(); LD(X, V ? vc : kc, UNIT(k)) lds_inc<false>(ctr); line = __hip_atomic_load(pace, RLX_WG); SCHED(); }
    static_assert(P0 % 2 == 0 && P0 + 2 <= 42 && NS == 6 && NU == 256, "even counts: two items in flight, in R[0] and R[1]");
    QK(R[0], w) REQ(R[0], 0, P0) QK(R[1], w + NS) REQ(R[1], 0, P0 + 1)
#pragma unroll
    for (int i = 2; i < P0; ++i) QK(R[i], w + i * NS)
    for (int k = P0; k + 2 < n; k += 2) { QK(R[0], UNIT(k)) REQ(R[0], 0, k + 2) QK(R[1], UNIT(k + 1)) REQ(R[1], 0, k + 3) }
    QK(R[0], UNIT(n - 2)) REQ(R[0], 1, 0) QK(R[1], UNIT(n - 1)) REQ(R[1], 1, 1)
    {
      float M = fmaxf(m, dpp<ROR8>(m)); M = max16(M); M = max32(M);
      float l = 0.f;
#pragma unroll
      for (int i = 0; i < 6; ++i) { const int k = 8 * i + r;
        LAS float* q = (LAS float*)(L + L_SC) + (k < n ? UNIT(k) : NU) * 8 + c;
        const float p = __builtin_amdgcn_exp2f(*q - M); *q = p; l += k < n ? p : 0.f; }
      l = sum16(l); l = tr16(l, l); l = tr32(l, l);
      if (lane == 0) { ml[w] = M; ml[8 + w] = l; }
      lds_inc<true>(done);
    }
#pragma unroll
    for (int k = 0; k < 12; ++k) acc[k] = f32x2{0.f, 0.f};
    for (int k = 0; k + 2 < n; k += 2) { PV(R[0], UNIT(k)) REQ(R[0], 1, k + 2) PV(R[1], UNIT(k + 1)) REQ(R[1], 1, k + 3) }
    PV(R[0], UNIT(n - 2)) PV(R[1], UNIT(n - 1))
    {
      float o[24];
#pragma unroll
      for (int k = 0; k < 12; ++k) { o[2 * k] = acc[k][0]; o[2 * k + 1] = acc[k][1]; }
#pragma unroll
      for (int k = 0; k < 12; ++k) o[k] = tr32(o[k], o[k + 12]);
#pragma unroll
      for (int k = 0; k < 6; ++k) o[k] = tr16(o[k], o[k + 6]);
      LAS float* mg = (LAS float*)(L + L_MG) + w * 192;
#pragma unroll
      for (int k = 0; k < 3; ++k) mg[k * 64 + lane] = tr8(o[k], o[k + 3], r & 1);
    }
    lds_inc<true>(done + 1);
    for (int spn = 0; lds_ld(done + 1) < 256 + NS && spn < (1 << 22); ++spn) {}
    asm volatile("" ::: "memory");
    if (w < 3) {
      const LAS float* mg = (const LAS float*)(L + L_MG);
      float o = 0.f;
#pragma unroll
      for (int i = 0; i < NS; ++i) o = fmaf(ml[16 + i], mg[i * 192 + tid], o);
      put((gu64*)(ws + WS_PX) + bid * 256 + tid, tag, asu(o));
    } else __builtin_amdgcn_s_sleep(4);
    static_assert(NS == 6, "six streaming waves");
    const int bb = w >= 3, di = (lane >> 5) * 12 + ((lane >> 4) & 1) * 6 + ((lane >> 3) & 1) * 3 + (w - 3 * bb);
    unsigned g4[4]; int o4[4];
#pragma unroll
    for (int i = 0; i < 4; ++i) o4[i] = WS_PX + (h * 8 + bb * 4 + i) * 2048 + (tid - bb * 192) * 8;
    {
      const u64 pv = __hip_atomic_load(pace, RLX_WG);
      const int tend = (int)fmaf((float)NI, asf(__builtin_amdgcn_readfirstlane((unsigned)(pv >> 32))), asf(__builtin_amdgcn_readfirstlane((unsigned)pv)));
      __builtin_amdgcn_s_sleep(6);
      const unsigned tb = now();
      while ((int)(now() - ts) < tend + TG1 && now() - tb < TGX) __builtin_amdgcn_s_sleep(1);
    }
    gather<0>(g4, o4, wsr, tag, ts);
    *(LAS u16*)(L + L_OA + (bb * DH + (di >> 3) * 64 + c * 8 + (di & 7)) * 2) = f2bf((asf(g4[0]) + asf(g4[1])) + (asf(g4[2]) + asf(g4[3])));
    barrier();
    if (w == 5) {
      unsigned gv[8]; int go[8];
#pragma unroll
      for (int i = 0; i < 8; ++i) go[i] = (lane & 15) < 10 ? WS_RX + (j * 32 + h) * 2560 + i * 320 + (lane >> 4) * 80 + (lane & 15) * 8 : OOB;
      __builtin_amdgcn_s_sleep(PD2);
      gather<0>(gv, go, wsr, tag, ts);
      float t = 0.f;
#pragma unroll
      for (int i = 0; i < 8; ++i) t += asf(gv[i]);
      t = tr16(t, t); t = tr32(t, t);
      asm volatile("" : "+v"(xres), "+v"(b2v));
      const float g = rbf(rbf(dpp<SHL5>(t)) + asf(b2v << 16));
      const float gate = rbf(g * __builtin_amdgcn_rcpf(1.f + __expf(-g)));
      __builtin_amdgcn_raw_buffer_store_b16(f2bf(asf(xres << 16) + rbf(rbf(t) * gate)), __builtin_amdgcn_make_buffer_rsrc((void*)out, 0, NB * D * 2, RSRC3),
                                            lane < 5 ? oidx * 2 : OOB, 0, AUX_SC1);
      if (blockIdx.x == 0 && lane == 0) __hip_atomic_store((GAS unsigned*)(ws + WS_EPOCH), tag, RLX_AGENT);
    }
  } else {
    __builtin_amdgcn_s_setprio(3);
    const int a = w - NS;
    const unsigned epoch = __hip_atomic_load((GAS unsigned*)(ws + WS_EPOCH), RLX_AGENT);
    const int xb = lane >> 5, xl = lane & 31, xo = xb * (D * 2) + xl * 40;
    const __amdgpu_buffer_rsrc_t xr = __builtin_amdgcn_make_buffer_rsrc((void*)x, 0, NB * D * 2, RSRC3);
    const v4u x0 = __builtin_amdgcn_raw_buffer_load_b128(xr, xo, 0, 0), x1 = __builtin_amdgcn_raw_buffer_load_b128(xr, xo + 16, 0, 0);
    const v2u x2 = __builtin_amdgcn_raw_buffer_load_b64(xr, xo + 32, 0, 0);
    u16 xs[3];
#pragma unroll
    for (int i = 0; i < 3; ++i) xs[i] = __builtin_amdgcn_raw_buffer_load_b16(xr, xl + 32 * i < 80 ? xb * (D * 2) + (80 * j + xl + 32 * i) * 2 : OOB, 0, 0);
    v4u wa[10], wb[10];
    { const __amdgpu_buffer_rsrc_t ra = __builtin_amdgcn_make_buffer_rsrc((void*)wq, 0, D * H * DH * 2, RSRC3);
      const __amdgpu_buffer_rsrc_t rb = __builtin_amdgcn_make_buffer_rsrc((void*)(a ? w1 : wq), 0, a ? D * F * 2 : D * H * DH * 2, RSRC3);
      const int sa = H * DH * 2, sb = a ? F * 2 : H * DH * 2;
      const int va = r * sa + c * 16, vb = (a && c >= 6) ? OOB : r * sb + c * 16;
      const int oa = 80 * j * sa + h * (DH * 2) + a * 256, ob = 80 * j * sb + (a ? h * 96 : h * (DH * 2) + 128);
#pragma unroll
      for (int u = 0; u < 10; ++u) { wa[u] = __builtin_amdgcn_raw_buffer_load_b128(ra, va, oa + u * 8 * sa, AUX_NT); wb[u] = __builtin_amdgcn_raw_buffer_load_b128(rb, vb, ob + u * 8 * sb, AUX_NT); }
    }
    SCHED();
    unsigned tag = (unsigned)__builtin_amdgcn_readfirstlane(epoch) * 0x9E3779B1u + 0x7F4A7C15u;
    tag = tag ? tag : 1u;
    if (a == 0 && lane == 0) ctr[1] = tag;
    {
      f32x2 v[10];
#pragma unroll
      for (int k = 0; k < 4; ++k) { v[k] = unpk(x0[k]); v[4 + k] = unpk(x1[k]); }
      v[8] = unpk(x2[0]); v[9] = unpk(x2[1]);
      float s = 0.f, q = 0.f;
#pragma unroll
      for (int k = 0; k < 10; ++k) s += v[k][0] + v[k][1];
      s = sum16(s); s = tr16(s, s);
      const float mean = s * (1.f / D);
#pragma unroll
      for (int k = 0; k < 10; ++k) { const f32x2 d = v[k] - mean; q = fmaf(d[0], d[0], fmaf(d[1], d[1], q)); }
      q = sum16(q); q = tr16(q, q);
      const float mb = rbf(mean), sd = rbf(sqrtf(rbf(rbf(q * (1.f / D)) + 1e-5f)));
      LAS float* xn = (LAS float*)(L + L_XN) + (a * 2 + xb) * 96 + xl;
#pragma unroll
      for (int i = 0; i < 3; ++i) xn[32 * i] = rbf(rbf(bf2f(xs[i]) - mb) / sd);
    }
    asm volatile("s_waitcnt lgkmcnt(0)" ::: "memory");
    { f32x2 p2[16];
#pragma unroll
      for (int k = 0; k < 16; ++k) p2[k] = f32x2{0.f, 0.f};
      const LAS float* xn = (const LAS float*)(L + L_XN) + a * 192 + r;
#pragma unroll
      for (int u = 0; u < 10; ++u) { const float xa = xn[8 * u], xc = xn[96 + 8 * u];
#pragma unroll
        for (int k = 0; k < 4; ++k) {
          const f32x2 fa = unpk(wa[u][k]), fb = unpk(wb[u][k]);
          p2[k] = __builtin_elementwise_fma(f32x2{xa, xa}, fa, p2[k]); p2[4 + k] = __builtin_elementwise_fma(f32x2{xa, xa}, fb, p2[4 + k]);
          p2[8 + k] = __builtin_elementwise_fma(f32x2{xc, xc}, fa, p2[8 + k]); p2[12 + k] = __builtin_elementwise_fma(f32x2{xc, xc}, fb, p2[12 + k]);
        } }
      float pa[32];
#pragma unroll
      for (int k = 0; k < 16; ++k) { pa[2 * k] = p2[k][0]; pa[2 * k + 1] = p2[k][1]; }
#pragma unroll
      for (int k = 0; k < 16; ++k) pa[k] = tr32(pa[k], pa[k + 16]);
#pragma unroll
      for (int k = 0; k < 8; ++k) pa[k] = tr16(pa[k], pa[k + 8]);
      gu64* qx = (gu64*)(ws + WS_QX) + bid * 512 + (lane >> 5) * 256 + (2 * a + ((lane >> 4) & 1)) * 64 + (lane & 15);
#pragma unroll
      for (int t = 0; t < 4; ++t) put(qx + t * 16, tag, asu(tr8(pa[t], pa[t + 4], r & 1)));
    }
    const int col = (lane & 7) * 8 + ((lane >> 3) & 1) * 4 + (lane >> 4);
    if (a == 0) {
      unsigned gv[24]; int go[24];
#pragma unroll
      for (int i = 0; i < 24; ++i) go[i] = WS_QX + ((h * 8 + i / 3) * 512 + b * 256 + (i % 3) * 64 + lane) * 8;
      { const unsigned tq = max(QD, now() - ts + QX);
        while (now() - ts < tq) __builtin_amdgcn_s_sleep(2); }
      gather<0>(gv, go, wsr, tag, ts);
#pragma unroll
      for (int u = 0; u < 3; ++u) { float q = 0.f;
#pragma unroll
        for (int p = 0; p < 8; ++p) q += asf(gv[p * 3 + u]);
        *(LAS u16*)(L + L_Q + (u * 64 + col) * 2) = f2bf(q); }
    }
    barrier();
    __builtin_amdgcn_s_setprio(0);
    const __amdgpu_buffer_rsrc_t ro = __builtin_amdgcn_make_buffer_rsrc((void*)wo, 0, H * DH * D * 2, RSRC3), r2 = __builtin_amdgcn_make_buffer_rsrc((void*)w2, 0, F * D * 2, RSRC3);
    const int so = DH * h * (D * 2) + 160 * j, s2 = 48 * h * (D * 2) + 160 * j;
    auto woff = [](int id) { const int row = id / 10; return row * (D * 2) + (id - row * 10) * 16; };
    if (a == 0) {
      { const unsigned char* vp = (const unsigned char*)vc + (size_t)(b * H + h) * (SK * DH * 2);
        touch(vp + (rot & (NU - 1)) * (32 * DH * 2), b1); }
      touch(wo + DH * h * D, wo + (DH * h + DH - 2) * D); touch(w2 + 48 * h * D, w2 + (48 * h + 46) * D); touch(out, out);
      while ((int)lds_ld(ctr) < PH && now() - ts < SPIN_TICKS) __builtin_amdgcn_s_sleep(8);
#pragma unroll 1
      for (int n = 0; n < 8; ++n) {
        __builtin_amdgcn_raw_ptr_buffer_load_lds(r2, (LAS void*)(L + L_WO + (30 + n) * 1024), 16, n * 64 + lane < 480 ? woff(n * 64 + lane) : OOB, s2, 0, AUX_NT);
        __builtin_amdgcn_s_sleep(3);
      }
      const bool live = (lane & 7) < 6;
      const float bias = bf2f(b1[48 * h + (live ? col : 0)]);
      unsigned gv[16]; int go[16];
#pragma unroll
      for (int i = 0; i < 16; ++i) go[i] = live ? WS_QX + ((h * 8) * 512 + i * 256 + 192 + lane) * 8 : OOB;
      gather<16>(gv, go, wsr, tag, ts);
      float hs[2] = {0.f, 0.f};
#pragma unroll
      for (int p = 0; p < 16; ++p) hs[p & 1] += asf(gv[p]);
      if (live) {
#pragma unroll
        for (int u = 0; u < 2; ++u) { float t = rbf(rbf(hs[u]) + bias); t = t > 0.f ? t : 0.f; *(LAS u16*)(L + L_HA + (u * 48 + col) * 2) = f2bf(t * t); } }
      while (lds_ld(done) < NS && now() - ts < SPIN_TICKS) __builtin_amdgcn_s_sleep(8);
      asm volatile("" ::: "memory");
      LAS float* ml = (LAS float*)(L + L_ML);
      const float mw = ml[lane < NS ? lane : 0];
      { float M = ml[0], ls = 0.f;
#pragma unroll
        for (int i = 1; i < NS; ++i) M = fmaxf(M, ml[i]);
#pragma unroll
        for (int i = 0; i < NS; ++i) ls = fmaf(__builtin_amdgcn_exp2f(ml[i] - M), ml[8 + i], ls);
        if (lane == 0) put((gu64*)(ws + WS_PX) + bid * 256 + 192, tag, asu(M + __builtin_amdgcn_logf(ls))); }
      while ((int)lds_ld(ctr) < PL && now() - ts < SPIN_TICKS) __builtin_amdgcn_s_sleep(16);
      { unsigned ev[4]; int eo[4];
#pragma unroll
        for (int i = 0; i < 4; ++i) eo[i] = WS_PX + (h * 8 + b * 4 + i) * 2048 + 192 * 8;
        gather<16>(ev, eo, wsr, tag, ts);
        const float e0 = asf(ev[0]), e1 = asf(ev[1]), e2 = asf(ev[2]), e3 = asf(ev[3]), em = fmaxf(fmaxf(e0, e1), fmaxf(e2, e3));
        const float sum = (__builtin_amdgcn_exp2f(e0 - em) + __builtin_amdgcn_exp2f(e1 - em)) + (__builtin_amdgcn_exp2f(e2 - em) + __builtin_amdgcn_exp2f(e3 - em));
        if (lane < NS) ml[16 + lane] = __builtin_amdgcn_exp2f(mw - em) * __builtin_amdgcn_rcpf(sum);
        VMCNT(0);
        lds_inc<true>(done + 1, 256u); }
    } else {
      float lt = PINIT, l0 = (float)(int)(now() - ts) - (float)(NS * P0 + 2 * NS) * lt, up = 0.f, tp = 0.f, tf = 0.f, plan = 0.f;
      unsigned Tp = 0u, hp = 0u, uq = 0u;
      bool fresh = false;
      if (lane == 0) __hip_atomic_store(pace, ((u64)asu(lt) << 32) | asu(l0), RLX_WG);
      for (int rd = 0; rd < 12; ++rd) {
        const unsigned Tk = PT0 + rd * PDT;
        gu64* cl = (gu64*)(ws + WS_CT) + rd * 544;
        while (now() - ts < Tk && lds_ld(ctr) < NI - PSTOP) __builtin_amdgcn_s_sleep(8);
        const unsigned um = lds_ld(ctr), hm = lds_ld(done + 3), hw = min((hm - hp) / max(um - uq, 1u), 63u);
        hp = hm; uq = um;
        if (um >= NI - PSTOP) break;
        if (lane == 0) put(cl + blockIdx.x, tag, (ts << 16) | (hw << 10) | um);
        while (now() - ts < Tk + PLAG) __builtin_amdgcn_s_sleep(8);
        float su = 0.f, st = 0.f, sh = 0.f; int n = 0;
#pragma unroll
        for (int k = 0; k < 4; ++k) { const u64 v = __hip_atomic_load(cl + lane + 64 * k, RLX_AGENT); const bool ok = (unsigned)(v >> 32) == tag;
          su += ok ? (float)((unsigned)v & 0x3ffu) : 0.f; sh += ok ? (float)(((unsigned)v >> 10) & 63u) : 0.f; st += ok ? (float)((int)((unsigned)v - (ts << 16)) >> 16) : 0.f; n += __builtin_popcountll(__ballot(ok)); }
        if (n < 224) continue;
        su = sum16(su); su = tr16(su, su); su = tr32(su, su); st = sum16(st); st = tr16(st, st); st = tr32(st, st); sh = sum16(sh); sh = tr16(sh, sh); sh = tr32(sh, sh);
        const float ub = su / (float)n, tm = (float)(Tk - Tp) / (ub - up);
        const bool first = Tp == 0u;
        const float uv = up;
        up = ub; Tp = Tk;
        if (first) continue;
        const float hb = sh / (float)n;
        const bool mixed = fresh || (uv < NU + PTR && ub > NU - PTR);
        fresh = false;
        if (uv < NU - PTR && ub >= NU - PTR && tp > 0.f) { tf = 0.f; tp = fminf(tp, PVF); }
        else if (tp == 0.f || (!mixed && tm > plan * (hb < PHL ? PTH : PT2))) {
          tp = fminf(tm * (ub < NU ? PEPS : PEPV), PCAP); tf = tp * PFL; fresh = true; }
        else if (!mixed)
          tp = fmaxf(tf, tp * fminf(fmaxf(1.f - PG * (hb - PHT) - PG2 * fmaxf(hb - PHT - 3.f, 0.f), ub < PNU ? 1.f - PMX : 1.f), 1.f + PMX));
        const float left = (float)NI - PDL - ub, tend = (float)Tk + st / (float)n + left * tp + PSL, tnow = (float)(int)(now() - ts);
        plan = tp + PSL / left;
        const float ti = (tend - tnow) / ((float)NI - fminf((float)(lds_ld(ctr) + PA), (tnow - l0) / lt));
        if (ti > 0.f && ti < 1000.f) { lt = ti; l0 = tend - (float)NI * ti; if (lane == 0) __hip_atomic_store(pace, ((u64)asu(lt) << 32) | asu(l0), RLX_WG); }
      }
    }
    int vw[5];
#pragma unroll
    for (int i = 0; i < 5; ++i) vw[i] = woff(i * 64 + lane);
    const int vb = (lane >> 4) & 1, which = lane >> 5;
    gu64* rx[3];
#pragma unroll
    for (int t = 0; t < 3; ++t) {
      const int oi = vb * 80 + 16 * (3 * a + ((t == 2 && a) ? 1 : t)) + (lane & 15), hp = oi / 5;
      rx[t] = (gu64*)(ws + WS_RX) + ((j * 32 + hp) * 32 + h) * 10 + which * 5 + (oi - hp * 5);
    }
    const LAS unsigned char* base = L + L_WO + (((lane & 15) >> 2) + 8 * (lane >> 4)) * 160 + a * 96 + (lane & 3) * 8;
    const LAS unsigned char* ah = L + L_HA + (lane & 1) * 96;
#define TRB(off) __builtin_amdgcn_ds_read_tr16_b64_v4i16((LAS v4s*)(base + (off)))
#define BFRAG(t, k) __builtin_bit_cast(v8bf, __builtin_shufflevector(TRB((t) * 32 + (k) * 32 * 160), TRB((t) * 32 + (k) * 32 * 160 + 4 * 160), 0, 1, 2, 3, 4, 5, 6, 7))
    const f32x4 Z = {0.f, 0.f, 0.f, 0.f};
    float gsum[3];
    while (lds_ld(done + 1) < 256 && now() - ts < SPIN_TICKS) __builtin_amdgcn_s_sleep(8);
    asm volatile("" ::: "memory");
#pragma unroll
    for (int t = 0; t < 3; ++t) {
      const int tt = (t == 2 && a) ? 1 : t;
      f32x4 Ga = __builtin_amdgcn_mfma_f32_16x16x32_bf16(*(const LAS v8bf*)(ah + (lane >> 4) * 16), BFRAG(tt, 6), Z, 0, 0, 0);
      f32x4 Gb = __builtin_amdgcn_mfma_f32_16x16x16bf16_1k(*(const LAS v4s*)(ah + 64 + (lane >> 4) * 8), TRB(tt * 32 + 7 * 32 * 160 - 4 * (lane >> 4) * 160), Z, 0, 0, 0);
      asm volatile("" : "+v"(Ga), "+v"(Gb));
      gsum[t] = vb ? Ga[1] + Gb[1] : Ga[0] + Gb[0];
    }
    asm volatile("" : "+v"(vw[0]), "+v"(vw[1]), "+v"(vw[2]), "+v"(vw[3]), "+v"(vw[4]), "+v"(rx[0]), "+v"(rx[1]), "+v"(rx[2]), "+v"(gsum[0]), "+v"(gsum[1]), "+v"(gsum[2]));
    while (lds_ld(ctr) < NI && now() - ts < SPIN_TICKS) __builtin_amdgcn_s_sleep(2);
#pragma unroll
    for (int i = 0; i < 15; ++i) {
      __builtin_amdgcn_raw_ptr_buffer_load_lds(ro, (LAS void*)(L + L_WO + (15 * a + i) * 1024), 16, vw[i % 5], so + (3 * a + i / 5) * 32 * (D * 2), 0, AUX_NT);
      SCHED(); VMCNT(WPACE); }
    VMCNT(0);
    lds_inc<true>(done + 2);
    while (lds_ld(done + 2) < 2 && now() - ts < SPIN_TICKS) {}
    asm volatile("" ::: "memory");
    v8bf Bf[3][6];
#pragma unroll
    for (int t = 0; t < 3; ++t)
#pragma unroll
      for (int k = 0; k < 6; ++k) Bf[t][k] = BFRAG((t == 2 && a) ? 1 : t, k);
#pragma unroll
    for (int t = 0; t < 3; ++t) asm volatile("" : "+v"(Bf[t][0]), "+v"(Bf[t][1]), "+v"(Bf[t][2]), "+v"(Bf[t][3]), "+v"(Bf[t][4]), "+v"(Bf[t][5]));
    barrier();
    const LAS unsigned char* ao = L + L_OA + (lane & 1) * (DH * 2) + (lane >> 4) * 16;
    v8bf A[6];
#pragma unroll
    for (int k = 0; k < 6; ++k) A[k] = *(const LAS v8bf*)(ao + k * 64);
    SCHED();
    f32x4 S[3] = {Z, Z, Z};
#pragma unroll
    for (int k = 0; k < 6; ++k)
#pragma unroll
      for (int t = 0; t < 3; ++t) S[t] = __builtin_amdgcn_mfma_f32_16x16x32_bf16(A[k], Bf[t][k], S[t], 0, 0, 0);
    asm volatile("" : "+v"(S[0]), "+v"(S[1]), "+v"(S[2]));
#pragma unroll
    for (int t = 0; t < 3; ++t) { float s0 = S[t][0], s1 = S[t][1]; asm volatile("" : "+v"(s0), "+v"(s1));
      float v = vb ? s1 : s0; v = which ? gsum[t] : v; asm volatile("" : "+v"(v));
      if (t < 2 || a == 0) put(rx[t], tag, asu(v)); }
  }
}

extern "C" void kernel_launch(void* const* d_in, const int* in_sizes, int n_in, void* d_out, int out_size, void* d_ws, size_t ws_size, hipStream_t stream) {
  fused_decode<<<256, NTHR, 0, stream>>>((const u16*)d_in[0], (const u16*)d_in[1], (const u16*)d_in[2], (const u16*)d_in[3], (const u16*)d_in[4],
                                         (const u16*)d_in[6], (const u16*)d_in[7], (const u16*)d_in[8], (const u16*)d_in[9], (u16*)d_out, (unsigned char*)d_ws);
}
```
